```python
import torch

def _softmax(x, axis=-1):
    return torch.softmax(x, dim=axis)
def _gelu(x):
    return torch.nn.functional.gelu(x, approximate="tanh")
def _silu(x):
    return torch.nn.functional.silu(x)
def _sigmoid(x):
    return torch.sigmoid(x)
def _layernorm(x, axis=-1, eps=1e-5):
    m = x.mean(dim=axis, keepdim=True)
    v = x.var(dim=axis, keepdim=True, unbiased=False)
    return (x - m) / torch.sqrt(v + eps)
def _scatter_add(tgt, idx, src, axis=0):
    return tgt.clone().index_add_(axis, idx, src)
def _segment_sum(src, seg, n):
    out = torch.zeros((n,) + src.shape[1:], dtype=src.dtype)
    return out.index_add_(0, seg, src)
def _pad(x, pw):
    flat = tuple(v for pair in reversed(pw) for v in pair)
    return torch.nn.functional.pad(x, flat)

kernel_name = 'parallel_sum__ssm_conv_s5__mixer_token_mlp__prompt'

def setup_inputs():

    _w = (lambda a: (lambda t: t.to(torch.bfloat16) if t.is_floating_point() else t)(torch.as_tensor(a)))
    def _A_setup():
        B, S, D = 1, 2048, 1024
        P = D
        Lambda_re = -0.5 * (lambda _x: torch.exp(torch.as_tensor(_x)))((lambda *s: torch.randn(*s, dtype=torch.bfloat16) * 0.1)(P))
        Lambda_im = (lambda *s: torch.randn(*s, dtype=torch.bfloat16) * 0.4)(P) * 1.0
        B_re = (lambda *s: torch.randn(*s, dtype=torch.bfloat16) * 0.2)(P, D) * (D ** -0.5)
        B_im = (lambda *s: torch.randn(*s, dtype=torch.bfloat16) * 0.2)(P, D) * (D ** -0.5)
        C_re = (lambda *s: torch.randn(*s, dtype=torch.bfloat16) * 0.2)(D, P) * (P ** -0.5)
        C_im = (lambda *s: torch.randn(*s, dtype=torch.bfloat16) * 0.2)(D, P) * (P ** -0.5)
        log_dt = (lambda *s: torch.randn(*s, dtype=torch.bfloat16) * 0.1)(P) * 0.3 - 2.0
        Dskip = (lambda *s: torch.randn(*s, dtype=torch.bfloat16) * 0.05)(D)
        return {
            "x": _w((lambda *s: torch.randn(*s, dtype=torch.bfloat16) * 0.05)(B, S, D)),
            "Lambda_re": _w(Lambda_re),
            "Lambda_im": _w(Lambda_im),
            "B_re": _w(B_re),
            "B_im": _w(B_im),
            "C_re": _w(C_re),
            "C_im": _w(C_im),
            "log_dt": _w(log_dt),
            "Dskip": _w(Dskip),
        }
    _A_in = _A_setup()

    def _B_setup():
        B, S, D, F = 1, 2048, 1024, 3584
        return {
            "x": _w((lambda *s: torch.randn(*s, dtype=torch.bfloat16) * 0.05)(B, S, D)),
            "w1": _w((lambda *s: torch.randn(*s, dtype=torch.bfloat16) * 0.8)(S, F) * (S ** -0.5)),
            "b1": _w((lambda *s: torch.randn(*s, dtype=torch.bfloat16) * 3.2)(F) * 0.02),
            "w2": _w((lambda *s: torch.randn(*s, dtype=torch.bfloat16) * 0.4)(F, S) * (F ** -0.5)),
            "b2": _w((lambda *s: torch.randn(*s, dtype=torch.bfloat16) * 1.6)(S) * 0.02),
        }
    _B_in = _B_setup()

    return {
        "A_x": _A_in["x"],
        "A_Lambda_re": _A_in["Lambda_re"],
        "A_Lambda_im": _A_in["Lambda_im"],
        "A_B_re": _A_in["B_re"],
        "A_B_im": _A_in["B_im"],
        "A_C_re": _A_in["C_re"],
        "A_C_im": _A_in["C_im"],
        "A_log_dt": _A_in["log_dt"],
        "A_Dskip": _A_in["Dskip"],
        "B_x": _B_in["x"],
        "B_w1": _B_in["w1"],
        "B_b1": _B_in["b1"],
        "B_w2": _B_in["w2"],
        "B_b2": _B_in["b2"],
    }

def reference(A_x, A_Lambda_re, A_Lambda_im, A_B_re, A_B_im, A_C_re, A_C_im, A_log_dt, A_Dskip, B_x, B_w1, B_b1, B_w2, B_b2):
    def _A_ref(x, Lambda_re, Lambda_im, B_re, B_im, C_re, C_im, log_dt, Dskip):
        Bsz, L, H = x.shape
        Lam = Lambda_re + 1j * Lambda_im
        Bmat = B_re + 1j * B_im
        Cmat = C_re + 1j * C_im
        dt = torch.exp(log_dt)
        Lam_bar = torch.exp(dt * Lam)
        B_bar = ((Lam_bar - 1.0) / Lam)[:, None] * Bmat
        Bu = torch.einsum("bld,pd->blp", x + 0j, B_bar)
        h = torch.zeros_like(Bu[:, 0, :])
        hs = []
        for t in range(L):
            h = Lam_bar[None, :] * h + Bu[:, t, :]
            hs.append(h)
        hseq = torch.stack(hs, 1)
        y = 2.0 * torch.einsum("blp,dp->bld", hseq, Cmat).real + x * Dskip[None, None, :]
        return y

    def _B_ref(x, w1, b1, w2, b2):
        B, S, D = x.shape
        h = _layernorm(x, axis=-1)
        u = torch.einsum("bsd,sf->bdf", h, w1) + b1
        u = _gelu(u)
        y = torch.einsum("bdf,fs->bds", u, w2) + b2
        return (lambda x, *d: x.permute(*d))(y, 0, 2, 1)

    x = A_x
    xn = _layernorm(x, axis=-1)
    return x + _A_ref(xn, A_Lambda_re, A_Lambda_im, A_B_re, A_B_im, A_C_re, A_C_im, A_log_dt, A_Dskip) + _B_ref(xn, B_w1, B_b1, B_w2, B_b2)
```

```cpp
#include <hip/hip_runtime.h>
#include <hip/hip_bf16.h>
#include <stdint.h>

typedef unsigned short u16;
constexpr int S = 2048, D = 1024, P = 1024, F = 3584;

__device__ __forceinline__ float bf2f(u16 v) { return __uint_as_float(((unsigned)v) << 16); }
__device__ __forceinline__ u16 f2bf(float f) { unsigned u = __float_as_uint(f); u += 0x7FFFu + ((u >> 16) & 1u); return (u16)(u >> 16); }
__device__ __forceinline__ float rbf(float f) { return bf2f(f2bf(f)); }

__device__ float block_sum(float v, float* red) {
  for (int o = 32; o >= 1; o >>= 1) v += __shfl_xor(v, o);
  __syncthreads();
  if ((threadIdx.x & 63) == 0) red[threadIdx.x >> 6] = v;
  __syncthreads();
  float s = 0; for (int i = 0; i < (int)(blockDim.x >> 6); ++i) s += red[i];
  return s;
}

__global__ void k_ln(const u16* x, u16* xn, u16* h) {
  __shared__ float red[4];
  int r = blockIdx.x; float v[4];
  for (int i = 0; i < 4; ++i) v[i] = bf2f(x[r * D + threadIdx.x * 4 + i]);
  for (int pass = 0; pass < 2; ++pass) {
    float s = v[0] + v[1] + v[2] + v[3];
    float mean = block_sum(s, red) / D;
    float q = 0; for (int i = 0; i < 4; ++i) q += (v[i] - mean) * (v[i] - mean);
    float var = block_sum(q, red) / D;
    float mb = rbf(mean), vb = rbf(var);
    float sd = rbf(sqrtf(rbf(vb + 1e-5f)));
    for (int i = 0; i < 4; ++i) v[i] = rbf(rbf(v[i] - mb) / sd);
    u16* o = pass == 0 ? xn : h;
    for (int i = 0; i < 4; ++i) o[r * D + threadIdx.x * 4 + i] = f2bf(v[i]);
  }
}

__global__ void k_bu(const u16* xn, const u16* Bre, const u16* Bim, float* bu) {
  int p = blockIdx.x * 64 + (threadIdx.x & 63), s = blockIdx.y * 4 + (threadIdx.x >> 6);
  float ar = 0, ai = 0;
  for (int d = 0; d < D; ++d) { float xv = bf2f(xn[s * D + d]); ar += xv * bf2f(Bre[p * D + d]); ai += xv * bf2f(Bim[p * D + d]); }
  bu[(s * P + p) * 2] = ar; bu[(s * P + p) * 2 + 1] = ai;
}
__global__ void k_scan(float* bu, const u16* Lre, const u16* Lim, const u16* logdt) {
  int p = blockIdx.x * 64 + threadIdx.x;
  float lr = bf2f(Lre[p]), li = bf2f(Lim[p]); float dt = rbf(expf(bf2f(logdt[p])));
  float er = expf(dt * lr); float lbr = er * cosf(dt * li), lbi = er * sinf(dt * li);
  float nr = lbr - 1.f, ni = lbi; float den = lr * lr + li * li;
  float cr = (nr * lr + ni * li) / den, ci = (ni * lr - nr * li) / den;
  float hr = 0, hi = 0;
  for (int s = 0; s < S; ++s) {
    float ar = bu[(s * P + p) * 2], ai = bu[(s * P + p) * 2 + 1];
    float ur = cr * ar - ci * ai, ui = cr * ai + ci * ar;
    float tr = lbr * hr - lbi * hi + ur, ti = lbr * hi + lbi * hr + ui;
    hr = tr; hi = ti;
    bu[(s * P + p) * 2] = hr; bu[(s * P + p) * 2 + 1] = hi;
  }
}
__global__ void k_u(const u16* h, const u16* w1, const u16* b1, float* u) {
  int f = blockIdx.x * 64 + (threadIdx.x & 63), d = blockIdx.y * 4 + (threadIdx.x >> 6);
  float a = 0;
  for (int s = 0; s < S; ++s) a += bf2f(h[s * D + d]) * bf2f(w1[s * F + f]);
  a += bf2f(b1[f]);
  float g = 0.5f * a * (1.f + tanhf(0.7978845608028654f * (a + 0.044715f * a * a * a)));
  u[d * F + f] = rbf(g);
}
__global__ void k_out(const u16* x, const u16* xn, const float* hs, const u16* Cre, const u16* Cim, const u16* Dsk,
                      const float* u, const u16* w2, const u16* b2, float* out) {
  int s = blockIdx.x * 64 + (threadIdx.x & 63), d = blockIdx.y * 4 + (threadIdx.x >> 6);
  float a = 0;
  for (int p = 0; p < P; ++p) a += hs[(s * P + p) * 2] * bf2f(Cre[d * P + p]) - hs[(s * P + p) * 2 + 1] * bf2f(Cim[d * P + p]);
  float b = 0;
  for (int f = 0; f < F; ++f) b += u[d * F + f] * bf2f(w2[f * S + s]);
  b += bf2f(b2[s]);
  out[s * D + d] = bf2f(x[s * D + d]) + (2.f * a + rbf(bf2f(xn[s * D + d]) * bf2f(Dsk[d]))) + b;
}

extern "C" void kernel_launch(void* const* d_in, const int* in_sizes, int n_in, void* d_out, int out_size, void* d_ws, size_t ws_size, hipStream_t stream) {
  const u16* x = (const u16*)d_in[0]; const u16* Lre = (const u16*)d_in[1]; const u16* Lim = (const u16*)d_in[2];
  const u16* Bre = (const u16*)d_in[3]; const u16* Bim = (const u16*)d_in[4]; const u16* Cre = (const u16*)d_in[5]; const u16* Cim = (const u16*)d_in[6];
  const u16* logdt = (const u16*)d_in[7]; const u16* Dsk = (const u16*)d_in[8];
  const u16* w1 = (const u16*)d_in[10]; const u16* b1 = (const u16*)d_in[11]; const u16* w2 = (const u16*)d_in[12]; const u16* b2 = (const u16*)d_in[13];
  char* ws = (char*)d_ws;
  u16* xn = (u16*)ws; u16* h = (u16*)(ws + (size_t)S * D * 2); float* bu = (float*)(ws + (size_t)S * D * 4);
  float* u = (float*)(ws + (size_t)S * D * 4 + (size_t)S * P * 8);
  k_ln<<<S, 256, 0, stream>>>(x, xn, h);
  k_bu<<<dim3(P / 64, S / 4), 256, 0, stream>>>(xn, Bre, Bim, bu);
  k_scan<<<P / 64, 64, 0, stream>>>(bu, Lre, Lim, logdt);
  k_u<<<dim3(F / 64, D / 4), 256, 0, stream>>>(h, w1, b1, u);
  k_out<<<dim3(S / 64, D / 4), 256, 0, stream>>>(x, xn, bu, Cre, Cim, Dsk, u, w2, b2, (float*)d_out);
}
```

```cpp
#include <hip/hip_runtime.h>
#include <stdint.h>

typedef unsigned short u16;
typedef __bf16 bf16x8 __attribute__((ext_vector_type(8)));
typedef short s16x4 __attribute__((ext_vector_type(4)));
typedef short s16x8 __attribute__((ext_vector_type(8)));
typedef float f32x4 __attribute__((ext_vector_type(4)));
typedef unsigned u32x4 __attribute__((ext_vector_type(4)));
typedef unsigned u32x2 __attribute__((ext_vector_type(2)));
#define LAS __attribute__((address_space(3)))
#define GAS __attribute__((address_space(1)))
typedef GAS unsigned gu32;
typedef GAS unsigned long long gu64;
#define RLX_AGENT __ATOMIC_RELAXED, __HIP_MEMORY_SCOPE_AGENT

constexpr int S = 2048, D = 1024, P = 1024, F = 3584;
constexpr size_t MB = 1024 * 1024;
constexpr size_t WS_XN = 0, WS_HL = 4 * MB, WS_HT = 8 * MB, WS_U = 16 * MB, WS_XB = 24 * MB, WS_SYNC = 32 * MB;
constexpr int GRAN_BYTES = 8 * P * 16;
constexpr int FLAG_BYTES = 256 * 256;
constexpr int SYNC_BYTES = GRAN_BYTES + FLAG_BYTES;
constexpr unsigned SPIN_CAP = 1u << 22;

__device__ __forceinline__ float bf2f(u16 v) { return __uint_as_float(((unsigned)v) << 16); }
__device__ __forceinline__ float bflo(unsigned v) { return __uint_as_float(v << 16); }
__device__ __forceinline__ float bfhi(unsigned v) { return __uint_as_float(v & 0xffff0000u); }
__device__ __forceinline__ unsigned pk_bf16(float lo, float hi) {
  typedef __bf16 bf16x2 __attribute__((ext_vector_type(2)));
  typedef float f32x2 __attribute__((ext_vector_type(2)));
  f32x2 v = {lo, hi};
  bf16x2 r = __builtin_convertvector(v, bf16x2);
  return __builtin_bit_cast(unsigned, r);
}
__device__ __forceinline__ float rbf(float f) { return bflo(pk_bf16(f, f)); }

__device__ __forceinline__ void glds16(const void* g, LAS char* l) { __builtin_amdgcn_global_load_lds(g, (LAS void*)l, 16, 0, 0); }
template <int N> __device__ __forceinline__ void wait_vm() { asm volatile("s_waitcnt vmcnt(%0)" ::"n"(N) : "memory"); }
__device__ __forceinline__ void wait_lgkm0() { asm volatile("" ::: "memory"); __builtin_amdgcn_s_waitcnt(0xC07F); asm volatile("" ::: "memory"); }
__device__ __forceinline__ void bar() {
  asm volatile("" ::: "memory");
  __builtin_amdgcn_s_barrier();
  asm volatile("" ::: "memory");
}
#define SCHED_FENCE() __builtin_amdgcn_sched_barrier(0)
__device__ __forceinline__ bf16x8 lds_rd128(LAS char* p) { return *(LAS bf16x8*)p; }
__device__ __forceinline__ bf16x8 lds_rdtr(LAS char* p0, LAS char* p1) {
  s16x4 a = __builtin_amdgcn_ds_read_tr16_b64_v4i16((LAS s16x4*)p0);
  s16x4 b = __builtin_amdgcn_ds_read_tr16_b64_v4i16((LAS s16x4*)p1);
  s16x8 ab = __builtin_shufflevector(a, b, 0, 1, 2, 3, 4, 5, 6, 7);
  return __builtin_bit_cast(bf16x8, ab);
}
#define MFMA(a, b, c) __builtin_amdgcn_mfma_f32_16x16x32_bf16((a), (b), (c), 0, 0, 0)

template <int NR, int NM> __device__ __forceinline__ void sched_phase() {
#pragma unroll
  for (int i = 0; i < NR; ++i) {
    __builtin_amdgcn_sched_group_barrier(0x008, 1, 0);
    __builtin_amdgcn_sched_group_barrier(0x100, 1, 0);
  }
  if (NM > NR) __builtin_amdgcn_sched_group_barrier(0x008, NM - NR, 0);
}

template <int L, int NS, class Issue> __device__ __forceinline__ void producer_loop(int nt, Issue issue) {
#pragma unroll 1
  for (int t = 0; t < NS - 1; ++t) issue(t, t);
  int slot = NS - 1;
#pragma unroll 1
  for (int t = 0; t < nt; ++t) {
    const int rem = nt - 1 - t;
    if (rem >= NS - 2) wait_vm<(NS - 2) * L>();
    else if (NS > 3 && rem == 1) wait_vm<L>();
    else if (NS > 4 && rem == 2) wait_vm<2 * L>();
    else if (NS > 5 && rem == 3) wait_vm<3 * L>();
    else if (NS > 6 && rem == 4) wait_vm<4 * L>();
    else wait_vm<0>();
    bar();
    if (t + NS - 1 < nt) { issue(t + NS - 1, slot); slot = (slot + 1 == NS) ? 0 : slot + 1; }
  }
}

__device__ __forceinline__ float wave_sum(float v) {
  v += __builtin_bit_cast(float, __builtin_amdgcn_update_dpp(0, __builtin_bit_cast(int, v), 0xB1, 0xf, 0xf, true));
  v += __builtin_bit_cast(float, __builtin_amdgcn_update_dpp(0, __builtin_bit_cast(int, v), 0x4E, 0xf, 0xf, true));
  v += __builtin_bit_cast(float, __builtin_amdgcn_update_dpp(0, __builtin_bit_cast(int, v), 0x141, 0xf, 0xf, true));
  v += __builtin_bit_cast(float, __builtin_amdgcn_update_dpp(0, __builtin_bit_cast(int, v), 0x140, 0xf, 0xf, true));
  const int iv = __builtin_bit_cast(int, v);
  const float a = __builtin_bit_cast(float, __builtin_amdgcn_readlane(iv, 0)), b = __builtin_bit_cast(float, __builtin_amdgcn_readlane(iv, 16));
  const float c = __builtin_bit_cast(float, __builtin_amdgcn_readlane(iv, 32)), d = __builtin_bit_cast(float, __builtin_amdgcn_readlane(iv, 48));
  return (a + b) + (c + d);
}

__global__ __launch_bounds__(256) void k0_ln(const u16* __restrict__ x, u16* __restrict__ xn, u16* __restrict__ hl, u32x4* __restrict__ sync) {
  const int lane = threadIdx.x & 63, row = blockIdx.x * 4 + (threadIdx.x >> 6);
  const int gid = blockIdx.x * 256 + threadIdx.x;
  const u32x4 r0 = *(const u32x4*)(x + (size_t)row * D + lane * 8), r1 = *(const u32x4*)(x + (size_t)row * D + 512 + lane * 8);
  if (gid < SYNC_BYTES / 16) sync[gid] = (u32x4){0u, 0u, 0u, 0u};
  float v[16];
#pragma unroll
  for (int i = 0; i < 4; ++i) { v[2 * i] = bflo(r0[i]); v[2 * i + 1] = bfhi(r0[i]); v[8 + 2 * i] = bflo(r1[i]); v[8 + 2 * i + 1] = bfhi(r1[i]); }
#pragma unroll
  for (int pass = 0; pass < 2; ++pass) {
    float s = 0.f;
#pragma unroll
    for (int i = 0; i < 16; ++i) s += v[i];
    const float mean = wave_sum(s) * (1.0f / D);
    float q = 0.f;
#pragma unroll
    for (int i = 0; i < 16; ++i) { const float d = v[i] - mean; q = fmaf(d, d, q); }
    const float var = wave_sum(q) * (1.0f / D);
    const float mb = rbf(mean), sd = rbf(sqrtf(rbf(rbf(var) + 1e-5f)));
    u32x4 o0, o1;
#pragma unroll
    for (int i = 0; i < 8; ++i) {
      const unsigned dd = pk_bf16(v[2 * i] - mb, v[2 * i + 1] - mb);
      const unsigned w = pk_bf16(bflo(dd) / sd, bfhi(dd) / sd);
      v[2 * i] = bflo(w); v[2 * i + 1] = bfhi(w);
      if (i < 4) o0[i] = w; else o1[i - 4] = w;
    }
    u16* o = (pass == 0 ? xn : hl) + (size_t)row * D + lane * 8;
    *(u32x4*)o = o0; *(u32x4*)(o + 512) = o1;
  }
}

struct K1Args {
  const u16 *xn, *hl, *Bre, *Bim, *Lre, *Lim, *logdt, *w1, *b1;
  u16 *hT, *u;
  unsigned long long* gran;
};

constexpr int A1_NS = 3, A1_STAGE = 49152, A1_NT = D / 64;
__device__ __forceinline__ void a1_body(const K1Args& a, LAS char* lds, int chunk, int cb, int wid, int lane) {
  const int T0 = chunk * 256, p0 = cb * 64;
  if (wid >= 4) {
    const int pw = wid - 4;
    if (pw < 2) {
      unsigned voff[16];
#pragma unroll
      for (int j = 0; j < 16; ++j) {
        const int rho = 8 * j + (lane >> 3), m = rho >> 4, i = rho & 15;
        const int tok = T0 + 128 * pw + 32 * (i >> 2) + 4 * m + (i & 3);
        const int c = (lane & 7) ^ ((rho >> 1) & 7);
        voff[j] = (unsigned)(tok * D + c * 8) * 2u;
      }
      producer_loop<16, A1_NS>(A1_NT, [&](int t, int slot) {
        const char* base = (const char*)a.xn + t * 128;
        LAS char* l = lds + slot * A1_STAGE + pw * 16384;
#pragma unroll
        for (int j = 0; j < 16; ++j) glds16(base + voff[j], l + j * 1024);
      });
    } else {
      const int part = pw - 2;
      unsigned voff[8];
#pragma unroll
      for (int j = 0; j < 8; ++j) {
        const int wn = j >> 2, q = (j >> 1) & 1, hf = j & 1, i = hf * 8 + (lane >> 3);
        const int st = p0 + 32 * wn + 16 * q + i;
        const int c = (lane & 7) ^ ((i >> 1) & 7);
        voff[j] = (unsigned)(st * D + c * 8) * 2u;
      }
      const u16* Bm = part ? a.Bim : a.Bre;
      producer_loop<8, A1_NS>(A1_NT, [&](int t, int slot) {
        const char* base = (const char*)Bm + t * 128;
        LAS char* l = lds + slot * A1_STAGE + 32768 + part * 2048;
#pragma unroll
        for (int j = 0; j < 8; ++j) glds16(base + voff[j], l + (j >> 1) * 4096 + (j & 1) * 1024);
      });
    }
    return;
  }
  const int wm = wid >> 1, wn = wid & 1, cl = lane & 15, g = lane >> 4;
  const unsigned rb = (unsigned)(cl * 128 + ((g ^ (cl >> 1)) * 16));
  const unsigned abase = rb + wm * 16384, bbase = rb + 32768 + wn * 8192;
  f32x4 acc[8][4];
#pragma unroll
  for (int m = 0; m < 8; ++m)
#pragma unroll
    for (int n = 0; n < 4; ++n) acc[m][n] = (f32x4){0.f, 0.f, 0.f, 0.f};
  bf16x8 xa[8], xb[4], ya[8], yb[4];
#define A1_READ(fa, fb, so, ks) do { LAS char* pa_ = lds + (so) + (abase ^ ((ks) * 64)); LAS char* pb_ = lds + (so) + (bbase ^ ((ks) * 64)); \
    fb[0] = lds_rd128(pb_); fa[0] = lds_rd128(pa_); \
    _Pragma("unroll") for (int n = 1; n < 4; ++n) fb[n] = lds_rd128(pb_ + n * 2048); \
    _Pragma("unroll") for (int m = 1; m < 8; ++m) fa[m] = lds_rd128(pa_ + m * 2048); } while (0)
#define A1_MMA(fa, fb) do { _Pragma("unroll") for (int m = 0; m < 8; ++m) _Pragma("unroll") for (int n = 0; n < 4; ++n) acc[m][n] = MFMA(fa[m], fb[n], acc[m][n]); } while (0)
  int so = 0;
  bar();
  A1_READ(xa, xb, so, 0);
#pragma unroll 1
  for (int t = 0; t < A1_NT - 1; ++t) {
    SCHED_FENCE();
    A1_READ(ya, yb, so, 1);
    A1_MMA(xa, xb);
    sched_phase<12, 32>();
    SCHED_FENCE();
    wait_lgkm0();
    bar();
    SCHED_FENCE();
    so = (so + A1_STAGE == A1_NS * A1_STAGE) ? 0 : so + A1_STAGE;
    A1_READ(xa, xb, so, 0);
    A1_MMA(ya, yb);
    sched_phase<12, 32>();
  }
  SCHED_FENCE();
  A1_READ(ya, yb, so, 1);
  A1_MMA(xa, xb);
  sched_phase<12, 32>();
  SCHED_FENCE();
  A1_MMA(ya, yb);
#undef A1_READ
#undef A1_MMA
  float ar[2], ai[2], cr[2], ci[2], l32r[2], l32i[2], l64r[2], l64i[2], l128r[2], l128i[2], l256r[2], l256i[2];
#pragma unroll
  for (int q = 0; q < 2; ++q) {
    const int st = p0 + 32 * wn + 16 * q + cl;
    const float lr = bf2f(a.Lre[st]), li = bf2f(a.Lim[st]), dt = rbf(expf(bf2f(a.logdt[st])));
    const float er = expf(dt * lr);
    float sn, cs;
    sincosf(dt * li, &sn, &cs);
    ar[q] = er * cs; ai[q] = er * sn;
    const float nr = ar[q] - 1.0f, ni = ai[q], den = lr * lr + li * li;
    cr[q] = 2.0f * (nr * lr + ni * li) / den; ci[q] = 2.0f * (ni * lr - nr * li) / den;
    float pr = ar[q], pi = ai[q];
#pragma unroll
    for (int k = 0; k < 5; ++k) { const float t = pr * pr - pi * pi; pi = 2.0f * pr * pi; pr = t; }
    l32r[q] = pr; l32i[q] = pi;
    l64r[q] = pr * pr - pi * pi; l64i[q] = 2.0f * pr * pi;
    l128r[q] = l64r[q] * l64r[q] - l64i[q] * l64i[q]; l128i[q] = 2.0f * l64r[q] * l64i[q];
    l256r[q] = l128r[q] * l128r[q] - l128i[q] * l128i[q]; l256i[q] = 2.0f * l128r[q] * l128i[q];
  }
  float er[2] = {0.f, 0.f}, ei[2] = {0.f, 0.f};
#pragma unroll
  for (int m = 0; m < 8; ++m)
#pragma unroll
    for (int j = 0; j < 4; ++j)
#pragma unroll
      for (int q = 0; q < 2; ++q) {
        const float nr = fmaf(ar[q], er[q], fmaf(-ai[q], ei[q], acc[m][2 * q][j]));
        const float ni = fmaf(ar[q], ei[q], fmaf(ai[q], er[q], acc[m][2 * q + 1][j]));
        er[q] = nr; ei[q] = ni;
      }
  float pr_[2], pi_[2];
  LAS float* tot = (LAS float*)(lds + A1_NS * A1_STAGE);
#pragma unroll
  for (int q = 0; q < 2; ++q) {
    float vr = __shfl_up(er[q], 16), vi = __shfl_up(ei[q], 16);
    if (g >= 1) { const float t = er[q] + (l32r[q] * vr - l32i[q] * vi); ei[q] = ei[q] + (l32r[q] * vi + l32i[q] * vr); er[q] = t; }
    vr = __shfl_up(er[q], 32); vi = __shfl_up(ei[q], 32);
    if (g >= 2) { const float t = er[q] + (l64r[q] * vr - l64i[q] * vi); ei[q] = ei[q] + (l64r[q] * vi + l64i[q] * vr); er[q] = t; }
    vr = __shfl_up(er[q], 16); vi = __shfl_up(ei[q], 16);
    pr_[q] = g >= 1 ? vr : 0.f; pi_[q] = g >= 1 ? vi : 0.f;
    if (wm == 0 && g == 3) { tot[(32 * wn + 16 * q + cl) * 2] = er[q]; tot[(32 * wn + 16 * q + cl) * 2 + 1] = ei[q]; }
  }
  wait_lgkm0();
  bar();
  float t0r[2], t0i[2];
  gu64* gran = (gu64*)a.gran;
#pragma unroll
  for (int q = 0; q < 2; ++q) {
    t0r[q] = tot[(32 * wn + 16 * q + cl) * 2]; t0i[q] = tot[(32 * wn + 16 * q + cl) * 2 + 1];
    if (wm == 1 && g == 3 && chunk < 7) {
      const float xr = er[q] + (l128r[q] * t0r[q] - l128i[q] * t0i[q]), xi = ei[q] + (l128r[q] * t0i[q] + l128i[q] * t0r[q]);
      gu64* gp = gran + ((size_t)chunk * P + p0 + 32 * wn + 16 * q + cl) * 2;
      __hip_atomic_store(gp, (1ull << 32) | __float_as_uint(xr), RLX_AGENT);
      __hip_atomic_store(gp + 1, (1ull << 32) | __float_as_uint(xi), RLX_AGENT);
    }
  }
  float hr[2] = {0.f, 0.f}, hi[2] = {0.f, 0.f};
  if (chunk > 0) {
    float gr[2][2], gi[2][2];
    for (unsigned spins = 0;; ++spins) {
      bool ok = true;
#pragma unroll
      for (int q = 0; q < 2; ++q)
#pragma unroll
        for (int k = 0; k < 2; ++k) {
          const int cc = g + 4 * k;
          gr[q][k] = 0.f; gi[q][k] = 0.f;
          if (cc < chunk) {
            gu64* gp = gran + ((size_t)cc * P + p0 + 32 * wn + 16 * q + cl) * 2;
            const unsigned long long vr = __hip_atomic_load(gp, RLX_AGENT), vi = __hip_atomic_load(gp + 1, RLX_AGENT);
            ok &= ((vr >> 32) == 1ull) & ((vi >> 32) == 1ull);
            gr[q][k] = __uint_as_float((unsigned)vr); gi[q][k] = __uint_as_float((unsigned)vi);
          }
        }
      if (__all(ok) || spins > SPIN_CAP) break;
      __builtin_amdgcn_s_sleep(1);
    }
#pragma unroll
    for (int q = 0; q < 2; ++q) {
      float sr = 0.f, si = 0.f;
#pragma unroll
      for (int k = 0; k < 2; ++k) {
        float vr = gr[q][k], vi = gi[q][k];
        const int e = chunk - 1 - (g + 4 * k);
#pragma unroll
        for (int i = 0; i < 6; ++i)
          if (i < e) { const float t = l256r[q] * vr - l256i[q] * vi; vi = l256r[q] * vi + l256i[q] * vr; vr = t; }
        sr += vr; si += vi;
      }
      sr += __shfl_xor(sr, 16); si += __shfl_xor(si, 16);
      sr += __shfl_xor(sr, 32); si += __shfl_xor(si, 32);
      hr[q] = sr; hi[q] = si;
    }
  }
#pragma unroll
  for (int q = 0; q < 2; ++q) {
    if (wm == 1) { const float t = t0r[q] + (l128r[q] * hr[q] - l128i[q] * hi[q]); hi[q] = t0i[q] + (l128r[q] * hi[q] + l128i[q] * hr[q]); hr[q] = t; }
    if (g & 1) { const float t = l32r[q] * hr[q] - l32i[q] * hi[q]; hi[q] = l32r[q] * hi[q] + l32i[q] * hr[q]; hr[q] = t; }
    if (g & 2) { const float t = l64r[q] * hr[q] - l64i[q] * hi[q]; hi[q] = l64r[q] * hi[q] + l64i[q] * hr[q]; hr[q] = t; }
    hr[q] += pr_[q]; hi[q] += pi_[q];
  }
#pragma unroll
  for (int mm = 0; mm < 4; ++mm) {
    u32x4 ore[2], oim[2];
#pragma unroll
    for (int h2 = 0; h2 < 2; ++h2) {
      float vr[2][4], vi[2][4];
#pragma unroll
      for (int j = 0; j < 4; ++j)
#pragma unroll
        for (int q = 0; q < 2; ++q) {
          const int m = 2 * mm + h2;
          const float nr = fmaf(ar[q], hr[q], fmaf(-ai[q], hi[q], acc[m][2 * q][j]));
          const float ni = fmaf(ar[q], hi[q], fmaf(ai[q], hr[q], acc[m][2 * q + 1][j]));
          hr[q] = nr; hi[q] = ni;
          vr[q][j] = cr[q] * nr - ci[q] * ni;
          vi[q][j] = -(cr[q] * ni + ci[q] * nr);
        }
#pragma unroll
      for (int q = 0; q < 2; ++q) {
        ore[q][2 * h2] = pk_bf16(vr[q][0], vr[q][1]); ore[q][2 * h2 + 1] = pk_bf16(vr[q][2], vr[q][3]);
        oim[q][2 * h2] = pk_bf16(vi[q][0], vi[q][1]); oim[q][2 * h2 + 1] = pk_bf16(vi[q][2], vi[q][3]);
      }
    }
#pragma unroll
    for (int q = 0; q < 2; ++q) {
      u16* o = a.hT + (size_t)(p0 + 32 * wn + 16 * q + cl) * S + T0 + 128 * wm + 32 * g + 8 * mm;
      *(u32x4*)o = ore[q];
      *(u32x4*)(o + (size_t)P * S) = oim[q];
    }
  }
}

constexpr int B1_NS = 7, B1_ABYTES = 32 * 448, B1_STAGE = B1_ABYTES + 32 * 256, B1_NT = S / 32;
__device__ __forceinline__ float gelu_tanh(float x) {
  const float z = x * fmaf(x * x, -0.10294323967f, -2.30220819813f);
  return x * __builtin_amdgcn_rcpf(1.0f + __builtin_amdgcn_exp2f(z));
}
__device__ __forceinline__ void b1_body(const K1Args& a, LAS char* lds, int ft, int dt, int wid, int lane) {
  const int f0 = ft * 224, d0 = dt * 128;
  if (wid >= 4) {
    const int pw = wid - 4;
    if (pw < 2) {
      unsigned voff[7];
#pragma unroll
      for (int j = 0; j < 7; ++j) {
        const int ch = (pw * 7 + j) * 64 + lane, k = ch / 28, pos = ch - k * 28;
        const int c = pos ^ (3 * ((k >> 3) & 1));
        voff[j] = (unsigned)(k * F + f0 + c * 8) * 2u;
      }
      producer_loop<7, B1_NS>(B1_NT, [&](int t, int slot) {
        const char* base = (const char*)a.w1 + (size_t)t * (32 * F * 2);
        LAS char* l = lds + slot * B1_STAGE + pw * 7168;
#pragma unroll
        for (int j = 0; j < 7; ++j) glds16(base + voff[j], l + j * 1024);
      });
    } else {
      unsigned voff[4];
#pragma unroll
      for (int j = 0; j < 4; ++j) {
        const int k = ((pw - 2) * 4 + j) * 4 + (lane >> 4), pos = lane & 15;
        const int c = pos ^ (((k & 3) << 1) | (((k >> 3) & 1) << 3));
        voff[j] = (unsigned)(k * D + d0 + c * 8) * 2u;
      }
      producer_loop<4, B1_NS>(B1_NT, [&](int t, int slot) {
        const char* base = (const char*)a.hl + (size_t)t * (32 * D * 2);
        LAS char* l = lds + slot * B1_STAGE + B1_ABYTES + (pw - 2) * 4096;
#pragma unroll
        for (int j = 0; j < 4; ++j) glds16(base + voff[j], l + j * 1024);
      });
    }
    return;
  }
  const int wm = wid >> 1, wn = wid & 1, g = lane >> 4, q = (lane & 15) >> 2, p = lane & 3;
  const int k0 = 8 * g + q;
  const unsigned a0 = (unsigned)(k0 * 448 + ((14 * wm + p) ^ (3 * (g & 1))) * 16 + (p & 1) * 8);
  const unsigned a1 = a0 ^ 8u;
  const unsigned a2 = (unsigned)(k0 * 448 + ((14 * wm + 12 + (p >> 1)) ^ (3 * (g & 1))) * 16 + (p & 1) * 8);
  unsigned bb[4];
#pragma unroll
  for (int n = 0; n < 4; ++n) bb[n] = (unsigned)(B1_ABYTES + k0 * 256 + ((8 * wn + 2 * n + (p >> 1)) ^ ((q << 1) | ((g & 1) << 3))) * 16 + (p & 1) * 8);
  f32x4 acc[7][4];
#pragma unroll
  for (int m = 0; m < 7; ++m)
#pragma unroll
    for (int n = 0; n < 4; ++n) acc[m][n] = (f32x4){0.f, 0.f, 0.f, 0.f};
  bf16x8 xa[7], xb[4], ya[7], yb[4];
#define B1_READ(fa, fb, so) do { LAS char* s_ = lds + (so); \
    fb[0] = lds_rdtr(s_ + bb[0], s_ + bb[0] + 1024); fa[0] = lds_rdtr(s_ + a0, s_ + a0 + 1792); \
    _Pragma("unroll") for (int n = 1; n < 4; ++n) fb[n] = lds_rdtr(s_ + bb[n], s_ + bb[n] + 1024); \
    _Pragma("unroll") for (int m = 1; m < 6; ++m) { const unsigned o_ = ((m & 1) ? a1 : a0) + (m >> 1) * 64; fa[m] = lds_rdtr(s_ + o_, s_ + o_ + 1792); } \
    fa[6] = lds_rdtr(s_ + a2, s_ + a2 + 1792); } while (0)
#define B1_MMA(fa, fb) do { _Pragma("unroll") for (int m = 0; m < 7; ++m) _Pragma("unroll") for (int n = 0; n < 4; ++n) acc[m][n] = MFMA(fa[m], fb[n], acc[m][n]); } while (0)
#define B1_NEXT() so = (so + B1_STAGE == B1_NS * B1_STAGE) ? 0 : so + B1_STAGE
  int so = 0;
  bar();
  B1_READ(xa, xb, so);
#pragma unroll 1
  for (int t = 0; t < B1_NT - 2; t += 2) {
    SCHED_FENCE(); wait_lgkm0(); bar(); SCHED_FENCE();
    B1_NEXT();
    B1_READ(ya, yb, so);
    B1_MMA(xa, xb);
    sched_phase<22, 28>();
    SCHED_FENCE(); wait_lgkm0(); bar(); SCHED_FENCE();
    B1_NEXT();
    B1_READ(xa, xb, so);
    B1_MMA(ya, yb);
    sched_phase<22, 28>();
  }
  SCHED_FENCE(); wait_lgkm0(); bar(); SCHED_FENCE();
  B1_NEXT();
  B1_READ(ya, yb, so);
  B1_MMA(xa, xb);
  sched_phase<22, 28>();
  SCHED_FENCE();
  B1_MMA(ya, yb);
#undef B1_READ
#undef B1_MMA
#undef B1_NEXT
  const int cl = lane & 15, fw = f0 + 112 * wm;
  u32x2 bias[7];
#pragma unroll
  for (int m = 0; m < 6; ++m) bias[m] = *(const u32x2*)(a.b1 + fw + 32 * (m >> 1) + 8 * g + 4 * ((m & 1) ^ (g & 1)));
  bias[6] = *(const u32x2*)(a.b1 + fw + 96 + 4 * g);
#pragma unroll
  for (int n = 0; n < 4; ++n) {
    u16* o = a.u + (size_t)(d0 + 64 * wn + 16 * n + cl) * F + fw;
    u32x2 w[7];
#pragma unroll
    for (int m = 0; m < 7; ++m) {
      const float v0 = gelu_tanh(acc[m][n][0] + bflo(bias[m][0])), v1 = gelu_tanh(acc[m][n][1] + bfhi(bias[m][0]));
      const float v2 = gelu_tanh(acc[m][n][2] + bflo(bias[m][1])), v3 = gelu_tanh(acc[m][n][3] + bfhi(bias[m][1]));
      w[m][0] = pk_bf16(v0, v1); w[m][1] = pk_bf16(v2, v3);
    }
#pragma unroll
    for (int mm = 0; mm < 3; ++mm) {
      const bool sw = g & 1;
      const u32x2 lo = sw ? w[2 * mm + 1] : w[2 * mm], hi = sw ? w[2 * mm] : w[2 * mm + 1];
      *(u32x4*)(o + 32 * mm + 8 * g) = (u32x4){lo[0], lo[1], hi[0], hi[1]};
    }
    *(u32x2*)(o + 96 + 4 * g) = w[6];
  }
}

__global__ __launch_bounds__(512, 2) void k1_gemm(K1Args a) {
  extern __shared__ __attribute__((aligned(16))) char smem[];
  LAS char* lds = (LAS char*)smem;
  const int wid = __builtin_amdgcn_readfirstlane(threadIdx.x >> 6), lane = threadIdx.x & 63;
  const int x = blockIdx.x & 7, idx = blockIdx.x >> 3;
  if (idx < 16) a1_body(a, lds, 4 * (x & 1) + (idx & 3), 4 * (x >> 1) + (idx >> 2), wid, lane);
  else b1_body(a, lds, 4 * (x >> 1) + ((idx - 16) & 3), 4 * (x & 1) + ((idx - 16) >> 2), wid, lane);
}

struct K2Args {
  const u16 *x, *xn, *Cre, *Cim, *Dsk, *hT, *u, *w2, *b2;
  float* out;
  float* xb;
  unsigned* flags;
};
constexpr int K2_NS = 5, K2_STAGE = 32768, K2_NT = 44;
__global__ __launch_bounds__(512, 2) void k2_gemm(K2Args a) {
  extern __shared__ __attribute__((aligned(16))) char smem[];
  LAS char* lds = (LAS char*)smem;
  const int wid = __builtin_amdgcn_readfirstlane(threadIdx.x >> 6), lane = threadIdx.x & 63;
  const int x = blockIdx.x & 7, idx = blockIdx.x >> 3;
  const int kh = x & 1, t0 = (4 * (x >> 1) + (idx & 3)) * 128, d0 = (idx >> 2) * 128;
  const int kt0 = kh * K2_NT;
  if (wid >= 4) {
    const int pw = wid - 4;
    if (pw < 2) {
      const unsigned r0 = (unsigned)(d0 + 64 * pw + (lane >> 3));
      const unsigned ce = (unsigned)(((lane & 7) ^ (lane >> 4)) * 16), co = ce ^ 64u;
      const char *pcre = (const char*)a.Cre, *pcim = (const char*)a.Cim, *pu = (const char*)a.u;
      producer_loop<8, K2_NS>(K2_NT, [=](int t, int slot) {
        const int kt = kt0 + t;
        LAS char* l = lds + slot * K2_STAGE + pw * 8192;
        const unsigned ld = kt < 32 ? P * 2 : F * 2;
        const char* base = kt < 32 ? (kt < 16 ? pcre : pcim) + (kt & 15) * 128 : pu + (kt - 32) * 128;
        const unsigned ve = r0 * ld + ce, vo = r0 * ld + co;
#pragma unroll
        for (int j = 0; j < 8; ++j) glds16(base + (size_t)(j * 8 * ld) + ((j & 1) ? vo : ve), l + j * 1024);
      });
    } else {
      unsigned voff[8];
#pragma unroll
      for (int j = 0; j < 8; ++j) {
        const int k = ((pw - 2) * 8 + j) * 4 + (lane >> 4), pos = lane & 15;
        const int c = pos ^ (((k & 3) << 1) | (((k >> 3) & 1) << 3));
        voff[j] = (unsigned)(k * S + t0 + c * 8) * 2u;
      }
      const char *pht = (const char*)a.hT, *pw2 = (const char*)a.w2;
      producer_loop<8, K2_NS>(K2_NT, [=](int t, int slot) {
        const int kt = kt0 + t;
        const char* base = kt < 32 ? pht + (size_t)kt * (64 * S * 2) : pw2 + (size_t)(kt - 32) * (64 * S * 2);
        LAS char* l = lds + slot * K2_STAGE + 16384 + (pw - 2) * 8192;
#pragma unroll
        for (int j = 0; j < 8; ++j) glds16(base + voff[j], l + j * 1024);
      });
    }
    return;
  }
  const int wm = wid >> 1, wn = wid & 1, cl = lane & 15, g = lane >> 4, q = cl >> 2, p = lane & 3;
  const unsigned abase = (unsigned)((wm * 64 + cl) * 128 + ((g ^ (cl >> 1)) * 16));
  unsigned bb[4];
#pragma unroll
  for (int n = 0; n < 4; ++n) bb[n] = (unsigned)(16384 + (8 * g + q) * 256 + ((8 * wn + 2 * n + (p >> 1)) ^ ((q << 1) | ((g & 1) << 3))) * 16 + (p & 1) * 8);
  f32x4 acc[4][4];
  {
    f32x4 init[2][4];
#pragma unroll
    for (int mi = 0; mi < 2; ++mi) {
      const int d = d0 + 64 * wm + 16 * (2 * kh + mi) + 4 * g;
      const u32x2 dk = *(const u32x2*)(a.Dsk + d);
#pragma unroll
      for (int n = 0; n < 4; ++n) {
        const int tok = t0 + 64 * wn + 16 * n + cl;
        const u32x2 xv = *(const u32x2*)(a.x + (size_t)tok * D + d), nv = *(const u32x2*)(a.xn + (size_t)tok * D + d);
        const float b2 = bf2f(a.b2[tok]);
        const unsigned s0 = pk_bf16(bflo(nv[0]) * bflo(dk[0]), bfhi(nv[0]) * bfhi(dk[0])), s1 = pk_bf16(bflo(nv[1]) * bflo(dk[1]), bfhi(nv[1]) * bfhi(dk[1]));
        init[mi][n] = (f32x4){bflo(xv[0]) + bflo(s0) + b2, bfhi(xv[0]) + bfhi(s0) + b2, bflo(xv[1]) + bflo(s1) + b2, bfhi(xv[1]) + bfhi(s1) + b2};
      }
    }
    const f32x4 z = {0.f, 0.f, 0.f, 0.f};
#pragma unroll
    for (int n = 0; n < 4; ++n) {
      acc[0][n] = kh ? z : init[0][n]; acc[1][n] = kh ? z : init[1][n];
      acc[2][n] = kh ? init[0][n] : z; acc[3][n] = kh ? init[1][n] : z;
    }
  }
  bf16x8 xa[4], xb[4], ya[4], yb[4];
#define K2_READ(fa, fb, so, ks) do { LAS char* pa_ = lds + (so) + (abase ^ ((ks) * 64)); LAS char* s_ = lds + (so) + (ks) * 8192; \
    fa[0] = lds_rd128(pa_); fb[0] = lds_rdtr(s_ + bb[0], s_ + bb[0] + 1024); \
    _Pragma("unroll") for (int n = 1; n < 4; ++n) fb[n] = lds_rdtr(s_ + bb[n], s_ + bb[n] + 1024); \
    _Pragma("unroll") for (int m = 1; m < 4; ++m) fa[m] = lds_rd128(pa_ + m * 2048); } while (0)
#define K2_MMA(fa, fb) do { _Pragma("unroll") for (int m = 0; m < 4; ++m) _Pragma("unroll") for (int n = 0; n < 4; ++n) acc[m][n] = MFMA(fa[m], fb[n], acc[m][n]); } while (0)
  int so = 0;
  bar();
  K2_READ(xa, xb, so, 0);
#pragma unroll 1
  for (int t = 0; t < K2_NT - 1; ++t) {
    SCHED_FENCE();
    K2_READ(ya, yb, so, 1);
    K2_MMA(xa, xb);
    sched_phase<12, 16>();
    SCHED_FENCE();
    wait_lgkm0();
    bar();
    SCHED_FENCE();
    so = (so + K2_STAGE == K2_NS * K2_STAGE) ? 0 : so + K2_STAGE;
    K2_READ(xa, xb, so, 0);
    K2_MMA(ya, yb);
    sched_phase<12, 16>();
  }
  SCHED_FENCE();
  K2_READ(ya, yb, so, 1);
  K2_MMA(xa, xb);
  sched_phase<12, 16>();
  SCHED_FENCE();
  K2_MMA(ya, yb);
#undef K2_READ
#undef K2_MMA
  const int me = blockIdx.x, pa = blockIdx.x ^ 1;
  __amdgpu_buffer_rsrc_t rs = __builtin_amdgcn_make_buffer_rsrc((void*)a.xb, 0, 256 * 32768, 0x00020000);
  const unsigned xo = (unsigned)(wid * 8192 + lane * 16);
  f32x4 keep[2][4];
#pragma unroll
  for (int mi = 0; mi < 2; ++mi)
#pragma unroll
    for (int n = 0; n < 4; ++n) {
      const f32x4 snd = kh ? acc[mi][n] : acc[2 + mi][n];
      keep[mi][n] = kh ? acc[2 + mi][n] : acc[mi][n];
      __builtin_amdgcn_raw_buffer_store_b128(__builtin_bit_cast(u32x4, snd), rs, xo + (mi * 4 + n) * 1024, me * 32768, 16);
    }
  wait_vm<0>();
  bar();
  gu32* flags = (gu32*)a.flags;
  if (wid == 0) {
    if (lane == 0) __hip_atomic_store(flags + me * 64, 1u, RLX_AGENT);
    for (unsigned spins = 0; spins < SPIN_CAP; ++spins) {
      if (__builtin_amdgcn_readfirstlane(__hip_atomic_load(flags + pa * 64, RLX_AGENT)) == 1u) break;
      __builtin_amdgcn_s_sleep(1);
    }
  }
  bar();
#pragma unroll
  for (int mi = 0; mi < 2; ++mi)
#pragma unroll
    for (int n = 0; n < 4; ++n) {
      const u32x4 r = __builtin_amdgcn_raw_buffer_load_b128(rs, xo + (mi * 4 + n) * 1024, pa * 32768, 16);
      keep[mi][n] += __builtin_bit_cast(f32x4, r);
    }
#pragma unroll
  for (int mi = 0; mi < 2; ++mi)
#pragma unroll
    for (int n = 0; n < 4; ++n) {
      const int d = d0 + 64 * wm + 16 * (2 * kh + mi) + 4 * g, tok = t0 + 64 * wn + 16 * n + cl;
      *(f32x4*)(a.out + (size_t)tok * D + d) = keep[mi][n];
    }
}

constexpr int K1_LDS = 160 * 1024 - 2048, K2_LDS = K2_NS * K2_STAGE;
static_assert(A1_NS * A1_STAGE + 512 <= K1_LDS && B1_NS * B1_STAGE <= K1_LDS, "lds");

extern "C" void kernel_launch(void* const* d_in, const int* in_sizes, int n_in, void* d_out, int out_size, void* d_ws, size_t ws_size,
                              hipStream_t stream) {
  char* ws = (char*)d_ws;
  (void)hipFuncSetAttribute((const void*)k1_gemm, hipFuncAttributeMaxDynamicSharedMemorySize, K1_LDS);
  (void)hipFuncSetAttribute((const void*)k2_gemm, hipFuncAttributeMaxDynamicSharedMemorySize, K2_LDS);
  k0_ln<<<S / 4, 256, 0, stream>>>((const u16*)d_in[0], (u16*)(ws + WS_XN), (u16*)(ws + WS_HL), (u32x4*)(ws + WS_SYNC));
  K1Args a1;
  a1.xn = (const u16*)(ws + WS_XN); a1.hl = (const u16*)(ws + WS_HL);
  a1.Bre = (const u16*)d_in[3]; a1.Bim = (const u16*)d_in[4]; a1.Lre = (const u16*)d_in[1]; a1.Lim = (const u16*)d_in[2];
  a1.logdt = (const u16*)d_in[7]; a1.w1 = (const u16*)d_in[10]; a1.b1 = (const u16*)d_in[11];
  a1.hT = (u16*)(ws + WS_HT); a1.u = (u16*)(ws + WS_U); a1.gran = (unsigned long long*)(ws + WS_SYNC);
  k1_gemm<<<256, 512, K1_LDS, stream>>>(a1);
  K2Args a2;
  a2.x = (const u16*)d_in[0]; a2.xn = (const u16*)(ws + WS_XN); a2.Cre = (const u16*)d_in[5]; a2.Cim = (const u16*)d_in[6];
  a2.Dsk = (const u16*)d_in[8]; a2.hT = (const u16*)(ws + WS_HT); a2.u = (const u16*)(ws + WS_U); a2.w2 = (const u16*)d_in[12];
  a2.b2 = (const u16*)d_in[13]; a2.out = (float*)d_out; a2.xb = (float*)(ws + WS_XB); a2.flags = (unsigned*)(ws + WS_SYNC + GRAN_BYTES);
  k2_gemm<<<256, 512, K2_LDS, stream>>>(a2);
}
```

```cpp
#include <hip/hip_runtime.h>
#include <stdint.h>

typedef unsigned short u16;
typedef unsigned long long u64;
typedef __bf16 bf16x8 __attribute__((ext_vector_type(8)));
typedef short s16x4 __attribute__((ext_vector_type(4)));
typedef short s16x8 __attribute__((ext_vector_type(8)));
typedef float f32x4 __attribute__((ext_vector_type(4)));
typedef unsigned u32x4 __attribute__((ext_vector_type(4)));
typedef unsigned u32x2 __attribute__((ext_vector_type(2)));
#define LAS __attribute__((address_space(3)))
#define GAS __attribute__((address_space(1)))
typedef GAS unsigned long long gu64;
typedef __amdgpu_buffer_rsrc_t rsrc_t;
#define RLX_AGENT __ATOMIC_RELAXED, __HIP_MEMORY_SCOPE_AGENT

constexpr int S = 2048, D = 1024, P = 1024, F = 3584;
constexpr size_t MB = 1024 * 1024;
constexpr size_t WS_XN = 0, WS_HL = 4 * MB, WS_HT = 8 * MB, WS_U = 16 * MB, WS_XB = 24 * MB, WS_SYNC = 32 * MB;
constexpr int SY_LN = 64, SY_H = 320, SY_U = 448, SY_X = 576, SY_GR = 1024;
constexpr unsigned SPIN_CAP = 1u << 17;
#define NBA 16
#define STAMP(i)
#define PSTAMP(w, i)

__device__ __forceinline__ float bf2f(u16 v) { return __uint_as_float(((unsigned)v) << 16); }
__device__ __forceinline__ float bflo(unsigned v) { return __uint_as_float(v << 16); }
__device__ __forceinline__ float bfhi(unsigned v) { return __uint_as_float(v & 0xffff0000u); }
__device__ __forceinline__ unsigned pk_bf16(float lo, float hi) {
  typedef __bf16 bf16x2 __attribute__((ext_vector_type(2)));
  typedef float f32x2 __attribute__((ext_vector_type(2)));
  f32x2 v = {lo, hi};
  bf16x2 r = __builtin_convertvector(v, bf16x2);
  return __builtin_bit_cast(unsigned, r);
}
__device__ __forceinline__ float rbf(float f) { return bflo(pk_bf16(f, f)); }

__device__ __forceinline__ rsrc_t mk_rsrc(const void* p, unsigned bytes) { return __builtin_amdgcn_make_buffer_rsrc((void*)p, 0, bytes, 0x00020000); }
__device__ __forceinline__ void dma16(rsrc_t rs, unsigned voff, unsigned soff, LAS char* l) { __builtin_amdgcn_raw_ptr_buffer_load_lds(rs, (LAS void*)l, 16, voff, soff, 0, 0); }
__device__ __forceinline__ void dma16c(rsrc_t rs, unsigned voff, unsigned soff, LAS char* l) { __builtin_amdgcn_raw_ptr_buffer_load_lds(rs, (LAS void*)l, 16, voff, soff, 0, 16); }
__device__ __forceinline__ void st16_wt(rsrc_t rs, unsigned voff, unsigned soff, u32x4 v) { __builtin_amdgcn_raw_buffer_store_b128(v, rs, voff, soff, 16); }
__device__ __forceinline__ void st8_wt(rsrc_t rs, unsigned voff, unsigned soff, u32x2 v) { __builtin_amdgcn_raw_buffer_store_b64(v, rs, voff, soff, 16); }
template <int N> __device__ __forceinline__ void wait_vm() { asm volatile("s_waitcnt vmcnt(%0)" ::"n"(N) : "memory"); }
__device__ __forceinline__ void wait_lgkm0() { asm volatile("" ::: "memory"); __builtin_amdgcn_s_waitcnt(0xC07F); asm volatile("" ::: "memory"); }
__device__ __forceinline__ void bar() {
  asm volatile("" ::: "memory");
  __builtin_amdgcn_s_barrier();
  asm volatile("" ::: "memory");
}
#define SCHED_FENCE() __builtin_amdgcn_sched_barrier(0)
__device__ __forceinline__ bf16x8 lds_rd128(LAS char* p) { return *(LAS bf16x8*)p; }
__device__ __forceinline__ bf16x8 lds_rdtr(LAS char* p0, LAS char* p1) {
  s16x4 a = __builtin_amdgcn_ds_read_tr16_b64_v4i16((LAS s16x4*)p0);
  s16x4 b = __builtin_amdgcn_ds_read_tr16_b64_v4i16((LAS s16x4*)p1);
  s16x8 ab = __builtin_shufflevector(a, b, 0, 1, 2, 3, 4, 5, 6, 7);
  return __builtin_bit_cast(bf16x8, ab);
}
#define MFMA(a, b, c) __builtin_amdgcn_mfma_f32_16x16x32_bf16((a), (b), (c), 0, 0, 0)

template <int NR, int NM> __device__ __forceinline__ void sched_phase() {
#pragma unroll
  for (int i = 0; i < NR; ++i) {
    __builtin_amdgcn_sched_group_barrier(0x008, 1, 0);
    __builtin_amdgcn_sched_group_barrier(0x100, 1, 0);
  }
  if (NM > NR) __builtin_amdgcn_sched_group_barrier(0x008, NM - NR, 0);
}

template <int NL> __device__ __forceinline__ void wait_flags(gu64* p, bool active, u64 tag, int sid) {
#pragma unroll 1
  for (unsigned spins = 0; spins < SPIN_CAP; ++spins) {
    bool ok = true;
    if (active) {
#pragma unroll
      for (int k = 0; k < NL; ++k) ok &= __hip_atomic_load(p + 64 * k, RLX_AGENT) == tag;
    }
    if (__all(ok)) break;
    __builtin_amdgcn_s_sleep(2);
  }
  PSTAMP(4, sid); PSTAMP(6, sid);
  __builtin_amdgcn_fence(__ATOMIC_ACQUIRE, "wavefront");
}

template <int NS, class Issue> __device__ __forceinline__ void prod_prologue(Issue issue) {
#pragma unroll 1
  for (int t = 0; t < NS - 1; ++t) issue(t, t);
}
template <int L, int NS, class Issue> __device__ __forceinline__ void prod_loop(int nt, Issue issue) {
  int slot = NS - 1;
#pragma unroll 1
  for (int t = 0; t < nt; ++t) {
    const int rem = nt - 1 - t;
    if (rem >= NS - 2) wait_vm<(NS - 2) * L>();
    else if (NS > 3 && rem == 1) wait_vm<L>();
    else if (NS > 4 && rem == 2) wait_vm<2 * L>();
    else if (NS > 5 && rem == 3) wait_vm<3 * L>();
    else if (NS > 6 && rem == 4) wait_vm<4 * L>();
    else wait_vm<0>();
    bar();
    if (t + NS - 1 < nt) { issue(t + NS - 1, slot); slot = (slot + 1 == NS) ? 0 : slot + 1; }
  }
}

__device__ __forceinline__ float wave_sum(float v) {
  v += __builtin_bit_cast(float, __builtin_amdgcn_update_dpp(0, __builtin_bit_cast(int, v), 0xB1, 0xf, 0xf, true));
  v += __builtin_bit_cast(float, __builtin_amdgcn_update_dpp(0, __builtin_bit_cast(int, v), 0x4E, 0xf, 0xf, true));
  v += __builtin_bit_cast(float, __builtin_amdgcn_update_dpp(0, __builtin_bit_cast(int, v), 0x141, 0xf, 0xf, true));
  v += __builtin_bit_cast(float, __builtin_amdgcn_update_dpp(0, __builtin_bit_cast(int, v), 0x140, 0xf, 0xf, true));
  const int iv = __builtin_bit_cast(int, v);
  const float a = __builtin_bit_cast(float, __builtin_amdgcn_readlane(iv, 0)), b = __builtin_bit_cast(float, __builtin_amdgcn_readlane(iv, 16));
  const float c = __builtin_bit_cast(float, __builtin_amdgcn_readlane(iv, 32)), d = __builtin_bit_cast(float, __builtin_amdgcn_readlane(iv, 48));
  return (a + b) + (c + d);
}

struct Args {
  const u16 *x, *Lre, *Lim, *Bre, *Bim, *Cre, *Cim, *logdt, *Dsk, *w1, *b1, *w2, *b2;
  float* out;
  char* ws;
};

constexpr int A1_NS = 3, A1_STAGE = 49152, A1_NT = D / 64;
constexpr int B1_NS = 7, B1_ABYTES = 32 * 448, B1_STAGE = B1_ABYTES + 32 * 256, B1_NT = S / 32;
constexpr int P2_NS = 4, P2_STAGE = 32768;
constexpr int LDS_SCR = P2_NS * P2_STAGE;
constexpr int LDS_BYTES = 160 * 1024;
static_assert(A1_NS * A1_STAGE <= LDS_BYTES && B1_NS * B1_STAGE <= LDS_BYTES, "lds");

__device__ __forceinline__ float gelu_tanh(float x) {
  const float z = x * fmaf(x * x, -0.10294323967f, -2.30220819813f);
  return x * __builtin_amdgcn_rcpf(1.0f + __builtin_amdgcn_exp2f(z));
}

__global__ __launch_bounds__(512, 2) void fused(Args a) {
  extern __shared__ __attribute__((aligned(16))) char smem[];
  LAS char* lds = (LAS char*)smem;
  const int wid = __builtin_amdgcn_readfirstlane(threadIdx.x >> 6), lane = threadIdx.x & 63;
  const int bid = blockIdx.x, xc = bid & 7, idx = bid >> 3, xg = xc >> 1;
  const bool roleA = !(xc & 1);
  gu64* sy = (gu64*)(a.ws + WS_SYNC);
  const u64 tag = *(const u64*)(a.ws + WS_SYNC) + 1ull;
  const int chunk = 4 * (xg & 1) + (idx & 3), cb = 8 * (xg >> 1) + (idx >> 2);
  const int ft = 4 * xg + (idx & 3), dt1 = idx >> 2;
  const int tt = 4 * xg + (idx & 3), dt = idx >> 2;
  const int t0 = tt * 128, d0 = dt * 128;
  const int kt0 = roleA ? 0 : 32 + NBA, nt2 = roleA ? 32 + NBA : 56 - NBA;
  STAMP(0);

  if (wid >= 4) {
    const int pw = wid - 4;
    asm volatile("" ::"s"(tag));
    if (roleA) {
      const int T0 = chunk * 256, p0 = cb * 64;
      if (pw < 2) {
        unsigned voff[16];
#pragma unroll
        for (int j = 0; j < 16; ++j) {
          const int rho = 8 * j + (lane >> 3), m = rho >> 4, i = rho & 15;
          const int tok = T0 + 128 * pw + 32 * (i >> 2) + 4 * m + (i & 3);
          const int c = (lane & 7) ^ ((rho >> 1) & 7);
          voff[j] = (unsigned)(tok * D + c * 8) * 2u;
        }
        const rsrc_t rs = mk_rsrc(a.ws + WS_XN, S * D * 2);
        auto issue = [&](int t, int slot) {
          LAS char* l = lds + slot * A1_STAGE + pw * 16384;
#pragma unroll
          for (int j = 0; j < 16; ++j) dma16c(rs, voff[j], t * 128, l + j * 1024);
        };
        bar();
        const int fb = lane < 16 ? (T0 + 128 * pw) / 8 + lane : ((((cb & 7) << 2) | (lane & 3)) << 3) + ((((cb >> 3) << 1) | ((lane >> 2) & 1)) << 1);
        wait_flags<1>(sy + SY_LN + fb, lane < 24, tag, 16);
        prod_prologue<A1_NS>(issue);
        prod_loop<16, A1_NS>(A1_NT, issue);
      } else {
        const int part = pw - 2;
        unsigned voff[8];
#pragma unroll
        for (int j = 0; j < 8; ++j) {
          const int wn = j >> 2, q = (j >> 1) & 1, hf = j & 1, i = hf * 8 + (lane >> 3);
          const int st = p0 + 32 * wn + 16 * q + i;
          const int c = (lane & 7) ^ ((i >> 1) & 7);
          voff[j] = (unsigned)(st * D + c * 8) * 2u;
        }
        const rsrc_t rs = mk_rsrc(part ? a.Bim : a.Bre, P * D * 2);
        auto issue = [&](int t, int slot) {
          LAS char* l = lds + slot * A1_STAGE + 32768 + part * 2048;
#pragma unroll
          for (int j = 0; j < 8; ++j) dma16(rs, voff[j], t * 128, l + (j >> 1) * 4096 + (j & 1) * 1024);
        };
        prod_prologue<A1_NS>(issue);
        bar();
        prod_loop<8, A1_NS>(A1_NT, issue);
      }
    } else {
      const int f0 = ft * 224, dd0 = dt1 * 128;
      if (pw < 2) {
        unsigned voff[7];
#pragma unroll
        for (int j = 0; j < 7; ++j) {
          const int ch = (pw * 7 + j) * 64 + lane, k = ch / 28, pos = ch - k * 28;
          const int c = pos ^ (3 * ((k >> 3) & 1));
          voff[j] = (unsigned)(k * F + f0 + c * 8) * 2u;
        }
        const rsrc_t rs = mk_rsrc(a.w1, S * F * 2);
        auto issue = [&](int t, int slot) {
          LAS char* l = lds + slot * B1_STAGE + pw * 7168;
#pragma unroll
          for (int j = 0; j < 7; ++j) dma16(rs, voff[j], t * (32 * F * 2), l + j * 1024);
        };
        prod_prologue<B1_NS>(issue);
        bar();
        prod_loop<7, B1_NS>(B1_NT, issue);
      } else {
        unsigned voff[4];
#pragma unroll
        for (int j = 0; j < 4; ++j) {
          const int k = ((pw - 2) * 4 + j) * 4 + (lane >> 4), pos = lane & 15;
          const int c = pos ^ (((k & 3) << 1) | (((k >> 3) & 1) << 3));
          voff[j] = (unsigned)(k * D + dd0 + c * 8) * 2u;
        }
        const rsrc_t rs = mk_rsrc(a.ws + WS_HL, S * D * 2);
        auto issue = [&](int t, int slot) {
          LAS char* l = lds + slot * B1_STAGE + B1_ABYTES + (pw - 2) * 4096;
#pragma unroll
          for (int j = 0; j < 4; ++j) dma16c(rs, voff[j], t * (32 * D * 2), l + j * 1024);
        };
        bar();
        wait_flags<4>(sy + SY_LN + lane, true, tag, 16);
        if (bid == 255 && pw == 3 && lane == 0) __hip_atomic_store(sy, tag, RLX_AGENT);
        prod_prologue<B1_NS>(issue);
        prod_loop<4, B1_NS>(B1_NT, issue);
      }
    }
    bar();
    if (pw < 2) {
      const unsigned r0 = (unsigned)(d0 + 64 * pw + (lane >> 3));
      const unsigned ce = (unsigned)(((lane & 7) ^ (lane >> 4)) * 16);
      const unsigned vce = r0 * (P * 2) + ce, vco = vce ^ 64u, vue = r0 * (F * 2) + ce, vuo = vue ^ 64u;
      const rsrc_t rcre = mk_rsrc(a.Cre, D * P * 2), rcim = mk_rsrc(a.Cim, D * P * 2), ru = mk_rsrc(a.ws + WS_U, D * F * 2);
      auto issue = [&](int t, int slot) {
        const int kt = kt0 + t;
        LAS char* l = lds + slot * P2_STAGE + pw * 8192;
        if (kt < 32) {
          const rsrc_t rs = kt < 16 ? rcre : rcim;
#pragma unroll
          for (int j = 0; j < 8; ++j) dma16(rs, (j & 1) ? vco : vce, (kt & 15) * 128 + j * 8 * (P * 2), l + j * 1024);
        } else {
          if (roleA && kt == 32) wait_flags<1>(sy + SY_U + dt * 16 + (lane & 15), true, tag, 18);
#pragma unroll
          for (int j = 0; j < 8; ++j) dma16c(ru, (j & 1) ? vuo : vue, (kt - 32) * 128 + j * 8 * (F * 2), l + j * 1024);
        }
      };
      if (roleA) {
        prod_prologue<P2_NS>(issue);
        bar();
        bar();
      } else {
        bar();
        wait_flags<1>(sy + SY_U + dt * 16 + (lane & 15), true, tag, 17);
        prod_prologue<P2_NS>(issue);
      }
      prod_loop<8, P2_NS>(nt2, issue);
    } else {
      unsigned voff[2];
#pragma unroll
      for (int j = 0; j < 2; ++j) {
        const int k = (pw - 2) * 32 + j * 8 + (lane >> 4), pos = lane & 15;
        const int c = pos ^ (((k & 3) << 1) | (((k >> 3) & 1) << 3));
        voff[j] = (unsigned)(((pw - 2) * 32 + (lane >> 4)) * S + t0 + c * 8) * 2u;
      }
      const rsrc_t rh = mk_rsrc(a.ws + WS_HT, 2 * P * S * 2), rw = mk_rsrc(a.w2, F * S * 2);
      auto issue = [&](int t, int slot) {
        const int kt = kt0 + t;
        const rsrc_t rs = kt < 32 ? rh : rw;
        const unsigned so = (unsigned)(kt < 32 ? kt : kt - 32) * (64 * S * 2);
        LAS char* l = lds + slot * P2_STAGE + 16384 + (pw - 2) * 8192;
#pragma unroll
        for (int j = 0; j < 8; ++j) dma16c(rs, voff[(j >> 1) & 1], so + j * 4 * (S * 2), l + j * 1024);
      };
      if (roleA) {
        bar();
        bar();
        wait_flags<1>(sy + SY_H + (tt >> 1) * 16 + (lane & 15), true, tag, 17);
        prod_prologue<P2_NS>(issue);
      } else {
        prod_prologue<P2_NS>(issue);
        bar();
      }
      prod_loop<8, P2_NS>(nt2, issue);
    }
    return;
  }

  const int wm = wid >> 1, wn = wid & 1, cl = lane & 15, g = lane >> 4, q4 = cl >> 2, p4 = lane & 3;
  {
    const int row = bid * 8 + wid * 2;
    u32x4 r[2][2];
#pragma unroll
    for (int i = 0; i < 2; ++i) { r[i][0] = *(const u32x4*)(a.x + (size_t)(row + i) * D + lane * 8); r[i][1] = *(const u32x4*)(a.x + (size_t)(row + i) * D + 512 + lane * 8); }
    if (roleA && wid == 0) {
      const rsrc_t rg = mk_rsrc(a.ws + WS_SYNC + SY_GR * 8, 8 * P * 16);
      st16_wt(rg, (unsigned)lane * 16u, (unsigned)(chunk * P + cb * 64) * 16u, (u32x4){0u, 0u, 0u, 0u});
    }
    const rsrc_t rx = mk_rsrc(a.ws + WS_XN, 2 * S * D * 2);
    float v[2][16];
#pragma unroll
    for (int i = 0; i < 2; ++i)
#pragma unroll
      for (int k = 0; k < 4; ++k) { v[i][2 * k] = bflo(r[i][0][k]); v[i][2 * k + 1] = bfhi(r[i][0][k]); v[i][8 + 2 * k] = bflo(r[i][1][k]); v[i][8 + 2 * k + 1] = bfhi(r[i][1][k]); }
#pragma unroll
    for (int pass = 0; pass < 2; ++pass) {
      float mean[2], var[2];
#pragma unroll
      for (int i = 0; i < 2; ++i) {
        float s = 0.f;
#pragma unroll
        for (int k = 0; k < 16; ++k) s += v[i][k];
        mean[i] = wave_sum(s) * (1.0f / D);
      }
#pragma unroll
      for (int i = 0; i < 2; ++i) {
        float qq = 0.f;
#pragma unroll
        for (int k = 0; k < 16; ++k) { const float d = v[i][k] - mean[i]; qq = fmaf(d, d, qq); }
        var[i] = wave_sum(qq) * (1.0f / D);
      }
#pragma unroll
      for (int i = 0; i < 2; ++i) {
        const float mb = rbf(mean[i]), rs_ = 1.0f / rbf(sqrtf(rbf(rbf(var[i]) + 1e-5f)));
        u32x4 o0, o1;
#pragma unroll
        for (int k = 0; k < 8; ++k) {
          const unsigned dd = pk_bf16(v[i][2 * k] - mb, v[i][2 * k + 1] - mb);
          const unsigned w = pk_bf16(bflo(dd) * rs_, bfhi(dd) * rs_);
          v[i][2 * k] = bflo(w); v[i][2 * k + 1] = bfhi(w);
          if (k < 4) o0[k] = w; else o1[k - 4] = w;
        }
        const unsigned so = (unsigned)(pass * S * D + (row + i) * D) * 2u;
        st16_wt(rx, (unsigned)lane * 16u, so, o0);
        st16_wt(rx, (unsigned)lane * 16u + 1024u, so, o1);
      }
    }
    asm volatile("" ::"s"(tag));
    STAMP(1);
    wait_vm<0>();
    bar();
    if (wid == 0 && lane == 0) __hip_atomic_store(sy + SY_LN + bid, tag, RLX_AGENT);
    STAMP(2);
  }

  if (roleA) {
    const int T0 = chunk * 256, p0 = cb * 64;
    const unsigned rb = (unsigned)(cl * 128 + ((g ^ (cl >> 1)) * 16));
    const unsigned abase = rb + wm * 16384, bbase = rb + 32768 + wn * 8192;
    f32x4 acc[8][4];
#pragma unroll
    for (int m = 0; m < 8; ++m)
#pragma unroll
      for (int n = 0; n < 4; ++n) acc[m][n] = (f32x4){0.f, 0.f, 0.f, 0.f};
    bf16x8 xa[8], xb[4], ya[8], yb[4];
#define A1_READ(fa, fb, so, ks) do { LAS char* pa_ = lds + (so) + (abase ^ ((ks) * 64)); LAS char* pb_ = lds + (so) + (bbase ^ ((ks) * 64)); \
    fb[0] = lds_rd128(pb_); fa[0] = lds_rd128(pa_); \
    _Pragma("unroll") for (int n = 1; n < 4; ++n) fb[n] = lds_rd128(pb_ + n * 2048); \
    _Pragma("unroll") for (int m = 1; m < 8; ++m) fa[m] = lds_rd128(pa_ + m * 2048); } while (0)
#define A1_MMA(fa, fb) do { _Pragma("unroll") for (int m = 0; m < 8; ++m) _Pragma("unroll") for (int n = 0; n < 4; ++n) acc[m][n] = MFMA(fa[m], fb[n], acc[m][n]); } while (0)
    int so = 0;
    bar();
    STAMP(3);
    A1_READ(xa, xb, so, 0);
#pragma unroll 1
    for (int t = 0; t < A1_NT - 1; ++t) {
      SCHED_FENCE();
      A1_READ(ya, yb, so, 1);
      A1_MMA(xa, xb);
      sched_phase<12, 32>();
      SCHED_FENCE();
      wait_lgkm0();
      bar();
      SCHED_FENCE();
      so = (so + A1_STAGE == A1_NS * A1_STAGE) ? 0 : so + A1_STAGE;
      A1_READ(xa, xb, so, 0);
      A1_MMA(ya, yb);
      sched_phase<12, 32>();
    }
    SCHED_FENCE();
    A1_READ(ya, yb, so, 1);
    A1_MMA(xa, xb);
    sched_phase<12, 32>();
    SCHED_FENCE();
    wait_lgkm0();
    bar();
    SCHED_FENCE();
    A1_MMA(ya, yb);
#undef A1_READ
#undef A1_MMA
    STAMP(4);
    float ar[2], ai[2], cr[2], ci[2], l32r[2], l32i[2], l64r[2], l64i[2], l128r[2], l128i[2], l256r[2], l256i[2];
#pragma unroll
    for (int q = 0; q < 2; ++q) {
      const int st = p0 + 32 * wn + 16 * q + cl;
      const float lr = bf2f(a.Lre[st]), li = bf2f(a.Lim[st]), dtt = rbf(expf(bf2f(a.logdt[st])));
      const float er = expf(dtt * lr);
      float sn, cs;
      sincosf(dtt * li, &sn, &cs);
      ar[q] = er * cs; ai[q] = er * sn;
      const float nr = ar[q] - 1.0f, ni = ai[q], den = lr * lr + li * li;
      cr[q] = 2.0f * (nr * lr + ni * li) / den; ci[q] = 2.0f * (ni * lr - nr * li) / den;
      float pr = ar[q], pi = ai[q];
#pragma unroll
      for (int k = 0; k < 5; ++k) { const float t = pr * pr - pi * pi; pi = 2.0f * pr * pi; pr = t; }
      l32r[q] = pr; l32i[q] = pi;
      l64r[q] = pr * pr - pi * pi; l64i[q] = 2.0f * pr * pi;
      l128r[q] = l64r[q] * l64r[q] - l64i[q] * l64i[q]; l128i[q] = 2.0f * l64r[q] * l64i[q];
      l256r[q] = l128r[q] * l128r[q] - l128i[q] * l128i[q]; l256i[q] = 2.0f * l128r[q] * l128i[q];
    }
    float er[2] = {0.f, 0.f}, ei[2] = {0.f, 0.f};
#pragma unroll
    for (int m = 0; m < 8; ++m)
#pragma unroll
      for (int j = 0; j < 4; ++j)
#pragma unroll
        for (int q = 0; q < 2; ++q) {
          const float nr = fmaf(ar[q], er[q], fmaf(-ai[q], ei[q], acc[m][2 * q][j]));
          const float ni = fmaf(ar[q], ei[q], fmaf(ai[q], er[q], acc[m][2 * q + 1][j]));
          er[q] = nr; ei[q] = ni;
        }
    float pr_[2], pi_[2];
    LAS float* tot = (LAS float*)(lds + LDS_BYTES - 512);
#pragma unroll
    for (int q = 0; q < 2; ++q) {
      float vr = __shfl_up(er[q], 16), vi = __shfl_up(ei[q], 16);
      if (g >= 1) { const float t = er[q] + (l32r[q] * vr - l32i[q] * vi); ei[q] = ei[q] + (l32r[q] * vi + l32i[q] * vr); er[q] = t; }
      vr = __shfl_up(er[q], 32); vi = __shfl_up(ei[q], 32);
      if (g >= 2) { const float t = er[q] + (l64r[q] * vr - l64i[q] * vi); ei[q] = ei[q] + (l64r[q] * vi + l64i[q] * vr); er[q] = t; }
      vr = __shfl_up(er[q], 16); vi = __shfl_up(ei[q], 16);
      pr_[q] = g >= 1 ? vr : 0.f; pi_[q] = g >= 1 ? vi : 0.f;
      if (wm == 0 && g == 3) { tot[(32 * wn + 16 * q + cl) * 2] = er[q]; tot[(32 * wn + 16 * q + cl) * 2 + 1] = ei[q]; }
    }
    wait_lgkm0();
    bar();
    float t0r[2], t0i[2];
    gu64* gran = sy + SY_GR;
#pragma unroll
    for (int q = 0; q < 2; ++q) {
      t0r[q] = tot[(32 * wn + 16 * q + cl) * 2]; t0i[q] = tot[(32 * wn + 16 * q + cl) * 2 + 1];
      if (wm == 1 && g == 3 && chunk < 7) {
        const float xr = er[q] + (l128r[q] * t0r[q] - l128i[q] * t0i[q]), xi = ei[q] + (l128r[q] * t0i[q] + l128i[q] * t0r[q]);
        gu64* gp = gran + ((size_t)chunk * P + p0 + 32 * wn + 16 * q + cl) * 2;
        __hip_atomic_store(gp, (1ull << 32) | __float_as_uint(xr), RLX_AGENT);
        __hip_atomic_store(gp + 1, (1ull << 32) | __float_as_uint(xi), RLX_AGENT);
      }
    }
    STAMP(5);
    float hr[2] = {0.f, 0.f}, hi[2] = {0.f, 0.f};
    if (chunk > 0) {
      float gr[2][2], gi[2][2];
#pragma unroll 1
      for (unsigned spins = 0;; ++spins) {
        bool ok = true;
#pragma unroll
        for (int q = 0; q < 2; ++q)
#pragma unroll
          for (int k = 0; k < 2; ++k) {
            const int cc = g + 4 * k;
            gr[q][k] = 0.f; gi[q][k] = 0.f;
            if (cc < chunk) {
              gu64* gp = gran + ((size_t)cc * P + p0 + 32 * wn + 16 * q + cl) * 2;
              const u64 vr = __hip_atomic_load(gp, RLX_AGENT), vi = __hip_atomic_load(gp + 1, RLX_AGENT);
              ok &= ((vr >> 32) == 1ull) & ((vi >> 32) == 1ull);
              gr[q][k] = __uint_as_float((unsigned)vr); gi[q][k] = __uint_as_float((unsigned)vi);
            }
          }
        if (__all(ok) || spins > SPIN_CAP) break;
        __builtin_amdgcn_s_sleep(1);
      }
#pragma unroll
      for (int q = 0; q < 2; ++q) {
        float sr = 0.f, si = 0.f;
#pragma unroll
        for (int k = 0; k < 2; ++k) {
          float vr = gr[q][k], vi = gi[q][k];
          const int e = chunk - 1 - (g + 4 * k);
#pragma unroll
          for (int i = 0; i < 6; ++i)
            if (i < e) { const float t = l256r[q] * vr - l256i[q] * vi; vi = l256r[q] * vi + l256i[q] * vr; vr = t; }
          sr += vr; si += vi;
        }
        sr += __shfl_xor(sr, 16); si += __shfl_xor(si, 16);
        sr += __shfl_xor(sr, 32); si += __shfl_xor(si, 32);
        hr[q] = sr; hi[q] = si;
      }
    }
    STAMP(6);
    const rsrc_t rh = mk_rsrc(a.ws + WS_HT, 2 * P * S * 2);
    LAS char* scr = lds + LDS_SCR + wid * 7936;
#pragma unroll
    for (int q = 0; q < 2; ++q) {
      if (wm == 1) { const float t = t0r[q] + (l128r[q] * hr[q] - l128i[q] * hi[q]); hi[q] = t0i[q] + (l128r[q] * hi[q] + l128i[q] * hr[q]); hr[q] = t; }
      if (g & 1) { const float t = l32r[q] * hr[q] - l32i[q] * hi[q]; hi[q] = l32r[q] * hi[q] + l32i[q] * hr[q]; hr[q] = t; }
      if (g & 2) { const float t = l64r[q] * hr[q] - l64i[q] * hi[q]; hi[q] = l64r[q] * hi[q] + l64i[q] * hr[q]; hr[q] = t; }
      hr[q] += pr_[q]; hi[q] += pi_[q];
    }
    (void)scr;
#pragma unroll
    for (int mm = 0; mm < 4; ++mm) {
      u32x4 ore[2], oim[2];
#pragma unroll
      for (int h2 = 0; h2 < 2; ++h2) {
        float vr[2][4], vi[2][4];
#pragma unroll
        for (int j = 0; j < 4; ++j)
#pragma unroll
          for (int q = 0; q < 2; ++q) {
            const int m = 2 * mm + h2;
            const float nr = fmaf(ar[q], hr[q], fmaf(-ai[q], hi[q], acc[m][2 * q][j]));
            const float ni = fmaf(ar[q], hi[q], fmaf(ai[q], hr[q], acc[m][2 * q + 1][j]));
            hr[q] = nr; hi[q] = ni;
            vr[q][j] = cr[q] * nr - ci[q] * ni;
            vi[q][j] = -(cr[q] * ni + ci[q] * nr);
          }
#pragma unroll
        for (int q = 0; q < 2; ++q) {
          ore[q][2 * h2] = pk_bf16(vr[q][0], vr[q][1]); ore[q][2 * h2 + 1] = pk_bf16(vr[q][2], vr[q][3]);
          oim[q][2 * h2] = pk_bf16(vi[q][0], vi[q][1]); oim[q][2 * h2 + 1] = pk_bf16(vi[q][2], vi[q][3]);
        }
      }
#pragma unroll
      for (int q = 0; q < 2; ++q) {
        const unsigned vo = (unsigned)((p0 + 32 * wn + 16 * q + cl) * S + T0 + 128 * wm + 32 * g + 8 * mm) * 2u;
        st16_wt(rh, vo, 0, ore[q]);
        st16_wt(rh, vo, P * S * 2, oim[q]);
      }
    }
    STAMP(7);
    wait_vm<0>();
    bar();
    if (wid == 0 && lane == 0) __hip_atomic_store(sy + SY_H + chunk * 16 + cb, tag, RLX_AGENT);
    STAMP(8);
  } else {
    const int f0 = ft * 224, dd0 = dt1 * 128;
    const int k0 = 8 * g + q4;
    const unsigned a0 = (unsigned)(k0 * 448 + ((14 * wm + p4) ^ (3 * (g & 1))) * 16 + (p4 & 1) * 8);
    const unsigned a1 = a0 ^ 8u;
    const unsigned a2 = (unsigned)(k0 * 448 + ((14 * wm + 12 + (p4 >> 1)) ^ (3 * (g & 1))) * 16 + (p4 & 1) * 8);
    unsigned bb[4];
#pragma unroll
    for (int n = 0; n < 4; ++n) bb[n] = (unsigned)(B1_ABYTES + k0 * 256 + ((8 * wn + 2 * n + (p4 >> 1)) ^ ((q4 << 1) | ((g & 1) << 3))) * 16 + (p4 & 1) * 8);
    f32x4 acc[7][4];
#pragma unroll
    for (int m = 0; m < 7; ++m)
#pragma unroll
      for (int n = 0; n < 4; ++n) acc[m][n] = (f32x4){0.f, 0.f, 0.f, 0.f};
    bf16x8 xa[7], xb[4], ya[7], yb[4];
#define B1_READ(fa, fb, so) do { LAS char* s_ = lds + (so); \
    fb[0] = lds_rdtr(s_ + bb[0], s_ + bb[0] + 1024); fa[0] = lds_rdtr(s_ + a0, s_ + a0 + 1792); \
    _Pragma("unroll") for (int n = 1; n < 4; ++n) fb[n] = lds_rdtr(s_ + bb[n], s_ + bb[n] + 1024); \
    _Pragma("unroll") for (int m = 1; m < 6; ++m) { const unsigned o_ = ((m & 1) ? a1 : a0) + (m >> 1) * 64; fa[m] = lds_rdtr(s_ + o_, s_ + o_ + 1792); } \
    fa[6] = lds_rdtr(s_ + a2, s_ + a2 + 1792); } while (0)
#define B1_MMA(fa, fb) do { _Pragma("unroll") for (int m = 0; m < 7; ++m) _Pragma("unroll") for (int n = 0; n < 4; ++n) acc[m][n] = MFMA(fa[m], fb[n], acc[m][n]); } while (0)
#define B1_NEXT() so = (so + B1_STAGE == B1_NS * B1_STAGE) ? 0 : so + B1_STAGE
    int so = 0;
    bar();
    STAMP(3);
    B1_READ(xa, xb, so);
#pragma unroll 1
    for (int t = 0; t < B1_NT - 2; t += 2) {
      SCHED_FENCE(); wait_lgkm0(); bar(); SCHED_FENCE();
      B1_NEXT();
      B1_READ(ya, yb, so);
      B1_MMA(xa, xb);
      sched_phase<22, 28>();
      SCHED_FENCE(); wait_lgkm0(); bar(); SCHED_FENCE();
      B1_NEXT();
      B1_READ(xa, xb, so);
      B1_MMA(ya, yb);
      sched_phase<22, 28>();
    }
    SCHED_FENCE(); wait_lgkm0(); bar(); SCHED_FENCE();
    B1_NEXT();
    B1_READ(ya, yb, so);
    B1_MMA(xa, xb);
    sched_phase<22, 28>();
    SCHED_FENCE(); wait_lgkm0(); bar(); SCHED_FENCE();
    B1_MMA(ya, yb);
#undef B1_READ
#undef B1_MMA
#undef B1_NEXT
    STAMP(4);
    const int fw = f0 + 112 * wm;
    u32x2 bias[7];
#pragma unroll
    for (int m = 0; m < 6; ++m) bias[m] = *(const u32x2*)(a.b1 + fw + 32 * (m >> 1) + 8 * g + 4 * ((m & 1) ^ (g & 1)));
    bias[6] = *(const u32x2*)(a.b1 + fw + 96 + 4 * g);
    const rsrc_t ru = mk_rsrc(a.ws + WS_U, D * F * 2);
#pragma unroll
    for (int n = 0; n < 4; ++n) {
      const unsigned vo = (unsigned)((dd0 + 64 * wn + 16 * n + cl) * F + fw) * 2u;
      u32x2 w[7];
#pragma unroll
      for (int m = 0; m < 7; ++m) {
        const float v0 = gelu_tanh(acc[m][n][0] + bflo(bias[m][0])), v1 = gelu_tanh(acc[m][n][1] + bfhi(bias[m][0]));
        const float v2 = gelu_tanh(acc[m][n][2] + bflo(bias[m][1])), v3 = gelu_tanh(acc[m][n][3] + bfhi(bias[m][1]));
        w[m][0] = pk_bf16(v0, v1); w[m][1] = pk_bf16(v2, v3);
      }
#pragma unroll
      for (int mm = 0; mm < 3; ++mm) {
        const bool sw = g & 1;
        const u32x2 lo = sw ? w[2 * mm + 1] : w[2 * mm], hi = sw ? w[2 * mm] : w[2 * mm + 1];
        st16_wt(ru, vo + (32 * mm + 8 * g) * 2, 0, (u32x4){lo[0], lo[1], hi[0], hi[1]});
      }
      st8_wt(ru, vo + (96 + 4 * g) * 2, 0, w[6]);
    }
    STAMP(7);
    wait_vm<0>();
    bar();
    if (wid == 0 && lane == 0) __hip_atomic_store(sy + SY_U + dt1 * 16 + ft, tag, RLX_AGENT);
    STAMP(8);
  }

  const unsigned abase = (unsigned)((wm * 64 + cl) * 128 + ((g ^ (cl >> 1)) * 16));
  unsigned bb[4];
#pragma unroll
  for (int n = 0; n < 4; ++n) bb[n] = (unsigned)(16384 + (8 * g + q4) * 256 + ((8 * wn + 2 * n + (p4 >> 1)) ^ ((q4 << 1) | ((g & 1) << 3))) * 16 + (p4 & 1) * 8);
  f32x4 acc[4][4];
#pragma unroll
  for (int m = 0; m < 4; ++m)
#pragma unroll
    for (int n = 0; n < 4; ++n) acc[m][n] = (f32x4){0.f, 0.f, 0.f, 0.f};
  bf16x8 xa[4], xb[4], ya[4], yb[4];
#define K2_READ(fa, fb, so, ks) do { LAS char* pa_ = lds + (so) + (abase ^ ((ks) * 64)); LAS char* s_ = lds + (so) + (ks) * 8192; \
    fa[0] = lds_rd128(pa_); fb[0] = lds_rdtr(s_ + bb[0], s_ + bb[0] + 1024); \
    _Pragma("unroll") for (int n = 1; n < 4; ++n) fb[n] = lds_rdtr(s_ + bb[n], s_ + bb[n] + 1024); \
    _Pragma("unroll") for (int m = 1; m < 4; ++m) fa[m] = lds_rd128(pa_ + m * 2048); } while (0)
#define K2_MMA(fa, fb) do { _Pragma("unroll") for (int m = 0; m < 4; ++m) _Pragma("unroll") for (int n = 0; n < 4; ++n) acc[m][n] = MFMA(fa[m], fb[n], acc[m][n]); } while (0)
  int so = 0;
  bar();
  STAMP(9);
  K2_READ(xa, xb, so, 0);
#pragma unroll 1
  for (int t = 0; t < nt2 - 1; ++t) {
    SCHED_FENCE();
    K2_READ(ya, yb, so, 1);
    K2_MMA(xa, xb);
    sched_phase<12, 16>();
    SCHED_FENCE();
    wait_lgkm0();
    bar();
    SCHED_FENCE();
    so = (so + P2_STAGE) & (P2_NS * P2_STAGE - 1);
    K2_READ(xa, xb, so, 0);
    K2_MMA(ya, yb);
    sched_phase<12, 16>();
  }
  SCHED_FENCE();
  K2_READ(ya, yb, so, 1);
  K2_MMA(xa, xb);
  sched_phase<12, 16>();
  SCHED_FENCE();
  K2_MMA(ya, yb);
#undef K2_READ
#undef K2_MMA
  STAMP(10);
  const int kh = roleA ? 0 : 1, pa = bid ^ 1;
  const rsrc_t rxb = mk_rsrc(a.ws + WS_XB, 256 * 32768);
  const unsigned xo = (unsigned)(wid * 8192 + lane * 16);
  f32x4 keep[2][4];
#pragma unroll
  for (int mi = 0; mi < 2; ++mi)
#pragma unroll
    for (int n = 0; n < 4; ++n) {
      const f32x4 snd = kh ? acc[mi][n] : acc[2 + mi][n];
      keep[mi][n] = kh ? acc[2 + mi][n] : acc[mi][n];
      st16_wt(rxb, xo + (mi * 4 + n) * 1024, bid * 32768, __builtin_bit_cast(u32x4, snd));
    }
  wait_vm<0>();
  bar();
  if (wid == 0 && lane == 0) __hip_atomic_store(sy + SY_X + bid, tag, RLX_AGENT);
  STAMP(11);
  {
    const rsrc_t rxn = mk_rsrc(a.ws + WS_XN, S * D * 2);
#pragma unroll
    for (int mi = 0; mi < 2; ++mi) {
      const int d = d0 + 64 * wm + 16 * (2 * kh + mi) + 4 * g;
      const u32x2 dk = *(const u32x2*)(a.Dsk + d);
#pragma unroll
      for (int n = 0; n < 4; ++n) {
        const int tok = t0 + 64 * wn + 16 * n + cl;
        const u32x2 xv = *(const u32x2*)(a.x + (size_t)tok * D + d);
        const u32x2 nv = __builtin_amdgcn_raw_buffer_load_b64(rxn, (unsigned)(tok * D + d) * 2u, 0, 16);
        const float b2 = bf2f(a.b2[tok]);
        const unsigned s0 = pk_bf16(bflo(nv[0]) * bflo(dk[0]), bfhi(nv[0]) * bfhi(dk[0])), s1 = pk_bf16(bflo(nv[1]) * bflo(dk[1]), bfhi(nv[1]) * bfhi(dk[1]));
        keep[mi][n] += (f32x4){bflo(xv[0]) + bflo(s0) + b2, bfhi(xv[0]) + bfhi(s0) + b2, bflo(xv[1]) + bflo(s1) + b2, bfhi(xv[1]) + bfhi(s1) + b2};
      }
    }
  }
  if (wid == 0) {
#pragma unroll 1
    for (unsigned spins = 0; spins < SPIN_CAP; ++spins) {
      if (__all(__hip_atomic_load(sy + SY_X + pa, RLX_AGENT) == tag)) break;
      __builtin_amdgcn_s_sleep(1);
    }
  }
  STAMP(12);
  bar();
#pragma unroll
  for (int mi = 0; mi < 2; ++mi)
#pragma unroll
    for (int n = 0; n < 4; ++n) {
      const u32x4 r = __builtin_amdgcn_raw_buffer_load_b128(rxb, xo + (mi * 4 + n) * 1024, pa * 32768, 16);
      keep[mi][n] += __builtin_bit_cast(f32x4, r);
    }
#pragma unroll
  for (int mi = 0; mi < 2; ++mi)
#pragma unroll
    for (int n = 0; n < 4; ++n) {
      const int d = d0 + 64 * wm + 16 * (2 * kh + mi) + 4 * g, tok = t0 + 64 * wn + 16 * n + cl;
      *(f32x4*)(a.out + (size_t)tok * D + d) = keep[mi][n];
    }
  STAMP(13);
}

extern "C" void kernel_launch(void* const* d_in, const int* in_sizes, int n_in, void* d_out, int out_size, void* d_ws, size_t ws_size,
                              hipStream_t stream) {
  (void)hipFuncSetAttribute((const void*)fused, hipFuncAttributeMaxDynamicSharedMemorySize, LDS_BYTES);
  Args a;
  a.x = (const u16*)d_in[0]; a.Lre = (const u16*)d_in[1]; a.Lim = (const u16*)d_in[2]; a.Bre = (const u16*)d_in[3]; a.Bim = (const u16*)d_in[4];
  a.Cre = (const u16*)d_in[5]; a.Cim = (const u16*)d_in[6]; a.logdt = (const u16*)d_in[7]; a.Dsk = (const u16*)d_in[8];
  a.w1 = (const u16*)d_in[10]; a.b1 = (const u16*)d_in[11]; a.w2 = (const u16*)d_in[12]; a.b2 = (const u16*)d_in[13];
  a.out = (float*)d_out; a.ws = (char*)d_ws;
  fused<<<256, 512, LDS_BYTES, stream>>>(a);
}
```

```cpp
#include <hip/hip_runtime.h>
#include <stdint.h>

typedef unsigned short u16;
typedef unsigned long long u64;
typedef __bf16 bf16x8 __attribute__((ext_vector_type(8)));
typedef short s16x4 __attribute__((ext_vector_type(4)));
typedef short s16x8 __attribute__((ext_vector_type(8)));
typedef float f32x2 __attribute__((ext_vector_type(2)));
typedef float f32x4 __attribute__((ext_vector_type(4)));
typedef unsigned u32x4 __attribute__((ext_vector_type(4)));
typedef unsigned u32x2 __attribute__((ext_vector_type(2)));
#define LAS __attribute__((address_space(3)))
#define GAS __attribute__((address_space(1)))
typedef GAS unsigned long long gu64;
typedef __amdgpu_buffer_rsrc_t rsrc_t;
#define RLX_AGENT __ATOMIC_RELAXED, __HIP_MEMORY_SCOPE_AGENT

constexpr int S = 2048, D = 1024, P = 1024, F = 3584;
constexpr size_t MB = 1024 * 1024;
constexpr size_t WS_XN = 0, WS_HL = 4 * MB, WS_HT = 8 * MB, WS_U = 16 * MB, WS_XB = 24 * MB, WS_SYNC = 32 * MB;
constexpr int SY_LN = 64, SY_H = 320, SY_U = 448, SY_X = 576, SY_GR = 1024;
constexpr unsigned SPIN_CAP = 1u << 17;
#define NBA 22
#define PF_DELAY 40
#define PF_GAP 20
constexpr unsigned CODE_PF_BYTES = 32 * 1024;
#define PSTAMP(w, i)
#define STAMP(i) PSTAMP(0, i)

__device__ __forceinline__ float bf2f(u16 v) { return __uint_as_float(((unsigned)v) << 16); }
__device__ __forceinline__ float bflo(unsigned v) { return __uint_as_float(v << 16); }
__device__ __forceinline__ float bfhi(unsigned v) { return __uint_as_float(v & 0xffff0000u); }
__device__ __forceinline__ unsigned pk_bf16(float lo, float hi) {
  typedef __bf16 bf16x2 __attribute__((ext_vector_type(2)));
  f32x2 v = {lo, hi};
  bf16x2 r = __builtin_convertvector(v, bf16x2);
  return __builtin_bit_cast(unsigned, r);
}
__device__ __forceinline__ float rbf(float f) { return bflo(pk_bf16(f, f)); }
__device__ __forceinline__ f32x2 fma2(f32x2 a, f32x2 b, f32x2 c) { return __builtin_elementwise_fma(a, b, c); }
__device__ __forceinline__ f32x2 bc2(float v) { return (f32x2){v, v}; }

__device__ __forceinline__ rsrc_t mk_rsrc(const void* p, unsigned bytes) { return __builtin_amdgcn_make_buffer_rsrc((void*)p, 0, bytes, 0x00020000); }
__device__ __forceinline__ void dma16(rsrc_t rs, unsigned voff, unsigned soff, LAS char* l) { __builtin_amdgcn_raw_ptr_buffer_load_lds(rs, (LAS void*)l, 16, voff, soff, 0, 0); }
__device__ __forceinline__ void dma16c(rsrc_t rs, unsigned voff, unsigned soff, LAS char* l) { __builtin_amdgcn_raw_ptr_buffer_load_lds(rs, (LAS void*)l, 16, voff, soff, 0, 16); }
__device__ __forceinline__ void st16_wt(rsrc_t rs, unsigned voff, unsigned soff, u32x4 v) { __builtin_amdgcn_raw_buffer_store_b128(v, rs, voff + soff, 0, 16); }
__device__ __forceinline__ void st8_wt(rsrc_t rs, unsigned voff, unsigned soff, u32x2 v) { __builtin_amdgcn_raw_buffer_store_b64(v, rs, voff + soff, 0, 16); }
template <int N> __device__ __forceinline__ void wait_vm() { asm volatile("s_waitcnt vmcnt(%0)" ::"n"(N) : "memory"); }
__device__ __forceinline__ void wait_lgkm0() { asm volatile("" ::: "memory"); __builtin_amdgcn_s_waitcnt(0xC07F); asm volatile("" ::: "memory"); }
__device__ __forceinline__ void bar() {
  asm volatile("" ::: "memory");
  __builtin_amdgcn_s_barrier();
  asm volatile("" ::: "memory");
}
#define SCHED_FENCE() __builtin_amdgcn_sched_barrier(0)
__device__ __forceinline__ bf16x8 lds_rd128(LAS char* p) { return *(LAS bf16x8*)p; }
__device__ __forceinline__ bf16x8 lds_rdtr(LAS char* p0, LAS char* p1) {
  s16x4 a = __builtin_amdgcn_ds_read_tr16_b64_v4i16((LAS s16x4*)p0);
  s16x4 b = __builtin_amdgcn_ds_read_tr16_b64_v4i16((LAS s16x4*)p1);
  s16x8 ab = __builtin_shufflevector(a, b, 0, 1, 2, 3, 4, 5, 6, 7);
  return __builtin_bit_cast(bf16x8, ab);
}
#define MFMA(a, b, c) __builtin_amdgcn_mfma_f32_16x16x32_bf16((a), (b), (c), 0, 0, 0)

template <int NR, int NM> __device__ __forceinline__ void sched_phase() {
#pragma unroll
  for (int i = 0; i < NR; ++i) {
    __builtin_amdgcn_sched_group_barrier(0x008, 1, 0);
    __builtin_amdgcn_sched_group_barrier(0x100, 1, 0);
  }
  if (NM > NR) __builtin_amdgcn_sched_group_barrier(0x008, NM - NR, 0);
}

template <int NL> __device__ __forceinline__ void wait_flags(gu64* p, bool active, u64 tag) {
#pragma unroll 1
  for (unsigned spins = 0; spins < SPIN_CAP; ++spins) {
    bool ok = true;
    if (active) {
#pragma unroll
      for (int k = 0; k < NL; ++k) ok &= __hip_atomic_load(p + 64 * k, RLX_AGENT) == tag;
    }
    if (__all(ok)) break;
    __builtin_amdgcn_s_sleep(2);
  }
  __builtin_amdgcn_fence(__ATOMIC_ACQUIRE, "wavefront");
}

template <int NS, class Issue> __device__ __forceinline__ void prod_prologue(Issue issue) {
#pragma unroll 1
  for (int t = 0; t < NS - 1; ++t) issue(t, t);
}
template <int L, int NS, class Issue> __device__ __forceinline__ void prod_loop(int nt, Issue issue) {
  int slot = NS - 1;
#pragma unroll 1
  for (int t = 0; t < nt; ++t) {
    const int rem = nt - 1 - t;
    if (rem >= NS - 2) wait_vm<(NS - 2) * L>();
    else if (NS > 3 && rem == 1) wait_vm<L>();
    else if (NS > 4 && rem == 2) wait_vm<2 * L>();
    else if (NS > 5 && rem == 3) wait_vm<3 * L>();
    else if (NS > 6 && rem == 4) wait_vm<4 * L>();
    else wait_vm<0>();
    bar();
    if (t + NS - 1 < nt) { issue(t + NS - 1, slot); slot = (slot + 1 == NS) ? 0 : slot + 1; }
  }
}

__device__ __forceinline__ float wave_sum(float v) {
  v += __builtin_bit_cast(float, __builtin_amdgcn_update_dpp(0, __builtin_bit_cast(int, v), 0xB1, 0xf, 0xf, true));
  v += __builtin_bit_cast(float, __builtin_amdgcn_update_dpp(0, __builtin_bit_cast(int, v), 0x4E, 0xf, 0xf, true));
  v += __builtin_bit_cast(float, __builtin_amdgcn_update_dpp(0, __builtin_bit_cast(int, v), 0x141, 0xf, 0xf, true));
  v += __builtin_bit_cast(float, __builtin_amdgcn_update_dpp(0, __builtin_bit_cast(int, v), 0x140, 0xf, 0xf, true));
  const int iv = __builtin_bit_cast(int, v);
  const float a = __builtin_bit_cast(float, __builtin_amdgcn_readlane(iv, 0)), b = __builtin_bit_cast(float, __builtin_amdgcn_readlane(iv, 16));
  const float c = __builtin_bit_cast(float, __builtin_amdgcn_readlane(iv, 32)), d = __builtin_bit_cast(float, __builtin_amdgcn_readlane(iv, 48));
  return (a + b) + (c + d);
}

struct Args {
  const u16 *x, *Lre, *Lim, *Bre, *Bim, *Cre, *Cim, *logdt, *Dsk, *w1, *b1, *w2, *b2;
  float* out;
  char* ws;
};

constexpr int A1_NS = 3, A1_STAGE = 49152, A1_NT = D / 64;
constexpr int B1_NS = 7, B1_ABYTES = 32 * 448, B1_STAGE = B1_ABYTES + 32 * 256, B1_NT = S / 32;
constexpr int P2_NS = 4, P2_STAGE = 32768;
constexpr int LDS_BYTES = 160 * 1024;
constexpr int LDS_COEF = A1_NS * A1_STAGE;
constexpr int LDS_TOT = LDS_COEF + 14 * 256;
constexpr int LDS_DUMMY = LDS_BYTES - 256;
static_assert(LDS_TOT + 512 <= LDS_DUMMY && B1_NS * B1_STAGE <= LDS_DUMMY && P2_NS * P2_STAGE <= LDS_COEF, "lds");

__device__ __forceinline__ f32x2 gelu_tanh2(f32x2 x) {
  const f32x2 z = x * fma2(x * x, bc2(-0.10294323967f), bc2(-2.30220819813f));
  const f32x2 d = (f32x2){__builtin_amdgcn_exp2f(z.x), __builtin_amdgcn_exp2f(z.y)} + bc2(1.0f);
  return x * (f32x2){__builtin_amdgcn_rcpf(d.x), __builtin_amdgcn_rcpf(d.y)};
}
#define CMUL(or_, oi_, ar_, ai_, br_, bi_) do { const float t_ = (ar_) * (br_) - (ai_) * (bi_); oi_ = (ar_) * (bi_) + (ai_) * (br_); or_ = t_; } while (0)

__global__ __launch_bounds__(512, 2) void fused(Args a) {
  extern __shared__ __attribute__((aligned(16))) char smem[];
  LAS char* lds = (LAS char*)smem;
  u64 pc0;
  asm volatile("s_getpc_b64 %0" : "=s"(pc0));
  const int wid = __builtin_amdgcn_readfirstlane(threadIdx.x >> 6), lane = threadIdx.x & 63;
  const int bid = blockIdx.x, xc = bid & 7, idx = bid >> 3, xg = xc >> 1;
  const bool roleA = !(xc & 1);
  gu64* sy = (gu64*)(a.ws + WS_SYNC);
  const u64 tag = *(const __attribute__((address_space(4))) u64*)(a.ws + WS_SYNC) + 1ull;
  const int chunk = 4 * (xg & 1) + (idx & 3), cb = 8 * (xg >> 1) + (idx >> 2);
  const int ft = 4 * xg + (idx & 3), dt1 = idx >> 2;
  const int tt = 4 * xg + (idx & 3), dt = idx >> 2;
  const int t0 = tt * 128, d0 = dt * 128;
  const int kt0 = roleA ? 0 : 32 + NBA, nt2 = roleA ? 32 + NBA : 56 - NBA;
  STAMP(0);

  if (wid >= 4) {
    const int pw = wid - 4;
    if (pw == 3) {
      auto pf_piece = [&](int i) {
        const u16* base = i < 1 ? a.Bre : i < 2 ? a.Bim : i < 9 ? a.w1 : i < 10 ? a.Cre : i < 11 ? a.Cim : a.w2;
        const int seg = i < 2 ? 0 : i < 9 ? i - 2 : i < 11 ? 0 : i - 11;
        const rsrc_t rs = mk_rsrc(base, (i < 2 || i == 9 || i == 10) ? 2u << 20 : 14u << 20);
        __builtin_amdgcn_raw_ptr_buffer_load_lds(rs, (LAS void*)(lds + LDS_DUMMY), 4, (unsigned)(lane * 128), (unsigned)(seg << 21) + (unsigned)bid * 8192u, 0, 0);
      };
      {
        const rsrc_t rc = mk_rsrc((const void*)(pc0 & ~255ull), CODE_PF_BYTES);
        const int c0 = (idx & 3) * 8192;
        __builtin_amdgcn_raw_ptr_buffer_load_lds(rc, (LAS void*)(lds + LDS_DUMMY), 4, (unsigned)(lane * 128), (unsigned)c0, 0, 0);
      }
      float lr = 0.f, li = 0.f, ld = 0.f;
      if (roleA) { lr = bf2f(a.Lre[cb * 64 + lane]); li = bf2f(a.Lim[cb * 64 + lane]); ld = bf2f(a.logdt[cb * 64 + lane]); asm volatile("" : "+v"(lr), "+v"(li), "+v"(ld)); }
      else __builtin_amdgcn_s_sleep(PF_DELAY);
      pf_piece(0); pf_piece(1); pf_piece(2);
      asm volatile("" ::"s"(tag)); bar();
      if (roleA) {
        LAS float* cf = (LAS float*)(lds + LDS_COEF) + lane;
        const float dtt = rbf(expf(ld)), er = expf(dtt * lr);
        float sn, cs;
        sincosf(dtt * li, &sn, &cs);
        float pr = er * cs, pi = er * sn;
        const float nr = pr - 1.0f, den = lr * lr + li * li;
        cf[0] = pr; cf[64] = pi;
        cf[128] = 2.0f * (nr * lr + pi * li) / den; cf[192] = 2.0f * (pi * lr - nr * li) / den;
#pragma unroll
        for (int k = 0; k < 8; ++k) {
          const float t = pr * pr - pi * pi; pi = 2.0f * pr * pi; pr = t;
          if (k >= 3) { cf[(4 + 2 * (k - 3)) * 64] = pr; cf[(5 + 2 * (k - 3)) * 64] = pi; }
        }
        wait_lgkm0();
      }
      int i = 3;
#pragma unroll 1
      for (; i < 6; ++i) { pf_piece(i); __builtin_amdgcn_s_sleep(PF_GAP); }
      const int nt = roleA ? A1_NT : B1_NT, msk = roleA ? 0 : 1;
#pragma unroll 1
      for (int t = 0; t < nt; ++t) {
        if (t == 4) {
          wait_flags<4>(sy + SY_LN + lane, true, tag);
          if (bid == 255 && lane == 0) __hip_atomic_store(sy, tag, RLX_AGENT);
        }
        bar();
        if (i < 18 && (t & msk) == 0 && (t < 1 || t >= 4)) { pf_piece(i); ++i; }
      }
#pragma unroll 1
      for (; i < 18; ++i) pf_piece(i);
    } else if (roleA) {
      const int T0 = chunk * 256, p0 = cb * 64;
      if (pw < 2) {
        unsigned voff[16];
#pragma unroll
        for (int j = 0; j < 16; ++j) {
          const int rho = 8 * j + (lane >> 3), m = rho >> 4, i = rho & 15;
          const int tok = T0 + 128 * pw + 32 * (i >> 2) + 16 * (i & 1) + 2 * m + ((i >> 1) & 1);
          const int c = (lane & 7) ^ ((rho >> 1) & 7);
          voff[j] = (unsigned)(tok * D + c * 8) * 2u;
        }
        const rsrc_t rs = mk_rsrc(a.ws + WS_XN, S * D * 2);
        auto issue = [&](int t, int slot) {
          LAS char* l = lds + slot * A1_STAGE + pw * 16384;
#pragma unroll
          for (int j = 0; j < 16; ++j) dma16c(rs, voff[j], t * 128, l + j * 1024);
        };
        asm volatile("" ::"s"(tag)); bar();
        wait_flags<1>(sy + SY_LN + (T0 + 128 * pw) / 8 + (lane & 15), true, tag);
        PSTAMP(4, 16);
        prod_prologue<A1_NS>(issue);
        prod_loop<16, A1_NS>(A1_NT, issue);
      } else {
        unsigned voff[8];
#pragma unroll
        for (int j = 0; j < 8; ++j) {
          const int wn = j >> 2, q = (j >> 1) & 1, hf = j & 1, i = hf * 8 + (lane >> 3);
          const int st = p0 + 32 * wn + 16 * q + i;
          const int c = (lane & 7) ^ ((i >> 1) & 7);
          voff[j] = (unsigned)(st * D + c * 8) * 2u;
        }
        const rsrc_t rre = mk_rsrc(a.Bre, P * D * 2), rim = mk_rsrc(a.Bim, P * D * 2);
        auto issue = [&](int t, int slot) {
          LAS char* l = lds + slot * A1_STAGE + 32768;
#pragma unroll
          for (int j = 0; j < 8; ++j) {
            dma16(rre, voff[j], t * 128, l + (j >> 1) * 4096 + (j & 1) * 1024);
            dma16(rim, voff[j], t * 128, l + (j >> 1) * 4096 + (j & 1) * 1024 + 2048);
          }
        };
        prod_prologue<A1_NS>(issue);
        asm volatile("" ::"s"(tag)); bar();
        prod_loop<16, A1_NS>(A1_NT, issue);
      }
    } else {
      const int f0 = ft * 224, dd0 = dt1 * 128;
      if (pw < 2) {
        unsigned voff[7];
#pragma unroll
        for (int j = 0; j < 7; ++j) {
          const int ch = (pw * 7 + j) * 64 + lane, k = ch / 28, pos = ch - k * 28;
          const int c = pos ^ (3 * ((k >> 3) & 1));
          voff[j] = (unsigned)(k * F + f0 + c * 8) * 2u;
        }
        const rsrc_t rs = mk_rsrc(a.w1, S * F * 2);
        auto issue = [&](int t, int slot) {
          LAS char* l = lds + slot * B1_STAGE + pw * 7168;
#pragma unroll
          for (int j = 0; j < 7; ++j) dma16(rs, voff[j], t * (32 * F * 2), l + j * 1024);
        };
        prod_prologue<B1_NS>(issue);
        asm volatile("" ::"s"(tag)); bar();
        prod_loop<7, B1_NS>(B1_NT, issue);
      } else {
        unsigned voff[2];
#pragma unroll
        for (int j = 0; j < 2; ++j) {
          const int k = j * 8 + (lane >> 4), pos = lane & 15;
          const int c = pos ^ (((k & 3) << 1) | (((k >> 3) & 1) << 3));
          voff[j] = (unsigned)((lane >> 4) * D + dd0 + c * 8) * 2u;
        }
        const rsrc_t rs = mk_rsrc(a.ws + WS_HL, S * D * 2);
        auto issue = [&](int t, int slot) {
          LAS char* l = lds + slot * B1_STAGE + B1_ABYTES;
#pragma unroll
          for (int j = 0; j < 8; ++j) dma16c(rs, voff[(j >> 1) & 1], t * (32 * D * 2) + j * 4 * (D * 2), l + j * 1024);
        };
        asm volatile("" ::"s"(tag)); bar();
        wait_flags<1>(sy + SY_LN + lane, true, tag);
        PSTAMP(6, 16);
        prod_prologue<B1_NS>(issue);
        prod_loop<8, B1_NS>(B1_NT, issue);
      }
    }
    bar();
    if (pw < 2) {
      const unsigned r0 = (unsigned)(d0 + 64 * pw + (lane >> 3));
      const unsigned vce = r0 * (P * 2) + (unsigned)(((lane & 7) ^ (lane >> 4)) * 16), vco = vce ^ 64u;
      const int r4 = lane >> 4;
      const unsigned vu = (unsigned)((lane >> 2) * 64 + (((lane & 3) ^ (((r4 & 1) << 1) ^ ((r4 >> 1) * 3))) * 16));
      const rsrc_t rcre = mk_rsrc(a.Cre, D * P * 2), rcim = mk_rsrc(a.Cim, D * P * 2), ru = mk_rsrc(a.ws + WS_U, D * F * 2);
      auto issue = [&](int t, int slot) {
        const int kt = kt0 + t;
        LAS char* l = lds + slot * P2_STAGE + pw * 8192;
        if (kt < 32) {
          const rsrc_t rs = kt < 16 ? rcre : rcim;
#pragma unroll
          for (int j = 0; j < 8; ++j) dma16(rs, (j & 1) ? vco : vce, (kt & 15) * 128 + j * 8 * (P * 2), l + j * 1024);
        } else {
          if (roleA && kt == 32) wait_flags<1>(sy + SY_U + dt * 16 + (lane & 15), true, tag);
#pragma unroll
          for (int j = 0; j < 8; ++j) dma16c(ru, vu, ((2 * (kt - 32) + (j & 1)) * D + d0 + 64 * pw + 16 * (j >> 1)) * 64, l + j * 1024);
        }
      };
      if (roleA) {
        prod_prologue<P2_NS>(issue);
        bar();
        bar();
      } else {
        bar();
        wait_flags<1>(sy + SY_U + dt * 16 + (lane & 15), true, tag);
        PSTAMP(4, 17);
        prod_prologue<P2_NS>(issue);
      }
      prod_loop<8, P2_NS>(nt2, issue);
    } else {
      unsigned voff[2];
#pragma unroll
      for (int j = 0; j < 2; ++j) {
        const int k = j * 8 + (lane >> 4), pos = lane & 15;
        const int c = pos ^ (((k & 3) << 1) | (((k >> 3) & 1) << 3));
        voff[j] = (unsigned)(((pw - 2) * 32 + (lane >> 4)) * S + t0 + c * 8) * 2u;
      }
      const unsigned vh = (unsigned)(lane * 16 + (pw - 2) * 8192);
      const rsrc_t rh = mk_rsrc(a.ws + WS_HT, 2 * P * S * 2), rw = mk_rsrc(a.w2, F * S * 2);
      auto issue = [&](int t, int slot) {
        const int kt = kt0 + t;
        LAS char* l = lds + slot * P2_STAGE + 16384 + (pw - 2) * 8192;
        if (kt < 32) {
#pragma unroll
          for (int j = 0; j < 8; ++j) dma16c(rh, vh, (tt * 32 + kt) * 16384 + j * 1024, l + j * 1024);
        } else {
#pragma unroll
          for (int j = 0; j < 8; ++j) dma16(rw, voff[(j >> 1) & 1], (kt - 32) * (64 * S * 2) + j * 4 * (S * 2), l + j * 1024);
        }
      };
      if (roleA) {
        bar();
        bar();
        wait_flags<1>(sy + SY_H + (tt >> 1) * 16 + (lane & 15), true, tag);
        PSTAMP(6, 17);
        prod_prologue<P2_NS>(issue);
      } else {
        wait_vm<0>();
        prod_prologue<P2_NS>(issue);
        bar();
      }
      prod_loop<8, P2_NS>(nt2, issue);
    }
    return;
  }

  const int wm = wid >> 1, wn = wid & 1;
  int cl = lane & 15, g = lane >> 4, q4 = cl >> 2, p4 = lane & 3, ln = lane;
#define RELANE() do { ln = __builtin_amdgcn_mbcnt_hi(~0u, __builtin_amdgcn_mbcnt_lo(~0u, 0u)); asm volatile("" : "+v"(ln)); cl = ln & 15; g = ln >> 4; q4 = cl >> 2; p4 = ln & 3; } while (0)
  {
    const int row = bid * 8 + wid * 2;
    u32x4 r[2][2];
#pragma unroll
    for (int i = 0; i < 2; ++i) { r[i][0] = *(const u32x4*)(a.x + (size_t)(row + i) * D + lane * 8); r[i][1] = *(const u32x4*)(a.x + (size_t)(row + i) * D + 512 + lane * 8); }
    if (roleA && wid == 0) {
      const rsrc_t rg = mk_rsrc(a.ws + WS_SYNC + SY_GR * 8, 8 * P * 16);
      st16_wt(rg, (unsigned)lane * 16u, (unsigned)(chunk * P + cb * 64) * 16u, (u32x4){0u, 0u, 0u, 0u});
    }
    const rsrc_t rx = mk_rsrc(a.ws + WS_XN, 2 * S * D * 2);
    f32x2 v[2][8];
#pragma unroll
    for (int i = 0; i < 2; ++i)
#pragma unroll
      for (int k = 0; k < 4; ++k) { v[i][k] = (f32x2){bflo(r[i][0][k]), bfhi(r[i][0][k])}; v[i][4 + k] = (f32x2){bflo(r[i][1][k]), bfhi(r[i][1][k])}; }
    STAMP(15);
#pragma unroll
    for (int pass = 0; pass < 2; ++pass) {
      float mean[2], var[2];
#pragma unroll
      for (int i = 0; i < 2; ++i) {
        f32x2 s = ((v[i][0] + v[i][1]) + (v[i][2] + v[i][3])) + ((v[i][4] + v[i][5]) + (v[i][6] + v[i][7]));
        mean[i] = wave_sum(s.x + s.y) * (1.0f / D);
      }
#pragma unroll
      for (int i = 0; i < 2; ++i) {
        f32x2 qq = {0.f, 0.f};
#pragma unroll
        for (int k = 0; k < 8; ++k) { const f32x2 d = v[i][k] - bc2(mean[i]); qq = fma2(d, d, qq); }
        var[i] = wave_sum(qq.x + qq.y) * (1.0f / D);
      }
#pragma unroll
      for (int i = 0; i < 2; ++i) {
        const float mb = rbf(mean[i]), rs_ = 1.0f / rbf(sqrtf(rbf(rbf(var[i]) + 1e-5f)));
        u32x4 o0, o1;
#pragma unroll
        for (int k = 0; k < 8; ++k) {
          const f32x2 d = v[i][k] - bc2(mb);
          const unsigned dd = pk_bf16(d.x, d.y);
          const f32x2 e = (f32x2){bflo(dd), bfhi(dd)} * bc2(rs_);
          const unsigned w = pk_bf16(e.x, e.y);
          v[i][k] = (f32x2){bflo(w), bfhi(w)};
          if (k < 4) o0[k] = w; else o1[k - 4] = w;
        }
        const unsigned so = (unsigned)(pass * S * D + (row + i) * D) * 2u;
        st16_wt(rx, (unsigned)lane * 16u, so, o0);
        st16_wt(rx, (unsigned)lane * 16u + 1024u, so, o1);
      }
    }
    STAMP(1);
    wait_vm<0>();
    asm volatile("" ::"s"(tag)); bar();
    if (wid == 0 && lane == 0) __hip_atomic_store(sy + SY_LN + bid, tag, RLX_AGENT);
    STAMP(2);
  }

  if (roleA) {
    const int p0 = cb * 64;
    const unsigned rb = (unsigned)(cl * 128 + ((g ^ (cl >> 1)) * 16));
    const unsigned abase = rb + wm * 16384, bbase = rb + 32768 + wn * 8192;
    f32x4 acc[8][4];
#pragma unroll
    for (int m = 0; m < 8; ++m)
#pragma unroll
      for (int n = 0; n < 4; ++n) acc[m][n] = (f32x4){0.f, 0.f, 0.f, 0.f};
    bf16x8 xa[8], xb[4], ya[8], yb[4];
#define A1_READ(fa, fb, so, ks) do { LAS char* pa_ = lds + (so) + (abase ^ ((ks) * 64)); LAS char* pb_ = lds + (so) + (bbase ^ ((ks) * 64)); \
    fb[0] = lds_rd128(pb_); fa[0] = lds_rd128(pa_); \
    _Pragma("unroll") for (int n = 1; n < 4; ++n) fb[n] = lds_rd128(pb_ + n * 2048); \
    _Pragma("unroll") for (int m = 1; m < 8; ++m) fa[m] = lds_rd128(pa_ + m * 2048); } while (0)
#define A1_MMA(fa, fb) do { _Pragma("unroll") for (int m = 0; m < 8; ++m) _Pragma("unroll") for (int n = 0; n < 4; ++n) acc[m][n] = MFMA(fa[m], fb[n], acc[m][n]); } while (0)
    int so = 0;
    bar();
    STAMP(3);
    A1_READ(xa, xb, so, 0);
#pragma unroll 1
    for (int t = 0; t < A1_NT; ++t) {
      SCHED_FENCE();
      A1_READ(ya, yb, so, 1);
      A1_MMA(xa, xb);
      sched_phase<12, 32>();
      SCHED_FENCE();
      wait_lgkm0();
      bar();
      SCHED_FENCE();
      so = (so + A1_STAGE == A1_NS * A1_STAGE) ? 0 : so + A1_STAGE;
      A1_READ(xa, xb, so, 0);
      A1_MMA(ya, yb);
      sched_phase<12, 32>();
    }
    SCHED_FENCE();
#undef A1_READ
#undef A1_MMA
    RELANE();
    STAMP(4);
    float cf[2][14];
#pragma unroll
    for (int q = 0; q < 2; ++q)
#pragma unroll
      for (int k = 0; k < 14; ++k) cf[q][k] = ((LAS float*)(lds + LDS_COEF))[k * 64 + 32 * wn + 16 * q + cl];
    f32x2 er[2], ei[2];
#pragma unroll
    for (int q = 0; q < 2; ++q) { er[q] = bc2(0.f); ei[q] = bc2(0.f); }
#pragma unroll
    for (int m = 0; m < 8; ++m)
#pragma unroll
      for (int jj = 0; jj < 2; ++jj)
#pragma unroll
        for (int q = 0; q < 2; ++q) {
          const f32x2 ur = {acc[m][2 * q][2 * jj], acc[m][2 * q][2 * jj + 1]}, ui = {acc[m][2 * q + 1][2 * jj], acc[m][2 * q + 1][2 * jj + 1]};
          const f32x2 nr = fma2(bc2(cf[q][0]), er[q], fma2(bc2(-cf[q][1]), ei[q], ur));
          const f32x2 ni = fma2(bc2(cf[q][0]), ei[q], fma2(bc2(cf[q][1]), er[q], ui));
          er[q] = nr; ei[q] = ni;
        }
    float sr[2], si[2], pr_[2], pi_[2];
    LAS float* tot = (LAS float*)(lds + LDS_TOT);
#pragma unroll
    for (int q = 0; q < 2; ++q) {
      float tr, ti;
      CMUL(tr, ti, cf[q][4], cf[q][5], er[q].x, ei[q].x);
      sr[q] = tr + er[q].y; si[q] = ti + ei[q].y;
      float vr = __shfl_up(sr[q], 16), vi = __shfl_up(si[q], 16);
      CMUL(tr, ti, cf[q][6], cf[q][7], vr, vi);
      if (g >= 1) { sr[q] += tr; si[q] += ti; }
      vr = __shfl_up(sr[q], 32); vi = __shfl_up(si[q], 32);
      CMUL(tr, ti, cf[q][8], cf[q][9], vr, vi);
      if (g >= 2) { sr[q] += tr; si[q] += ti; }
      vr = __shfl_up(sr[q], 16); vi = __shfl_up(si[q], 16);
      pr_[q] = g >= 1 ? vr : 0.f; pi_[q] = g >= 1 ? vi : 0.f;
      if (wm == 0 && g == 3) *(LAS f32x2*)(tot + (32 * wn + 16 * q + cl) * 2) = (f32x2){sr[q], si[q]};
    }
    wait_lgkm0();
    bar();
    f32x2 tq[2];
    gu64* gran = sy + SY_GR;
#pragma unroll
    for (int q = 0; q < 2; ++q) {
      tq[q] = *(LAS f32x2*)(tot + (32 * wn + 16 * q + cl) * 2);
      if (wm == 1 && g == 3 && chunk < 7) {
        float tr, ti;
        CMUL(tr, ti, cf[q][10], cf[q][11], tq[q].x, tq[q].y);
        gu64* gp = gran + ((size_t)chunk * P + p0 + 32 * wn + 16 * q + cl) * 2;
        __hip_atomic_store(gp, (1ull << 32) | __float_as_uint(tr + sr[q]), RLX_AGENT);
        __hip_atomic_store(gp + 1, (1ull << 32) | __float_as_uint(ti + si[q]), RLX_AGENT);
      }
    }
    STAMP(5);
#pragma unroll
    for (int m = 0; m < 8; ++m)
#pragma unroll
      for (int q = 0; q < 2; ++q) {
        const f32x4 ur = acc[m][2 * q], ui = acc[m][2 * q + 1];
        acc[m][2 * q] = ur * cf[q][2] - ui * cf[q][3];
        acc[m][2 * q + 1] = ui * (-cf[q][2]) - ur * cf[q][3];
        asm volatile("" : "+v"(acc[m][2 * q]), "+v"(acc[m][2 * q + 1]));
      }
    float hr[2] = {0.f, 0.f}, hi[2] = {0.f, 0.f};
    if (chunk > 0) {
      float gr[2][2], gi[2][2];
#pragma unroll 1
      for (unsigned spins = 0;; ++spins) {
        bool ok = true;
#pragma unroll
        for (int q = 0; q < 2; ++q)
#pragma unroll
          for (int k = 0; k < 2; ++k) {
            const int cc = (g + 4 * k < chunk) ? g + 4 * k : 0;
            gu64* gp = gran + ((size_t)cc * P + p0 + 32 * wn + 16 * q + cl) * 2;
            const u64 vr = __hip_atomic_load(gp, RLX_AGENT), vi = __hip_atomic_load(gp + 1, RLX_AGENT);
            ok &= ((vr >> 32) == 1ull) & ((vi >> 32) == 1ull);
            gr[q][k] = __uint_as_float((unsigned)vr); gi[q][k] = __uint_as_float((unsigned)vi);
          }
        if (__all(ok) || spins > SPIN_CAP) break;
        __builtin_amdgcn_s_sleep(1);
      }
#pragma unroll
      for (int q = 0; q < 2; ++q) {
        float ar_ = 0.f, ai_ = 0.f;
#pragma unroll
        for (int k = 0; k < 2; ++k) {
          float vr = gr[q][k], vi = gi[q][k];
          const int e = chunk - 1 - (g + 4 * k);
#pragma unroll
          for (int i = 0; i < 6 - 4 * k; ++i) {
            float tr, ti;
            CMUL(tr, ti, cf[q][12], cf[q][13], vr, vi);
            vr = i < e ? tr : vr; vi = i < e ? ti : vi;
          }
          ar_ += e >= 0 ? vr : 0.f; ai_ += e >= 0 ? vi : 0.f;
        }
        ar_ += __shfl_xor(ar_, 16); ai_ += __shfl_xor(ai_, 16);
        ar_ += __shfl_xor(ar_, 32); ai_ += __shfl_xor(ai_, 32);
        hr[q] = ar_; hi[q] = ai_;
      }
    }
    STAMP(6);
    f32x2 wr[2], wi[2];
#pragma unroll
    for (int q = 0; q < 2; ++q) {
      float tr, ti;
      if (wm == 1) { CMUL(tr, ti, cf[q][10], cf[q][11], hr[q], hi[q]); hr[q] = tr + tq[q].x; hi[q] = ti + tq[q].y; }
      CMUL(tr, ti, cf[q][6], cf[q][7], hr[q], hi[q]);
      if (g & 1) { hr[q] = tr; hi[q] = ti; }
      CMUL(tr, ti, cf[q][8], cf[q][9], hr[q], hi[q]);
      if (g & 2) { hr[q] = tr; hi[q] = ti; }
      hr[q] += pr_[q]; hi[q] += pi_[q];
      CMUL(tr, ti, cf[q][4], cf[q][5], hr[q], hi[q]);
      const f32x2 h0r = {hr[q], tr + er[q].x}, h0i = {hi[q], ti + ei[q].x};
      wr[q] = h0r * cf[q][2] - h0i * cf[q][3];
      wi[q] = h0i * (-cf[q][2]) - h0r * cf[q][3];
    }
    LAS char* scr = lds + wid * P2_STAGE + 16384;
    const unsigned fsw = (unsigned)(((cl & 3) << 1) | (((cl >> 3) & 1) << 3));
#pragma unroll
    for (int mh = 0; mh < 2; ++mh) {
      u32x4 o[2][2][2];
#pragma unroll
      for (int mq = 0; mq < 4; ++mq) {
        const int m = 4 * mh + mq;
#pragma unroll
        for (int q = 0; q < 2; ++q) {
          const f32x2 u0r = {acc[m][2 * q][0], acc[m][2 * q][1]}, u0i = {acc[m][2 * q + 1][0], acc[m][2 * q + 1][1]};
          const f32x2 u1r = {acc[m][2 * q][2], acc[m][2 * q][3]}, u1i = {acc[m][2 * q + 1][2], acc[m][2 * q + 1][3]};
          const f32x2 ar2 = bc2(cf[q][0]), ai2 = bc2(cf[q][1]);
          const f32x2 n0r = fma2(ar2, wr[q], fma2(ai2, wi[q], u0r)), n0i = fma2(ar2, wi[q], fma2(-ai2, wr[q], u0i));
          const f32x2 n1r = fma2(ar2, n0r, fma2(ai2, n0i, u1r)), n1i = fma2(ar2, n0i, fma2(-ai2, n0r, u1i));
          wr[q] = n1r; wi[q] = n1i;
          o[q][0][0][mq] = pk_bf16(n0r.x, n1r.x); o[q][0][1][mq] = pk_bf16(n0r.y, n1r.y);
          o[q][1][0][mq] = pk_bf16(n0i.x, n1i.x); o[q][1][1][mq] = pk_bf16(n0i.y, n1i.y);
        }
      }
#pragma unroll
      for (int q = 0; q < 2; ++q)
#pragma unroll
        for (int part = 0; part < 2; ++part)
#pragma unroll
          for (int run = 0; run < 2; ++run)
            *(LAS u32x4*)(scr + (part * 32 + 16 * q + cl) * 256 + (((unsigned)(4 * g + 2 * run + mh) ^ fsw) * 16)) = o[q][part][run];
    }
    const rsrc_t rh = mk_rsrc(a.ws + WS_HT, 2 * P * S * 2);
#pragma unroll
    for (int i = 0; i < 16; ++i) {
      const u32x4 v = *(LAS u32x4*)(scr + i * 1024 + ln * 16);
      st16_wt(rh, (unsigned)(ln * 16 + (i & 7) * 1024), (unsigned)(((2 * chunk + wm) * 32 + (i >> 3) * 16 + cb) * 16384 + wn * 8192), v);
    }
    STAMP(7);
    wait_vm<0>();
    bar();
    if (wid == 0 && ln == 0) __hip_atomic_store(sy + SY_H + chunk * 16 + cb, tag, RLX_AGENT);
    STAMP(8);
  } else {
    const int f0 = ft * 224, dd0 = dt1 * 128;
    const int k0 = 8 * g + q4;
    const unsigned a0 = (unsigned)(k0 * 448 + ((12 * wm + p4) ^ (3 * (g & 1))) * 16 + (p4 & 1) * 8);
    const unsigned a1 = a0 ^ 8u;
    const unsigned a2 = (unsigned)(k0 * 448 + ((24 + 2 * wm + (p4 >> 1)) ^ (3 * (g & 1))) * 16 + (p4 & 1) * 8);
    unsigned bb[4];
#pragma unroll
    for (int n = 0; n < 4; ++n) bb[n] = (unsigned)(B1_ABYTES + k0 * 256 + ((8 * wn + 2 * n + (p4 >> 1)) ^ ((q4 << 1) | ((g & 1) << 3))) * 16 + (p4 & 1) * 8);
    f32x4 acc[7][4];
#pragma unroll
    for (int m = 0; m < 7; ++m) {
      const u32x2 bv = *(const u32x2*)(a.b1 + f0 + (m < 6 ? 96 * wm + 32 * (m >> 1) + 8 * g + 4 * ((m & 1) ^ (g & 1)) : 192 + 16 * wm + 4 * g));
#pragma unroll
      for (int n = 0; n < 4; ++n) acc[m][n] = (f32x4){bflo(bv[0]), bfhi(bv[0]), bflo(bv[1]), bfhi(bv[1])};
    }
    bf16x8 xa[7], xb[4], ya[7], yb[4];
#define B1_READ(fa, fb, so) do { LAS char* s_ = lds + (so); \
    fb[0] = lds_rdtr(s_ + bb[0], s_ + bb[0] + 1024); fa[0] = lds_rdtr(s_ + a0, s_ + a0 + 1792); \
    _Pragma("unroll") for (int n = 1; n < 4; ++n) fb[n] = lds_rdtr(s_ + bb[n], s_ + bb[n] + 1024); \
    _Pragma("unroll") for (int m = 1; m < 6; ++m) { const unsigned o_ = ((m & 1) ? a1 : a0) + (m >> 1) * 64; fa[m] = lds_rdtr(s_ + o_, s_ + o_ + 1792); } \
    fa[6] = lds_rdtr(s_ + a2, s_ + a2 + 1792); } while (0)
#define B1_MMA(fa, fb) do { _Pragma("unroll") for (int m = 0; m < 7; ++m) _Pragma("unroll") for (int n = 0; n < 4; ++n) acc[m][n] = MFMA(fa[m], fb[n], acc[m][n]); } while (0)
#define B1_NEXT() so = (so + B1_STAGE == B1_NS * B1_STAGE) ? 0 : so + B1_STAGE
    int so = 0;
    bar();
    STAMP(3);
    B1_READ(xa, xb, so);
#pragma unroll 1
    for (int t = 0; t < B1_NT; t += 2) {
      SCHED_FENCE(); wait_lgkm0(); bar(); SCHED_FENCE();
      B1_NEXT();
      B1_READ(ya, yb, so);
      B1_MMA(xa, xb);
      sched_phase<22, 28>();
      SCHED_FENCE(); wait_lgkm0(); bar(); SCHED_FENCE();
      B1_NEXT();
      B1_READ(xa, xb, so);
      B1_MMA(ya, yb);
      sched_phase<22, 28>();
    }
    SCHED_FENCE();
#undef B1_READ
#undef B1_MMA
#undef B1_NEXT
    RELANE();
    STAMP(4);
    const rsrc_t ru = mk_rsrc(a.ws + WS_U, D * F * 2);
#pragma unroll
    for (int n = 0; n < 4; ++n) {
      const unsigned vo = (unsigned)((dd0 + 64 * wn + 16 * n + cl) * 64);
      u32x2 w[7];
#pragma unroll
      for (int m = 0; m < 7; ++m) {
        const f32x2 v0 = gelu_tanh2((f32x2){acc[m][n][0], acc[m][n][1]}), v1 = gelu_tanh2((f32x2){acc[m][n][2], acc[m][n][3]});
        w[m][0] = pk_bf16(v0.x, v0.y); w[m][1] = pk_bf16(v1.x, v1.y);
      }
#pragma unroll
      for (int mm = 0; mm < 3; ++mm) {
        const bool sw = g & 1;
        const u32x2 lo = sw ? w[2 * mm + 1] : w[2 * mm], hi = sw ? w[2 * mm] : w[2 * mm + 1];
        st16_wt(ru, vo + 16 * g, (unsigned)((ft * 7 + 3 * wm + mm) * D * 64), (u32x4){lo[0], lo[1], hi[0], hi[1]});
      }
      st8_wt(ru, vo + 8 * g, (unsigned)((ft * 7 + 6) * D * 64 + 32 * wm), w[6]);
    }
    STAMP(7);
    wait_vm<0>();
    bar();
    if (wid == 0 && ln == 0) __hip_atomic_store(sy + SY_U + dt1 * 16 + ft, tag, RLX_AGENT);
    STAMP(8);
  }

  const int kh = roleA ? 0 : 1, pa = bid ^ 1;
  f32x4 acc[4][4];
  {
    const rsrc_t rxn = mk_rsrc(a.ws + WS_XN, S * D * 2);
    f32x4 init[2][4];
#pragma unroll
    for (int mi = 0; mi < 2; ++mi) {
      const int d = d0 + 64 * wm + 16 * (2 * kh + mi) + 4 * g;
      const u32x2 dk = *(const u32x2*)(a.Dsk + d);
#pragma unroll
      for (int n = 0; n < 4; ++n) {
        const int tok = t0 + 64 * wn + 16 * n + cl;
        const u32x2 xv = *(const u32x2*)(a.x + (size_t)tok * D + d);
        const u32x2 nv = __builtin_amdgcn_raw_buffer_load_b64(rxn, (unsigned)(tok * D + d) * 2u, 0, 16);
        const float b2 = bf2f(a.b2[tok]);
        const unsigned s0 = pk_bf16(bflo(nv[0]) * bflo(dk[0]), bfhi(nv[0]) * bfhi(dk[0])), s1 = pk_bf16(bflo(nv[1]) * bflo(dk[1]), bfhi(nv[1]) * bfhi(dk[1]));
        init[mi][n] = (f32x4){bflo(xv[0]) + bflo(s0) + b2, bfhi(xv[0]) + bfhi(s0) + b2, bflo(xv[1]) + bflo(s1) + b2, bfhi(xv[1]) + bfhi(s1) + b2};
      }
    }
    const f32x4 z = {0.f, 0.f, 0.f, 0.f};
#pragma unroll
    for (int n = 0; n < 4; ++n) {
      acc[0][n] = kh ? z : init[0][n]; acc[1][n] = kh ? z : init[1][n];
      acc[2][n] = kh ? init[0][n] : z; acc[3][n] = kh ? init[1][n] : z;
    }
  }
  const unsigned ac0 = (unsigned)((wm * 64 + cl) * 128 + ((g ^ (cl >> 1)) * 16));
  const unsigned au0 = (unsigned)(wm * 8192 + cl * 64 + ((g ^ (((q4 & 1) << 1) ^ ((q4 >> 1) * 3))) * 16));
  unsigned ab0 = roleA ? ac0 : au0, ab1 = roleA ? (ac0 ^ 64u) : au0 + 1024u;
  unsigned bb[4];
#pragma unroll
  for (int n = 0; n < 4; ++n) bb[n] = (unsigned)(16384 + (8 * g + q4) * 256 + ((8 * wn + 2 * n + (p4 >> 1)) ^ ((q4 << 1) | ((g & 1) << 3))) * 16 + (p4 & 1) * 8);
  bf16x8 xa[4], xb[4], ya[4], yb[4];
#define K2_READ(fa, fb, so, ks) do { LAS char* pa_ = lds + (so) + ((ks) ? ab1 : ab0); LAS char* s_ = lds + (so) + (ks) * 8192; \
    fa[0] = lds_rd128(pa_); fb[0] = lds_rdtr(s_ + bb[0], s_ + bb[0] + 1024); \
    _Pragma("unroll") for (int n = 1; n < 4; ++n) fb[n] = lds_rdtr(s_ + bb[n], s_ + bb[n] + 1024); \
    _Pragma("unroll") for (int m = 1; m < 4; ++m) fa[m] = lds_rd128(pa_ + m * 2048); } while (0)
#define K2_MMA(fa, fb) do { _Pragma("unroll") for (int m = 0; m < 4; ++m) _Pragma("unroll") for (int n = 0; n < 4; ++n) acc[m][n] = MFMA(fa[m], fb[n], acc[m][n]); } while (0)
  int so = 0;
  STAMP(18);
  bar();
  STAMP(9);
  K2_READ(xa, xb, so, 0);
#pragma unroll 1
  for (int t = 0; t < nt2; ++t) {
    SCHED_FENCE();
    K2_READ(ya, yb, so, 1);
    K2_MMA(xa, xb);
    sched_phase<12, 16>();
    SCHED_FENCE();
    wait_lgkm0();
    bar();
    SCHED_FENCE();
    so = (so + P2_STAGE) & (P2_NS * P2_STAGE - 1);
    if (t == 31) { ab0 = au0; ab1 = au0 + 1024u; }
    K2_READ(xa, xb, so, 0);
    K2_MMA(ya, yb);
    sched_phase<12, 16>();
  }
  SCHED_FENCE();
#undef K2_READ
#undef K2_MMA
  STAMP(10);
  const rsrc_t rxb = mk_rsrc(a.ws + WS_XB, 256 * 32768);
  const unsigned xo = (unsigned)(wid * 8192 + ln * 16);
  f32x4 keep[2][4];
#pragma unroll
  for (int mi = 0; mi < 2; ++mi)
#pragma unroll
    for (int n = 0; n < 4; ++n) {
      const f32x4 snd = kh ? acc[mi][n] : acc[2 + mi][n];
      keep[mi][n] = kh ? acc[2 + mi][n] : acc[mi][n];
      st16_wt(rxb, xo + (mi * 4 + n) * 1024, bid * 32768, __builtin_bit_cast(u32x4, snd));
    }
  wait_vm<0>();
  bar();
  STAMP(11);
  if (wid == 0) {
    if (ln == 0) __hip_atomic_store(sy + SY_X + bid, tag, RLX_AGENT);
#pragma unroll 1
    for (unsigned spins = 0; spins < SPIN_CAP; ++spins) {
      if (__all(__hip_atomic_load(sy + SY_X + pa, RLX_AGENT) == tag)) break;
      __builtin_amdgcn_s_sleep(1);
    }
  }
  STAMP(12);
  bar();
#pragma unroll
  for (int mi = 0; mi < 2; ++mi)
#pragma unroll
    for (int n = 0; n < 4; ++n) {
      const u32x4 r = __builtin_amdgcn_raw_buffer_load_b128(rxb, xo + (mi * 4 + n) * 1024, pa * 32768, 16);
      keep[mi][n] += __builtin_bit_cast(f32x4, r);
    }
#pragma unroll
  for (int mi = 0; mi < 2; ++mi)
#pragma unroll
    for (int n = 0; n < 4; ++n) {
      const int d = d0 + 64 * wm + 16 * (2 * kh + mi) + 4 * g, tok = t0 + 64 * wn + 16 * n + cl;
      *(f32x4*)(a.out + (size_t)tok * D + d) = keep[mi][n];
    }
  STAMP(13);
}

extern "C" void kernel_launch(void* const* d_in, const int* in_sizes, int n_in, void* d_out, int out_size, void* d_ws, size_t ws_size,
                              hipStream_t stream) {
  (void)hipFuncSetAttribute((const void*)fused, hipFuncAttributeMaxDynamicSharedMemorySize, LDS_BYTES);
  Args a;
  a.x = (const u16*)d_in[0]; a.Lre = (const u16*)d_in[1]; a.Lim = (const u16*)d_in[2]; a.Bre = (const u16*)d_in[3]; a.Bim = (const u16*)d_in[4];
  a.Cre = (const u16*)d_in[5]; a.Cim = (const u16*)d_in[6]; a.logdt = (const u16*)d_in[7]; a.Dsk = (const u16*)d_in[8];
  a.w1 = (const u16*)d_in[10]; a.b1 = (const u16*)d_in[11]; a.w2 = (const u16*)d_in[12]; a.b2 = (const u16*)d_in[13];
  a.out = (float*)d_out; a.ws = (char*)d_ws;
  fused<<<256, 512, LDS_BYTES, stream>>>(a);
}
```

```cpp
#include <hip/hip_runtime.h>
#include <stdint.h>

typedef unsigned short u16;
typedef unsigned long long u64;
typedef __bf16 bf16x8 __attribute__((ext_vector_type(8)));
typedef short s16x4 __attribute__((ext_vector_type(4)));
typedef short s16x8 __attribute__((ext_vector_type(8)));
typedef float f32x2 __attribute__((ext_vector_type(2)));
typedef float f32x4 __attribute__((ext_vector_type(4)));
typedef unsigned u32x4 __attribute__((ext_vector_type(4)));
typedef unsigned u32x2 __attribute__((ext_vector_type(2)));
#define LAS __attribute__((address_space(3)))
#define GAS __attribute__((address_space(1)))
typedef GAS unsigned long long gu64;
typedef __amdgpu_buffer_rsrc_t rsrc_t;
#define RLX_AGENT __ATOMIC_RELAXED, __HIP_MEMORY_SCOPE_AGENT

constexpr int S = 2048, D = 1024, P = 1024, F = 3584;
constexpr size_t MB = 1024 * 1024;
constexpr size_t WS_HL = 4 * MB, WS_HT = 8 * MB, WS_U = 16 * MB, WS_XB = 24 * MB, WS_SYNC = 32 * MB;
constexpr int SY_LN = 64, SY_H = 320, SY_U = 448, SY_X = 576, SY_ST = 1024, SY_SB = 3072, SY_GR = 5120;
constexpr unsigned SYNC_BYTES = (SY_GR + 8 * P * 2) * 8;
constexpr unsigned SPIN_CAP = 1u << 17;
#define NBA 22
#define NAB 4
constexpr unsigned CODE_PF_BYTES = 40 * 1024;
#define PF_NST 12
#define PF_NST2 20
#define PF_WB 4
#define PF_DELAY2 8
#define A1_RS 2
#define PSTAMP(w, i)
#define TRACE(e)
#define TRACE_DUMP()
#define STAMP(i) PSTAMP(0, i)

__device__ __forceinline__ float bf2f(u16 v) { return __uint_as_float(((unsigned)v) << 16); }
__device__ __forceinline__ float bflo(unsigned v) { return __uint_as_float(v << 16); }
__device__ __forceinline__ float bfhi(unsigned v) { return __uint_as_float(v & 0xffff0000u); }
__device__ __forceinline__ unsigned pk_bf16(float lo, float hi) {
  typedef __bf16 bf16x2 __attribute__((ext_vector_type(2)));
  f32x2 v = {lo, hi};
  bf16x2 r = __builtin_convertvector(v, bf16x2);
  return __builtin_bit_cast(unsigned, r);
}
__device__ __forceinline__ float rbf(float f) { return bflo(pk_bf16(f, f)); }
__device__ __forceinline__ f32x2 fma2(f32x2 a, f32x2 b, f32x2 c) { return __builtin_elementwise_fma(a, b, c); }
__device__ __forceinline__ f32x2 bc2(float v) { return (f32x2){v, v}; }

__device__ __forceinline__ rsrc_t mk_rsrc(const void* p, unsigned bytes) { return __builtin_amdgcn_make_buffer_rsrc((void*)p, 0, bytes, 0x00020000); }
__device__ __forceinline__ void dma16(rsrc_t rs, unsigned voff, unsigned soff, LAS char* l) { __builtin_amdgcn_raw_ptr_buffer_load_lds(rs, (LAS void*)l, 16, voff, soff, 0, 0); }
__device__ __forceinline__ void dma16c(rsrc_t rs, unsigned voff, unsigned soff, LAS char* l) { __builtin_amdgcn_raw_ptr_buffer_load_lds(rs, (LAS void*)l, 16, voff, soff, 0, 16); }
__device__ __forceinline__ void dma4(rsrc_t rs, unsigned voff, unsigned soff, LAS char* l) { __builtin_amdgcn_raw_ptr_buffer_load_lds(rs, (LAS void*)l, 4, voff, soff, 0, 0); }
__device__ __forceinline__ void st16_wt(rsrc_t rs, unsigned voff, unsigned soff, u32x4 v) { __builtin_amdgcn_raw_buffer_store_b128(v, rs, voff + soff, 0, 16); }
__device__ __forceinline__ void st8_wt(rsrc_t rs, unsigned voff, unsigned soff, u32x2 v) { __builtin_amdgcn_raw_buffer_store_b64(v, rs, voff + soff, 0, 16); }
template <int N> __device__ __forceinline__ void wait_vm() { asm volatile("s_waitcnt vmcnt(%0)" ::"n"(N) : "memory"); }
__device__ __forceinline__ void wait_lgkm0() { asm volatile("" ::: "memory"); __builtin_amdgcn_s_waitcnt(0xC07F); asm volatile("" ::: "memory"); }
__device__ __forceinline__ void bar() {
  asm volatile("" ::: "memory");
  __builtin_amdgcn_s_barrier();
  asm volatile("" ::: "memory");
}
#define SCHED_FENCE() __builtin_amdgcn_sched_barrier(0)
__device__ __forceinline__ bf16x8 lds_rd128(unsigned p) { return *(LAS bf16x8*)(size_t)p; }
__device__ __forceinline__ s16x4 lds_rdtr(unsigned p) { return __builtin_amdgcn_ds_read_tr16_b64_v4i16((LAS s16x4*)(size_t)p); }
struct TrFrag { s16x4 h[2]; };
__device__ __forceinline__ bf16x8 frag(const bf16x8& f) { return f; }
__device__ __forceinline__ bf16x8 frag(const TrFrag& f) { s16x8 ab = __builtin_shufflevector(f.h[0], f.h[1], 0, 1, 2, 3, 4, 5, 6, 7); return __builtin_bit_cast(bf16x8, ab); }
#define MFMA(a, b, c) __builtin_amdgcn_mfma_f32_16x16x32_bf16(frag(a), frag(b), (c), 0, 0, 0)
template <int NM, class FA, class FB, class Side>
__device__ __forceinline__ void phase(f32x4 (*acc)[4], const FA* fa, const FB* fb, Side side) {
#pragma unroll
  for (int i = 0; i < NM; ++i) {
    acc[i >> 2][i & 3] = MFMA(fa[i >> 2], fb[i & 3], acc[i >> 2][i & 3]);
    side(i);
    SCHED_FENCE();
  }
}

template <int NL> __device__ __forceinline__ void wait_flags(gu64* p, bool active, u64 tag) {
#pragma unroll 1
  for (unsigned spins = 0; spins < SPIN_CAP; ++spins) {
    bool ok = true;
    if (active) {
#pragma unroll
      for (int k = 0; k < NL; ++k) ok &= __hip_atomic_load(p + 64 * k, RLX_AGENT) == tag;
    }
    if (__all(ok)) break;
    __builtin_amdgcn_s_sleep(2);
  }
  __builtin_amdgcn_fence(__ATOMIC_ACQUIRE, "wavefront");
}

__device__ __forceinline__ void lds_wait(unsigned addr, unsigned want) {
#pragma unroll 1
  for (unsigned spins = 0; spins < (SPIN_CAP << 4); ++spins) {
    if (__builtin_amdgcn_readfirstlane(__hip_atomic_load((LAS unsigned*)(size_t)addr, __ATOMIC_RELAXED, __HIP_MEMORY_SCOPE_WORKGROUP)) >= want) break;
    __builtin_amdgcn_s_sleep(1);
  }
  asm volatile("" ::: "memory");
}
__device__ __forceinline__ void publish(unsigned cnt, gu64* flag, u64 tag, int lane) {
  wait_vm<0>();
  wait_lgkm0();
  if (lane == 0 && __hip_atomic_fetch_add((LAS unsigned*)(size_t)cnt, 1u, __ATOMIC_RELAXED, __HIP_MEMORY_SCOPE_WORKGROUP) == 3u) __hip_atomic_store(flag, tag, RLX_AGENT);
}

template <int NS, class Issue> __device__ __forceinline__ void prod_prologue(Issue issue) {
#pragma unroll 1
  for (int t = 0; t < NS - 1; ++t) issue(t, t);
}
template <int L, int NS, bool TR = false, class Issue> __device__ __forceinline__ void prod_loop(int nt, Issue issue) {
  const int lane = threadIdx.x & 63; (void)lane;
  int slot = NS - 1;
#pragma unroll 1
  for (int t = 0; t < nt; ++t) {
    const int rem = nt - 1 - t;
    if (rem >= NS - 2) wait_vm<(NS - 2) * L>();
    else if (NS > 3 && rem == 1) wait_vm<L>();
    else if (NS > 4 && rem == 2) wait_vm<2 * L>();
    else if (NS > 5 && rem == 3) wait_vm<3 * L>();
    else if (NS > 6 && rem == 4) wait_vm<4 * L>();
    else wait_vm<0>();
    bar();
    if (t + NS - 1 < nt) { issue(t + NS - 1, slot); slot = (slot + 1 == NS) ? 0 : slot + 1; }
    if (TR) TRACE(t);
  }
}

__device__ __forceinline__ float wave_sum(float v) {
  v += __builtin_bit_cast(float, __builtin_amdgcn_update_dpp(0, __builtin_bit_cast(int, v), 0xB1, 0xf, 0xf, true));
  v += __builtin_bit_cast(float, __builtin_amdgcn_update_dpp(0, __builtin_bit_cast(int, v), 0x4E, 0xf, 0xf, true));
  v += __builtin_bit_cast(float, __builtin_amdgcn_update_dpp(0, __builtin_bit_cast(int, v), 0x141, 0xf, 0xf, true));
  v += __builtin_bit_cast(float, __builtin_amdgcn_update_dpp(0, __builtin_bit_cast(int, v), 0x140, 0xf, 0xf, true));
  const int iv = __builtin_bit_cast(int, v);
  const float a = __builtin_bit_cast(float, __builtin_amdgcn_readlane(iv, 0)), b = __builtin_bit_cast(float, __builtin_amdgcn_readlane(iv, 16));
  const float c = __builtin_bit_cast(float, __builtin_amdgcn_readlane(iv, 32)), d = __builtin_bit_cast(float, __builtin_amdgcn_readlane(iv, 48));
  return (a + b) + (c + d);
}

struct Args {
  const u16 *x, *Lre, *Lim, *Bre, *Bim, *Cre, *Cim, *logdt, *Dsk, *w1, *b1, *w2, *b2;
  float* out;
  char* ws;
};

constexpr int A1_NS = 3, A1_STAGE = 49152, A1_NT = D / 64;
constexpr int B1_NS = 7, B1_ABYTES = 32 * 448, B1_STAGE = B1_ABYTES + 32 * 256, B1_NT = S / 32;
constexpr int P2_NS = 4, P2_STAGE = 32768;
constexpr int LDS_BYTES = 160 * 1024;
constexpr int LDS_COEF = A1_NS * A1_STAGE;
constexpr int LDS_TOT = LDS_COEF + 14 * 256;
constexpr int LDS_SB = LDS_TOT + 512;
constexpr int LDS_STAT = LDS_SB + 512;
constexpr int LDS_RAW = LDS_STAT + 8 * 272;
constexpr int LDS_RES = P2_NS * P2_STAGE;
constexpr int LDS_CNT = LDS_BYTES - 256 - 2048 - 64;
constexpr int LDS_FLG = LDS_BYTES - 256 - 2048;
constexpr int LDS_DUMMY = LDS_BYTES - 256;
static_assert(LDS_RAW + 3072 + 768 <= LDS_CNT && B1_NS * B1_STAGE <= LDS_CNT && LDS_RES + 17920 <= LDS_CNT && P2_NS * P2_STAGE <= LDS_COEF && LDS_RAW % 16 == 0, "lds");

__device__ __forceinline__ f32x2 gelu_tanh2(f32x2 x) {
  const f32x2 z = x * fma2(x * x, bc2(-0.10294323967f), bc2(-2.30220819813f));
  const f32x2 d = (f32x2){__builtin_amdgcn_exp2f(z.x), __builtin_amdgcn_exp2f(z.y)} + bc2(1.0f);
  return x * (f32x2){__builtin_amdgcn_rcpf(d.x), __builtin_amdgcn_rcpf(d.y)};
}
#define CMUL(or_, oi_, ar_, ai_, br_, bi_) do { const float t_ = (ar_) * (br_) - (ai_) * (bi_); oi_ = (ar_) * (bi_) + (ai_) * (br_); or_ = t_; } while (0)

__global__ __launch_bounds__(512, 2) void fused(Args a) {
  extern __shared__ __attribute__((aligned(16))) char smem[];
  LAS char* lds = (LAS char*)smem;
  u64 pc0;
  asm volatile("s_getpc_b64 %0" : "=s"(pc0));
  const int wid = __builtin_amdgcn_readfirstlane(threadIdx.x >> 6), lane = threadIdx.x & 63;
  const int bid = blockIdx.x, xc = bid & 7, idx = bid >> 3, xg = xc >> 1;
  const bool roleA = !(xc & 1);
  const int ri = bid >> 1;
  gu64* sy = (gu64*)(a.ws + WS_SYNC);
  u64 tag;
#define LOAD_TAG() do { const __attribute__((address_space(4))) u64* ep_ = (const __attribute__((address_space(4))) u64*)(a.ws + WS_SYNC); \
    asm volatile("" : "+s"(ep_)::"memory"); tag = *ep_ + 1ull; } while (0)
#define HOLD_TAG() asm volatile("" ::"s"(tag))
#define tag32 ((unsigned)tag)
  const int chunk = 4 * (xg & 1) + (idx & 3), cb = 8 * (xg >> 1) + (idx >> 2);
  const int ft = 4 * xg + (idx & 3), dt1 = idx >> 2;
  const int tt = 4 * xg + (idx & 3), dt = idx >> 2;
  const int t0 = tt * 128, d0 = dt * 128;
  const int nC = roleA ? 32 - NAB : NAB, kc0 = roleA ? 0 : 32 - NAB, ku0 = roleA ? 0 : NBA, nt2 = nC + (roleA ? NBA : 56 - NBA);
  const rsrc_t rsy = mk_rsrc(a.ws + WS_SYNC, SYNC_BYTES);
  STAMP(0);

  auto touch = [&](const u16* base, unsigned bytes, int u0, int n) {
    const rsrc_t rs = mk_rsrc(base, bytes);
#pragma unroll 1
    for (int k = 0; k < n; ++k) dma4(rs, (unsigned)(lane * 128), (unsigned)(u0 + k) * 8192u, lds + LDS_DUMMY);
  };
  auto w1_voff = [&](int piece) {
    const int ch = piece * 64 + lane, k = ch / 28, pos = ch - k * 28;
    return (unsigned)(k * F + ft * 224 + (pos ^ (3 * ((k >> 3) & 1))) * 8) * 2u;
  };
  if (!roleA) {
    const int row = ri * 16 + wid * 2;
    u32x4 r[2][2], brow[2][2];
    {
      const u16* xp = a.x + (size_t)row * D;
      const u16* bp = (ri < 64 ? a.Bre : a.Bim) + (size_t)(row & 1023) * D;
      const unsigned lo = (unsigned)lane * 16u;
      asm volatile("global_load_dwordx4 %0, %4, %5\n\tglobal_load_dwordx4 %1, %4, %5 offset:1024\n\tglobal_load_dwordx4 %2, %4, %5 offset:2048\n\tglobal_load_dwordx4 %3, %4, %5 offset:3072"
                   : "=&v"(r[0][0]), "=&v"(r[0][1]), "=&v"(r[1][0]), "=&v"(r[1][1]) : "v"(lo), "s"(xp) : "memory");
      asm volatile("global_load_dwordx4 %0, %4, %5\n\tglobal_load_dwordx4 %1, %4, %5 offset:1024\n\tglobal_load_dwordx4 %2, %4, %5 offset:2048\n\tglobal_load_dwordx4 %3, %4, %5 offset:3072"
                   : "=&v"(brow[0][0]), "=&v"(brow[0][1]), "=&v"(brow[1][0]), "=&v"(brow[1][1]) : "v"(lo), "s"(bp) : "memory");
    }
    dma4(mk_rsrc(a.ws, (unsigned)(WS_SYNC + SYNC_BYTES)), lane < 32 ? (unsigned)(WS_HL + row * D * 2) : (unsigned)(WS_SYNC + (SY_ST + row) * 8), 0, lds + LDS_DUMMY);
    LOAD_TAG();
    bar();
    {
      const rsrc_t rs = mk_rsrc(a.w1, S * F * 2);
#pragma unroll
      for (int j = 0; j < 2; ++j) {
        const int piece = j == 0 ? wid : (wid < 6 ? wid + 8 : wid);
        const unsigned vo = w1_voff(piece);
#pragma unroll
        for (int t = 0; t < B1_NS - 1; ++t) dma16(rs, vo, t * (32 * F * 2), lds + t * B1_STAGE + piece * 1024);
      }
    }
    wait_vm<17>();
    asm volatile("" : "+v"(r[0][0]), "+v"(r[0][1]), "+v"(r[1][0]), "+v"(r[1][1])::"memory");
    const rsrc_t rx = mk_rsrc(a.ws + WS_HL, S * D * 2);
    f32x2 v[2][8];
#pragma unroll
    for (int i = 0; i < 2; ++i)
#pragma unroll
      for (int k = 0; k < 4; ++k) { v[i][k] = (f32x2){bflo(r[i][0][k]), bfhi(r[i][0][k])}; v[i][4 + k] = (f32x2){bflo(r[i][1][k]), bfhi(r[i][1][k])}; }
#pragma unroll
    for (int pass = 0; pass < 2; ++pass) {
      float mean[2], var[2];
#pragma unroll
      for (int i = 0; i < 2; ++i) {
        f32x2 s = ((v[i][0] + v[i][1]) + (v[i][2] + v[i][3])) + ((v[i][4] + v[i][5]) + (v[i][6] + v[i][7]));
        mean[i] = wave_sum(s.x + s.y) * (1.0f / D);
      }
#pragma unroll
      for (int i = 0; i < 2; ++i) {
        f32x2 qq = {0.f, 0.f};
#pragma unroll
        for (int k = 0; k < 8; ++k) { const f32x2 d = v[i][k] - bc2(mean[i]); qq = fma2(d, d, qq); }
        var[i] = wave_sum(qq.x + qq.y) * (1.0f / D);
      }
#pragma unroll
      for (int i = 0; i < 2; ++i) {
        const unsigned ms = pk_bf16(mean[i], sqrtf(rbf(rbf(var[i]) + 1e-5f)));
        const float mb = bflo(ms), rs_ = 1.0f / bfhi(ms);
        u32x4 o0, o1;
#pragma unroll
        for (int k = 0; k < 8; ++k) {
          const f32x2 d = v[i][k] - bc2(mb);
          const unsigned dd = pk_bf16(d.x, d.y);
          const f32x2 e = (f32x2){bflo(dd), bfhi(dd)} * bc2(rs_);
          const unsigned w = pk_bf16(e.x, e.y);
          v[i][k] = (f32x2){bflo(w), bfhi(w)};
          if (k < 4) o0[k] = w; else o1[k - 4] = w;
        }
        if (pass == 0) {
          if (lane == 0) __hip_atomic_store(sy + SY_ST + row + i, ((u64)tag32 << 32) | ms, RLX_AGENT);
        } else {
          st16_wt(rx, (unsigned)lane * 16u, (unsigned)((row + i) * D) * 2u, o0);
          st16_wt(rx, (unsigned)lane * 16u + 1024u, (unsigned)((row + i) * D) * 2u, o1);
        }
      }
    }
    STAMP(1);
    asm volatile("s_waitcnt vmcnt(0)" : "+v"(brow[0][0]), "+v"(brow[0][1]), "+v"(brow[1][0]), "+v"(brow[1][1])::"memory");
    bar();
    if (wid == 0 && lane == 0) __hip_atomic_store(sy + SY_LN + ri, tag, RLX_AGENT);
    STAMP(2);
    {
      typedef __bf16 bf16x2 __attribute__((ext_vector_type(2)));
      const bf16x2 ones = __builtin_bit_cast(bf16x2, 0x3f803f80u);
      float sums[2];
#pragma unroll
      for (int i = 0; i < 2; ++i) {
        float s = 0.f;
#pragma unroll
        for (int k = 0; k < 8; ++k) { const unsigned w = brow[i][k >> 2][k & 3]; s = __builtin_amdgcn_fdot2_f32_bf16(__builtin_bit_cast(bf16x2, w), ones, s, false); }
        sums[i] = wave_sum(s);
      }
      if (lane < 2) __hip_atomic_store(sy + SY_SB + row + lane, ((u64)tag32 << 32) | __float_as_uint(lane ? sums[1] : sums[0]), RLX_AGENT);
    }
  }

  if (wid >= 4) {
    const int pw = wid - 4;
    if (pw == 3) {
      auto flags_fetch = [&]() {
        dma16c(rsy, (unsigned)lane * 16u, SY_LN * 8, lds + LDS_FLG);
        dma16c(rsy, (unsigned)lane * 16u, SY_LN * 8 + 1024, lds + LDS_FLG + 1024);
      };
      auto flags_check = [&]() {
        bool ok = true;
#pragma unroll
        for (int k = 0; k < 4; ++k) ok &= *(LAS u64*)(lds + LDS_FLG + k * 512 + lane * 8) == tag;
        if (!__all(ok)) wait_flags<4>(sy + SY_LN + lane, true, tag);
      };
      if (lane < 4) *(LAS unsigned*)(size_t)(LDS_CNT + lane * 4) = 0u;
      dma4(mk_rsrc((const void*)(pc0 & ~255ull), CODE_PF_BYTES), (unsigned)(lane * 128), (unsigned)((idx % 5) * 8192), lds + LDS_DUMMY);
      if (roleA) {
        st16_wt(rsy, (unsigned)lane * 16u, (unsigned)(SY_GR * 8 + (chunk * P + cb * 64) * 16), (u32x4){0u, 0u, 0u, 0u});
        dma4(mk_rsrc(a.Lre, P * 2), (unsigned)(lane & 31) * 4u, cb * 128, lds + LDS_RAW + 3072);
        dma4(mk_rsrc(a.Lim, P * 2), (unsigned)(lane & 31) * 4u, cb * 128, lds + LDS_RAW + 3072 + 256);
        dma4(mk_rsrc(a.logdt, P * 2), (unsigned)(lane & 31) * 4u, cb * 128, lds + LDS_RAW + 3072 + 512);
        LOAD_TAG(); HOLD_TAG();
        const int bst = (ri * PF_NST) >> 7;
        auto step = [&](int t) { bar(); if (t == bst) touch(a.w1, S * F * 2, 128 + 13 * ri, 13); };
        step(0); step(1); step(2);
        if (bst <= 2) wait_vm<13>(); else wait_vm<0>();
        if (lane == 0) __hip_atomic_store(sy + SY_LN + 128 + ri, tag, RLX_AGENT);
        LAS float* cf = (LAS float*)(lds + LDS_COEF) + lane;
        const float lr = bf2f(*(LAS u16*)(lds + LDS_RAW + 3072 + lane * 2)), li = bf2f(*(LAS u16*)(lds + LDS_RAW + 3072 + 256 + lane * 2));
        const float dtt = rbf(expf(bf2f(*(LAS u16*)(lds + LDS_RAW + 3072 + 512 + lane * 2)))), er = expf(dtt * lr);
        float arg = dtt * li;
        asm volatile("" : "+v"(arg));
        step(3);
        float sn, cs;
        sincosf(arg, &sn, &cs);
        float pr = er * cs, pi = er * sn;
        asm volatile("" : "+v"(pr), "+v"(pi));
        step(4);
        const float nr = pr - 1.0f, den = lr * lr + li * li;
        cf[0] = pr; cf[64] = pi;
        cf[128] = 2.0f * (nr * lr + pi * li) / den; cf[192] = 2.0f * (pi * lr - nr * li) / den;
        step(5);
#pragma unroll
        for (int k = 0; k < 8; ++k) {
          const float t = pr * pr - pi * pi; pi = 2.0f * pr * pi; pr = t;
          if (k >= 3) { cf[(4 + 2 * (k - 3)) * 64] = pr; cf[(5 + 2 * (k - 3)) * 64] = pi; }
        }
#pragma unroll 1
        for (int t = 6; t < 12; ++t) step(t);
        const unsigned sbo = (unsigned)(SY_SB * 8 + cb * 512 + (lane >> 5) * (P * 8) + (lane & 31) * 16);
#pragma unroll 1
        for (unsigned spins = 0;; ++spins) {
          dma16c(rsy, (unsigned)lane * 16u, (unsigned)(SY_ST * 8 + chunk * 2048), lds + LDS_RAW);
          dma16c(rsy, (unsigned)lane * 16u, (unsigned)(SY_ST * 8 + chunk * 2048 + 1024), lds + LDS_RAW + 1024);
          dma16c(rsy, sbo, 0, lds + LDS_RAW + 2048);
          if (spins == 0) { flags_fetch(); step(12); step(13); }
          wait_vm<0>();
          u32x2 gs[4], gb[2];
          bool ok = true;
#pragma unroll
          for (int k = 0; k < 4; ++k) { gs[k] = *(LAS u32x2*)(lds + LDS_RAW + k * 512 + lane * 8); ok &= gs[k][1] == tag32; }
#pragma unroll
          for (int k = 0; k < 2; ++k) { gb[k] = *(LAS u32x2*)(lds + LDS_RAW + 2048 + k * 512 + lane * 8); ok &= gb[k][1] == tag32; }
          if (__all(ok) || spins > SPIN_CAP) {
#pragma unroll
            for (int k = 0; k < 4; ++k) {
              LAS float* e = (LAS float*)(lds + LDS_STAT + (2 * k + (lane >> 5)) * 272 + (lane & 15) * 16 + ((lane >> 4) & 1) * 4);
              e[0] = -bflo(gs[k][0]); e[2] = 1.0f / bfhi(gs[k][0]);
            }
#pragma unroll
            for (int k = 0; k < 2; ++k) *(LAS unsigned*)(lds + LDS_SB + k * 256 + lane * 4) = gb[k][0];
            break;
          }
          __builtin_amdgcn_s_sleep(2);
        }
        bar();
        flags_check();
        wait_lgkm0();
        step(15);
      } else {
#pragma unroll 1
        for (int t = 0; t < B1_NT; ++t) {
          if (t == 20) {
            wait_vm<0>();
            flags_check();
            if (bid == 255 && lane == 0) __hip_atomic_store(sy, tag, RLX_AGENT);
          }
          bar();
          TRACE(t);
          if (t == 12) flags_fetch();
        }
      }
    } else if (roleA) {
      const int T0 = chunk * 256, p0 = cb * 64;
      if (pw < 2) {
        unsigned voff[16];
#pragma unroll
        for (int j = 0; j < 16; ++j) {
          const int rho = 8 * j + (lane >> 3), m = rho >> 4, i = rho & 15;
          const int tok = T0 + 128 * pw + 32 * (i >> 2) + 16 * (i & 1) + 2 * m + ((i >> 1) & 1);
          const int c = (lane & 7) ^ ((rho >> 1) & 7);
          voff[j] = (unsigned)(tok * D + c * 8) * 2u;
        }
        const rsrc_t rs = mk_rsrc(a.x, S * D * 2);
        auto issue = [&](int t, int slot) {
          LAS char* l = lds + slot * A1_STAGE + pw * 16384;
#pragma unroll
          for (int j = 0; j < 16; ++j) dma16(rs, voff[j], t * 128, l + j * 1024);
        };
        prod_prologue<A1_NS>(issue);
        LOAD_TAG(); HOLD_TAG();
        prod_loop<16, A1_NS>(A1_NT, issue);
      } else {
        unsigned voff[8];
#pragma unroll
        for (int j = 0; j < 8; ++j) {
          const int wn = j >> 2, q = (j >> 1) & 1, hf = j & 1, i = hf * 8 + (lane >> 3);
          const int st = p0 + 32 * wn + 16 * q + i;
          const int c = (lane & 7) ^ ((i >> 1) & 7);
          voff[j] = (unsigned)(st * D + c * 8) * 2u;
        }
        const rsrc_t rre = mk_rsrc(a.Bre, P * D * 2), rim = mk_rsrc(a.Bim, P * D * 2);
        auto issue = [&](int t, int slot) {
          LAS char* l = lds + slot * A1_STAGE + 32768;
#pragma unroll
          for (int j = 0; j < 8; ++j) {
            dma16(rre, voff[j], t * 128, l + (j >> 1) * 4096 + (j & 1) * 1024);
            dma16(rim, voff[j], t * 128, l + (j >> 1) * 4096 + (j & 1) * 1024 + 2048);
          }
        };
        prod_prologue<A1_NS>(issue);
        LOAD_TAG(); HOLD_TAG();
        prod_loop<16, A1_NS, true>(A1_NT, issue);
      }
    } else {
      const int dd0 = dt1 * 128;
      if (pw < 2) {
        unsigned voff[7];
#pragma unroll
        for (int j = 0; j < 7; ++j) voff[j] = w1_voff(pw * 7 + j);
        const rsrc_t rs = mk_rsrc(a.w1, S * F * 2);
        auto issue = [&](int t, int slot) {
          LAS char* l = lds + slot * B1_STAGE + pw * 7168;
#pragma unroll
          for (int j = 0; j < 7; ++j) dma16(rs, voff[j], t * (32 * F * 2), l + j * 1024);
        };
        prod_loop<7, B1_NS>(B1_NT, issue);
      } else {
        unsigned voff[2];
#pragma unroll
        for (int j = 0; j < 2; ++j) {
          const int k = j * 8 + (lane >> 4), pos = lane & 15;
          const int c = pos ^ (((k & 3) << 1) | (((k >> 3) & 1) << 3));
          voff[j] = (unsigned)((lane >> 4) * D + dd0 + c * 8) * 2u;
        }
        const rsrc_t rs = mk_rsrc(a.ws + WS_HL, S * D * 2);
        auto issue = [&](int t, int slot) {
          LAS char* l = lds + slot * B1_STAGE + B1_ABYTES;
#pragma unroll
          for (int j = 0; j < 8; ++j) dma16c(rs, voff[(j >> 1) & 1], t * (32 * D * 2) + j * 4 * (D * 2), l + j * 1024);
        };
        wait_flags<1>(sy + SY_LN + lane, true, tag);
        PSTAMP(6, 16);
        prod_prologue<B1_NS>(issue);
        prod_loop<8, B1_NS>(B1_NT, issue);
      }
    }
    bar();
    if (pw == 0) {
      const unsigned r0 = (unsigned)(d0 + (lane >> 3));
      const unsigned vce = r0 * (P * 2) + (unsigned)(((lane & 7) ^ (lane >> 4)) * 16), vco = vce ^ 64u;
      const int r4 = lane >> 4, hx = roleA ? 0 : 8;
      const unsigned vu = (unsigned)((lane >> 2) * 64 + (((lane & 3) ^ (((r4 & 1) << 1) ^ ((r4 >> 1) * 3))) * 16));
      const rsrc_t rcre = mk_rsrc(a.Cre, D * P * 2), rcim = mk_rsrc(a.Cim, D * P * 2), ru = mk_rsrc(a.ws + WS_U, D * F * 2);
      auto issue = [&](int t, int slot) {
        LAS char* l = lds + slot * P2_STAGE;
        if (t < nC) {
          const int kt = kc0 + t;
          const rsrc_t rs = kt < 16 ? rcre : rcim;
#pragma unroll
          for (int j = 0; j < 16; ++j) dma16(rs, (j & 1) ? vco : vce, (kt & 15) * 128 + (j ^ hx) * 8 * (P * 2), l + j * 1024);
        } else {
          if (t == nC) { wait_flags<1>(sy + SY_U + dt * 16 + (lane & 15), true, tag); PSTAMP(4, 19); }
          const int kt = ku0 + t - nC;
#pragma unroll
          for (int j = 0; j < 16; ++j) dma16c(ru, vu, ((2 * kt + (j & 1)) * D + d0 + 16 * ((j ^ hx) >> 1)) * 64, l + j * 1024);
        }
      };
      prod_prologue<P2_NS>(issue);
      prod_loop<16, P2_NS>(nt2, issue);
    } else if (pw < 3) {
      unsigned voff[2];
#pragma unroll
      for (int j = 0; j < 2; ++j) {
        const int k = j * 8 + (lane >> 4), pos = lane & 15;
        const int c = pos ^ (((k & 3) << 1) | (((k >> 3) & 1) << 3));
        voff[j] = (unsigned)(((pw - 1) * 32 + (lane >> 4)) * S + t0 + c * 8) * 2u;
      }
      const unsigned vh = (unsigned)(lane * 16 + (pw - 1) * 8192);
      const rsrc_t rh = mk_rsrc(a.ws + WS_HT, 2 * P * S * 2), rw = mk_rsrc(a.w2, F * S * 2);
      auto issue = [&](int t, int slot) {
        LAS char* l = lds + slot * P2_STAGE + 16384 + (pw - 1) * 8192;
        if (t < nC) {
#pragma unroll
          for (int j = 0; j < 8; ++j) dma16c(rh, vh, (tt * 32 + kc0 + t) * 16384 + j * 1024, l + j * 1024);
        } else {
#pragma unroll
          for (int j = 0; j < 8; ++j) dma16(rw, voff[(j >> 1) & 1], (ku0 + t - nC) * (64 * S * 2) + j * 4 * (S * 2), l + j * 1024);
        }
      };
      if (roleA) lds_wait(LDS_CNT, 4u);
      wait_flags<1>(sy + SY_H + (tt >> 1) * 16 + (lane & 15), true, tag);
      PSTAMP(6, 17);
      prod_prologue<P2_NS>(issue);
      prod_loop<8, P2_NS>(nt2, issue);
    } else {
      constexpr int UA = NBA * 32, WB = PF_WB * 128 / 2;
      if (roleA) {
        touch(a.Cre, D * P * 2, 2 * ri, 2); touch(a.Cim, D * P * 2, 2 * ri, 2);
      } else {
        __builtin_amdgcn_s_sleep(PF_DELAY2);
        touch(a.w2, F * S * 2, UA + (PF_WB / 2) * ri, PF_WB / 2); touch(a.w2, F * S * 2, (PF_WB / 2) * ri, PF_WB / 2);
      }
      constexpr int RA = (UA - WB + 127) / 128, RB = (1792 - UA - WB + 127) / 128;
      const int bst = roleA ? (ri * PF_NST2) >> 7 : -1;
#pragma unroll 1
      for (int t = 0; t < nt2; ++t) {
        bar();
        TRACE(64 + t);
        if (t == bst) { touch(a.w2, F * S * 2, UA + WB + RB * ri, RB); touch(a.w2, F * S * 2, WB + RA * ri, RA); }
        const int rk = t - (nt2 - 13);
        if (rk >= 0 && rk < 8) {
          const rsrc_t rxx = mk_rsrc(a.x, S * D * 2);
#pragma unroll
          for (int k = 0; k < 2; ++k) dma16(rxx, (unsigned)((lane >> 3) * (D * 2) + ((lane & 7) ^ (lane >> 3)) * 16), (unsigned)((t0 + 16 * rk + 8 * k) * D + d0 + (roleA ? 0 : 64)) * 2u, lds + LDS_RES + (2 * rk + k) * 1024);
        }
        if (rk == 8) {
          dma16c(rsy, (unsigned)lane * 16u, (unsigned)(SY_ST + t0) * 8u, lds + LDS_RES + 16384);
          dma4(mk_rsrc(a.b2, S * 2), (unsigned)lane * 4u, (unsigned)t0 * 2u, lds + LDS_RES + 17408);
          dma4(mk_rsrc(a.Dsk, D * 2), (unsigned)(lane & 31) * 4u, (unsigned)(d0 + (roleA ? 0 : 64)) * 2u, lds + LDS_RES + 17664);
        }
        if (t == nt2 - 2) wait_vm<0>();
      }
      TRACE_DUMP();
    }
    return;
  }

  const int wm = wid >> 1, wn = wid & 1;
  int cl = lane & 15, g = lane >> 4, q4 = cl >> 2, p4 = lane & 3, ln = lane;
#define RELANE() do { ln = __builtin_amdgcn_mbcnt_hi(~0u, __builtin_amdgcn_mbcnt_lo(~0u, 0u)); asm volatile("" : "+v"(ln)); cl = ln & 15; g = ln >> 4; q4 = cl >> 2; p4 = ln & 3; } while (0)
  const int kh = roleA ? 0 : 1, pa = bid ^ 1;

  if (roleA) {
    LOAD_TAG(); HOLD_TAG();
    RELANE();
    const int p0 = cb * 64;
    const unsigned rb = (unsigned)(cl * 128 + ((g ^ (cl >> 1)) * 16));
    unsigned pa0 = rb + wm * 16384, pa1 = pa0 ^ 64u, pb0 = rb + 32768 + wn * 8192, pb1 = pb0 ^ 64u;
    f32x4 acc[8][4];
#pragma unroll
    for (int m = 0; m < 8; ++m)
#pragma unroll
      for (int n = 0; n < 4; ++n) acc[m][n] = (f32x4){0.f, 0.f, 0.f, 0.f};
    bf16x8 xa[8], xb[4], ya[8], yb[4];
    auto rd = [&](bf16x8* fa, bf16x8* fb, unsigned pa_, unsigned pb_, int r) {
      if (r == 0) fa[0] = lds_rd128(pa_); else if (r < 5) fb[r - 1] = lds_rd128(pb_ + (r - 1) * 2048); else fa[r - 4] = lds_rd128(pa_ + (r - 4) * 2048);
    };
    int slot = 0, dl = 0;
    bar();
    STAMP(3);
#pragma unroll
    for (int r = 0; r < 12; ++r) rd(xa, xb, pa0, pb0, r);
#pragma unroll 1
    for (int t = 0; t < A1_NT; ++t) {
      SCHED_FENCE(); wait_lgkm0(); SCHED_FENCE();
      phase<32>(acc, xa, xb, [&](int i) {
        if (i % A1_RS == 0 && i / A1_RS < 12) rd(ya, yb, pa1, pb1, i / A1_RS);
        if (i == 25) { dl = (slot == A1_NS - 1) ? -(A1_NS - 1) * A1_STAGE : A1_STAGE; slot = (slot == A1_NS - 1) ? 0 : slot + 1; }
        if (i == 27) { pa0 += dl; pb0 += dl; asm volatile("" : "+v"(pa0), "+v"(pb0)); }
        if (i == 29) { pa1 += dl; pb1 += dl; asm volatile("" : "+v"(pa1), "+v"(pb1)); }
      });
      wait_lgkm0();
      bar();
      SCHED_FENCE();
      phase<32>(acc, ya, yb, [&](int i) { if (i % A1_RS == 0 && i / A1_RS < 12) rd(xa, xb, pa0, pb0, i / A1_RS); });
    }
    RELANE();
    STAMP(4);
    float cf[2][14], sb[4];
#pragma unroll
    for (int q = 0; q < 2; ++q) {
#pragma unroll
      for (int k = 0; k < 14; ++k) cf[q][k] = ((LAS float*)(lds + LDS_COEF))[k * 64 + 32 * wn + 16 * q + cl];
      sb[2 * q] = ((LAS float*)(lds + LDS_SB))[32 * wn + 16 * q + cl]; sb[2 * q + 1] = ((LAS float*)(lds + LDS_SB))[64 + 32 * wn + 16 * q + cl];
    }
    f32x2 er[2], ei[2];
#pragma unroll
    for (int q = 0; q < 2; ++q) { er[q] = bc2(0.f); ei[q] = bc2(0.f); }
#pragma unroll
    for (int m = 0; m < 8; ++m)
#pragma unroll
      for (int jj = 0; jj < 2; ++jj) {
        const f32x4 st = *(LAS f32x4*)(lds + LDS_STAT + (4 * wm + g) * 272 + (2 * m + jj) * 16);
        const f32x2 c2 = {st[0], st[1]}, r2 = {st[2], st[3]};
#pragma unroll
        for (int q = 0; q < 2; ++q) {
          const f32x2 ur = fma2(c2, bc2(sb[2 * q]), (f32x2){acc[m][2 * q][2 * jj], acc[m][2 * q][2 * jj + 1]}) * r2;
          const f32x2 ui = fma2(c2, bc2(sb[2 * q + 1]), (f32x2){acc[m][2 * q + 1][2 * jj], acc[m][2 * q + 1][2 * jj + 1]}) * r2;
          acc[m][2 * q][2 * jj] = ur.x; acc[m][2 * q][2 * jj + 1] = ur.y; acc[m][2 * q + 1][2 * jj] = ui.x; acc[m][2 * q + 1][2 * jj + 1] = ui.y;
          const f32x2 nr = fma2(bc2(cf[q][0]), er[q], fma2(bc2(-cf[q][1]), ei[q], ur));
          const f32x2 ni = fma2(bc2(cf[q][0]), ei[q], fma2(bc2(cf[q][1]), er[q], ui));
          er[q] = nr; ei[q] = ni;
        }
        if (jj == 1 && (m & 1)) asm volatile("" : "+v"(er[0]), "+v"(ei[0]), "+v"(er[1]), "+v"(ei[1]));
      }
    float sr[2], si[2], pr_[2], pi_[2];
    LAS float* tot = (LAS float*)(lds + LDS_TOT);
#pragma unroll
    for (int q = 0; q < 2; ++q) {
      float tr, ti;
      CMUL(tr, ti, cf[q][4], cf[q][5], er[q].x, ei[q].x);
      sr[q] = tr + er[q].y; si[q] = ti + ei[q].y;
      float vr = __shfl_up(sr[q], 16), vi = __shfl_up(si[q], 16);
      CMUL(tr, ti, cf[q][6], cf[q][7], vr, vi);
      if (g >= 1) { sr[q] += tr; si[q] += ti; }
      vr = __shfl_up(sr[q], 32); vi = __shfl_up(si[q], 32);
      CMUL(tr, ti, cf[q][8], cf[q][9], vr, vi);
      if (g >= 2) { sr[q] += tr; si[q] += ti; }
      vr = __shfl_up(sr[q], 16); vi = __shfl_up(si[q], 16);
      pr_[q] = g >= 1 ? vr : 0.f; pi_[q] = g >= 1 ? vi : 0.f;
      if (wm == 0 && g == 3) *(LAS f32x2*)(tot + (32 * wn + 16 * q + cl) * 2) = (f32x2){sr[q], si[q]};
    }
    if (wm == 0) {
      wait_lgkm0();
      if (ln == 0) __hip_atomic_store((LAS unsigned*)(size_t)(LDS_CNT + 4 + 4 * wn), 1u, __ATOMIC_RELAXED, __HIP_MEMORY_SCOPE_WORKGROUP);
    } else {
      lds_wait(LDS_CNT + 4 + 4 * wn, 1u);
    }
    f32x2 tq[2];
    gu64* gran = sy + SY_GR;
#pragma unroll
    for (int q = 0; q < 2; ++q) {
      tq[q] = *(LAS f32x2*)(tot + (32 * wn + 16 * q + cl) * 2);
      if (wm == 1 && g == 3 && chunk < 7) {
        float tr, ti;
        CMUL(tr, ti, cf[q][10], cf[q][11], tq[q].x, tq[q].y);
        gu64* gp = gran + ((size_t)chunk * P + p0 + 32 * wn + 16 * q + cl) * 2;
        __hip_atomic_store(gp, (1ull << 32) | __float_as_uint(tr + sr[q]), RLX_AGENT);
        __hip_atomic_store(gp + 1, (1ull << 32) | __float_as_uint(ti + si[q]), RLX_AGENT);
      }
    }
    STAMP(5);
#pragma unroll
    for (int m = 0; m < 8; ++m)
#pragma unroll
      for (int q = 0; q < 2; ++q) {
        const f32x4 ur = acc[m][2 * q], ui = acc[m][2 * q + 1];
        acc[m][2 * q] = ur * cf[q][2] - ui * cf[q][3];
        acc[m][2 * q + 1] = ui * (-cf[q][2]) - ur * cf[q][3];
        asm volatile("" : "+v"(acc[m][2 * q]), "+v"(acc[m][2 * q + 1]));
      }
    float hr[2] = {0.f, 0.f}, hi[2] = {0.f, 0.f};
    if (chunk > 0) {
      float gr[2][2], gi[2][2];
#pragma unroll 1
      for (unsigned spins = 0;; ++spins) {
        bool ok = true;
#pragma unroll
        for (int q = 0; q < 2; ++q)
#pragma unroll
          for (int k = 0; k < 2; ++k) {
            const int cc = (g + 4 * k < chunk) ? g + 4 * k : 0;
            gu64* gp = gran + ((size_t)cc * P + p0 + 32 * wn + 16 * q + cl) * 2;
            const u64 vr = __hip_atomic_load(gp, RLX_AGENT), vi = __hip_atomic_load(gp + 1, RLX_AGENT);
            ok &= ((vr >> 32) == 1ull) & ((vi >> 32) == 1ull);
            gr[q][k] = __uint_as_float((unsigned)vr); gi[q][k] = __uint_as_float((unsigned)vi);
          }
        if (__all(ok) || spins > SPIN_CAP) break;
        __builtin_amdgcn_s_sleep(1);
      }
#pragma unroll
      for (int q = 0; q < 2; ++q) {
        float ar_ = 0.f, ai_ = 0.f;
#pragma unroll
        for (int k = 0; k < 2; ++k) {
          float vr = gr[q][k], vi = gi[q][k];
          const int e = chunk - 1 - (g + 4 * k);
#pragma unroll
          for (int i = 0; i < 6 - 4 * k; ++i) {
            float tr, ti;
            CMUL(tr, ti, cf[q][12], cf[q][13], vr, vi);
            vr = i < e ? tr : vr; vi = i < e ? ti : vi;
          }
          ar_ += e >= 0 ? vr : 0.f; ai_ += e >= 0 ? vi : 0.f;
        }
        ar_ += __shfl_xor(ar_, 16); ai_ += __shfl_xor(ai_, 16);
        ar_ += __shfl_xor(ar_, 32); ai_ += __shfl_xor(ai_, 32);
        hr[q] = ar_; hi[q] = ai_;
      }
    }
    STAMP(6);
    f32x2 wr[2], wi[2];
#pragma unroll
    for (int q = 0; q < 2; ++q) {
      float tr, ti;
      if (wm == 1) { CMUL(tr, ti, cf[q][10], cf[q][11], hr[q], hi[q]); hr[q] = tr + tq[q].x; hi[q] = ti + tq[q].y; }
      CMUL(tr, ti, cf[q][6], cf[q][7], hr[q], hi[q]);
      if (g & 1) { hr[q] = tr; hi[q] = ti; }
      CMUL(tr, ti, cf[q][8], cf[q][9], hr[q], hi[q]);
      if (g & 2) { hr[q] = tr; hi[q] = ti; }
      hr[q] += pr_[q]; hi[q] += pi_[q];
      CMUL(tr, ti, cf[q][4], cf[q][5], hr[q], hi[q]);
      const f32x2 h0r = {hr[q], tr + er[q].x}, h0i = {hi[q], ti + ei[q].x};
      wr[q] = h0r * cf[q][2] - h0i * cf[q][3];
      wi[q] = h0i * (-cf[q][2]) - h0r * cf[q][3];
    }
    LAS char* scr = lds + wid * P2_STAGE + 16384;
    const unsigned fsw = (unsigned)(((cl & 3) << 1) | (((cl >> 3) & 1) << 3));
#pragma unroll
    for (int mh = 0; mh < 2; ++mh) {
      u32x4 o[2][2][2];
#pragma unroll
      for (int mq = 0; mq < 4; ++mq) {
        const int m = 4 * mh + mq;
#pragma unroll
        for (int q = 0; q < 2; ++q) {
          const f32x2 u0r = {acc[m][2 * q][0], acc[m][2 * q][1]}, u0i = {acc[m][2 * q + 1][0], acc[m][2 * q + 1][1]};
          const f32x2 u1r = {acc[m][2 * q][2], acc[m][2 * q][3]}, u1i = {acc[m][2 * q + 1][2], acc[m][2 * q + 1][3]};
          const f32x2 ar2 = bc2(cf[q][0]), ai2 = bc2(cf[q][1]);
          const f32x2 n0r = fma2(ar2, wr[q], fma2(ai2, wi[q], u0r)), n0i = fma2(ar2, wi[q], fma2(-ai2, wr[q], u0i));
          const f32x2 n1r = fma2(ar2, n0r, fma2(ai2, n0i, u1r)), n1i = fma2(ar2, n0i, fma2(-ai2, n0r, u1i));
          wr[q] = n1r; wi[q] = n1i;
          o[q][0][0][mq] = pk_bf16(n0r.x, n1r.x); o[q][0][1][mq] = pk_bf16(n0r.y, n1r.y);
          o[q][1][0][mq] = pk_bf16(n0i.x, n1i.x); o[q][1][1][mq] = pk_bf16(n0i.y, n1i.y);
        }
      }
#pragma unroll
      for (int q = 0; q < 2; ++q)
#pragma unroll
        for (int part = 0; part < 2; ++part)
#pragma unroll
          for (int run = 0; run < 2; ++run)
            *(LAS u32x4*)(scr + (part * 32 + 16 * q + cl) * 256 + (((unsigned)(4 * g + 2 * run + mh) ^ fsw) * 16)) = o[q][part][run];
    }
    const rsrc_t rh = mk_rsrc(a.ws + WS_HT, 2 * P * S * 2);
#pragma unroll
    for (int i = 0; i < 16; ++i) {
      const u32x4 v = *(LAS u32x4*)(scr + i * 1024 + ln * 16);
      st16_wt(rh, (unsigned)(ln * 16 + (i & 7) * 1024), (unsigned)(((2 * chunk + wm) * 32 + (i >> 3) * 16 + cb) * 16384 + wn * 8192), v);
    }
    STAMP(7);
    publish(LDS_CNT, sy + SY_H + chunk * 16 + cb, tag, ln);
    STAMP(8);
  } else {
    RELANE();
    const int f0 = ft * 224, dd0 = dt1 * 128;
    const int k0 = 8 * g + q4;
    unsigned pa0 = (unsigned)(k0 * 448 + ((12 * wm + p4) ^ (3 * (g & 1))) * 16 + (p4 & 1) * 8);
    unsigned pa1 = (unsigned)(k0 * 448 + ((12 * wm + p4) ^ (3 * (g & 1))) * 16 + ((p4 & 1) ^ 1) * 8);
    unsigned pa2 = (unsigned)(k0 * 448 + ((24 + 2 * wm + (p4 >> 1)) ^ (3 * (g & 1))) * 16 + (p4 & 1) * 8);
    unsigned pb[4];
#pragma unroll
    for (int n = 0; n < 4; ++n) pb[n] = (unsigned)(B1_ABYTES + k0 * 256 + ((8 * wn + 2 * n + (p4 >> 1)) ^ ((q4 << 1) | ((g & 1) << 3))) * 16 + (p4 & 1) * 8);
    f32x4 acc[7][4];
#pragma unroll
    for (int m = 0; m < 7; ++m) {
      const u32x2 bv = *(const u32x2*)(a.b1 + f0 + (m < 6 ? 96 * wm + 32 * (m >> 1) + 8 * g + 4 * ((m & 1) ^ (g & 1)) : 192 + 16 * wm + 4 * g));
#pragma unroll
      for (int n = 0; n < 4; ++n) acc[m][n] = (f32x4){bflo(bv[0]), bfhi(bv[0]), bflo(bv[1]), bfhi(bv[1])};
    }
    TrFrag xa[7], xb[4], ya[7], yb[4];
    int slot = 0, dl = 0;
    auto side = [&](TrFrag* fa, TrFrag* fb, int i) {
      const int f = i >> 1, h = i & 1;
      if (f == 0) fa[0].h[h] = lds_rdtr(pa0 + h * 1792);
      else if (f < 5) fb[f - 1].h[h] = lds_rdtr(pb[f - 1] + h * 1024);
      else if (f < 10) fa[f - 4].h[h] = lds_rdtr((((f - 4) & 1) ? pa1 : pa0) + ((f - 4) >> 1) * 64 + h * 1792);
      else if (f == 10) fa[6].h[h] = lds_rdtr(pa2 + h * 1792);
      else if (i == 22) { dl = (slot == B1_NS - 1) ? -(B1_NS - 1) * B1_STAGE : B1_STAGE; slot = (slot == B1_NS - 1) ? 0 : slot + 1; }
      else if (i == 23) { pa0 += dl; pb[0] += dl; asm volatile("" : "+v"(pa0), "+v"(pb[0])); }
      else if (i == 24) { pb[1] += dl; pb[2] += dl; asm volatile("" : "+v"(pb[1]), "+v"(pb[2])); }
      else if (i == 25) { pb[3] += dl; pa1 += dl; asm volatile("" : "+v"(pb[3]), "+v"(pa1)); }
      else if (i == 26) { pa2 += dl; asm volatile("" : "+v"(pa2)); }
    };
    STAMP(15);
    bar();
    STAMP(3);
#pragma unroll
    for (int i = 0; i < 28; ++i) side(xa, xb, i);
#pragma unroll 1
    for (int t = 0; t < B1_NT; t += 2) {
      SCHED_FENCE(); wait_lgkm0(); bar(); SCHED_FENCE();
      phase<28>(acc, xa, xb, [&](int i) { side(ya, yb, i); });
      wait_lgkm0(); bar(); SCHED_FENCE();
      phase<28>(acc, ya, yb, [&](int i) { side(xa, xb, i); });
    }
    RELANE();
    STAMP(4);
    const rsrc_t ru = mk_rsrc(a.ws + WS_U, D * F * 2);
#pragma unroll
    for (int n = 0; n < 4; ++n) {
      const unsigned vo = (unsigned)((dd0 + 64 * wn + 16 * n + cl) * 64);
      u32x2 w[7];
#pragma unroll
      for (int m = 0; m < 7; ++m) {
        const f32x2 v0 = gelu_tanh2((f32x2){acc[m][n][0], acc[m][n][1]}), v1 = gelu_tanh2((f32x2){acc[m][n][2], acc[m][n][3]});
        w[m][0] = pk_bf16(v0.x, v0.y); w[m][1] = pk_bf16(v1.x, v1.y);
      }
#pragma unroll
      for (int mm = 0; mm < 3; ++mm) {
        const bool sw = g & 1;
        const u32x2 lo = sw ? w[2 * mm + 1] : w[2 * mm], hi = sw ? w[2 * mm] : w[2 * mm + 1];
        st16_wt(ru, vo + 16 * g, (unsigned)((ft * 7 + 3 * wm + mm) * D * 64), (u32x4){lo[0], lo[1], hi[0], hi[1]});
      }
      st8_wt(ru, vo + 8 * g, (unsigned)((ft * 7 + 6) * D * 64 + 32 * wm), w[6]);
    }
    STAMP(7);
    publish(LDS_CNT, sy + SY_U + dt1 * 16 + ft, tag, ln);
    STAMP(8);
  }

  f32x4 acc[4][4];
#pragma unroll
  for (int m = 0; m < 4; ++m)
#pragma unroll
    for (int n = 0; n < 4; ++n) acc[m][n] = (f32x4){0.f, 0.f, 0.f, 0.f};
  const unsigned au0 = (unsigned)(wm * 4096 + cl * 64 + ((g ^ (((q4 & 1) << 1) ^ ((q4 >> 1) * 3))) * 16));
  unsigned pa0 = (unsigned)((wm * 32 + cl) * 128 + ((g ^ (cl >> 1)) * 16)), pa1 = pa0 ^ 64u;
  const unsigned sw0 = pa0 ^ au0 ^ 65536u, sw1 = pa1 ^ (au0 + 1024u) ^ 65536u;
  unsigned pb[4];
#pragma unroll
  for (int n = 0; n < 4; ++n) pb[n] = (unsigned)(16384 + (8 * g + q4) * 256 + ((8 * wn + 2 * n + (p4 >> 1)) ^ ((q4 << 1) | ((g & 1) << 3))) * 16 + (p4 & 1) * 8);
  bf16x8 xa[4], ya[4];
  TrFrag xb[4], yb[4];
  static_assert(P2_NS == 4 && NAB % 2 == 0 && NBA % 2 == 0 && NAB >= P2_NS, "phase-2 loop: two stages per trip, 64-KB windows");
  auto rd = [&](bf16x8* fa, TrFrag* fb, int so, int ks, int r) {
    const unsigned pa_ = (ks ? pa1 : pa0) + so;
    if (r == 0) fa[0] = lds_rd128(pa_);
    else if (r < 9) fb[(r - 1) >> 1].h[(r - 1) & 1] = lds_rdtr(pb[(r - 1) >> 1] + so + ks * 8192 + ((r - 1) & 1) * 1024);
    else if (r < 12) fa[r - 8] = lds_rd128(pa_ + ((r - 8) >> 1) * 8192 + ((r - 8) & 1) * 2048);
  };
  STAMP(18);
  bar();
  STAMP(9);
#pragma unroll
  for (int r = 0; r < 12; ++r) rd(xa, xb, 0, 0, r);
#pragma unroll 1
  for (int t = 0; t < nt2; t += 2) {
    unsigned x0, x1;
    SCHED_FENCE(); wait_lgkm0(); SCHED_FENCE();
    phase<16>(acc, xa, xb, [&](int i) { rd(ya, yb, 0, 1, i); });
    wait_lgkm0(); bar(); SCHED_FENCE();
    phase<16>(acc, ya, yb, [&](int i) {
      rd(xa, xb, P2_STAGE, 0, i);
      if (i == 13) { const bool sw = (t + 2 == nC); x0 = sw ? sw0 : 65536u; x1 = sw ? sw1 : 65536u; asm volatile("" : "+v"(x0), "+v"(x1)); }
      if (i == 14) { pa0 ^= x0; asm volatile("" : "+v"(pa0)); }
    });
    wait_lgkm0(); SCHED_FENCE();
    phase<16>(acc, xa, xb, [&](int i) {
      rd(ya, yb, P2_STAGE, 1, i);
      if (i == 12) { pb[0] ^= 65536u; pb[1] ^= 65536u; asm volatile("" : "+v"(pb[0]), "+v"(pb[1])); }
      if (i == 13) { pb[2] ^= 65536u; pb[3] ^= 65536u; asm volatile("" : "+v"(pb[2]), "+v"(pb[3])); }
      if (i == 14) { pa1 ^= x1; asm volatile("" : "+v"(pa1)); }
    });
    wait_lgkm0(); bar(); SCHED_FENCE();
    phase<16>(acc, ya, yb, [&](int i) { rd(xa, xb, 0, 0, i); });
  }
  STAMP(10);
  const rsrc_t rxb = mk_rsrc(a.ws + WS_XB, 256 * 32768);
  const unsigned xo = (unsigned)(wid * 8192 + ln * 16);
#pragma unroll
  for (int mi = 0; mi < 2; ++mi)
#pragma unroll
    for (int n = 0; n < 4; ++n) st16_wt(rxb, xo + (mi * 4 + n) * 1024, bid * 32768, __builtin_bit_cast(u32x4, acc[2 + mi][n]));
  wait_vm<0>();
  bar();
  STAMP(11);
  if (wid == 0 && ln == 0) __hip_atomic_store(sy + SY_X + bid, tag, RLX_AGENT);
#pragma unroll
  for (int n = 0; n < 4; ++n) {
    const int tl = 64 * wn + 16 * n + cl;
    const unsigned ms = *(LAS unsigned*)(lds + LDS_RES + 16384 + tl * 8);
    const float mb = bflo(ms), rs_ = 1.0f / bfhi(ms), b2 = bf2f(*(LAS u16*)(lds + LDS_RES + 17408 + tl * 2));
#pragma unroll
    for (int mi = 0; mi < 2; ++mi) {
      const int c8 = 4 * wm + 2 * mi + (g >> 1);
      const u32x2 xv = *(LAS u32x2*)(lds + LDS_RES + tl * 128 + ((c8 ^ (tl & 7)) * 16) + (g & 1) * 8);
      const u32x2 dk = *(LAS u32x2*)(lds + LDS_RES + 17664 + c8 * 16 + (g & 1) * 8);
#pragma unroll
      for (int h = 0; h < 2; ++h) {
        const float x0 = bflo(xv[h]), x1 = bfhi(xv[h]);
        const unsigned dd = pk_bf16(x0 - mb, x1 - mb), xn = pk_bf16(bflo(dd) * rs_, bfhi(dd) * rs_);
        const unsigned sk = pk_bf16(bflo(xn) * bflo(dk[h]), bfhi(xn) * bfhi(dk[h]));
        acc[mi][n][2 * h] += x0 + bflo(sk) + b2; acc[mi][n][2 * h + 1] += x1 + bfhi(sk) + b2;
      }
      asm volatile("" : "+v"(acc[mi][n]));
    }
  }
  if (wid == 0) {
#pragma unroll 1
    for (unsigned spins = 0; spins < SPIN_CAP; ++spins) {
      if (__all(__hip_atomic_load(sy + SY_X + pa, RLX_AGENT) == tag)) break;
      __builtin_amdgcn_s_sleep(1);
    }
  }
  STAMP(12);
  bar();
#pragma unroll
  for (int mi = 0; mi < 2; ++mi)
#pragma unroll
    for (int n = 0; n < 4; ++n) {
      const u32x4 r = __builtin_amdgcn_raw_buffer_load_b128(rxb, xo + (mi * 4 + n) * 1024, pa * 32768, 16);
      acc[mi][n] += __builtin_bit_cast(f32x4, r);
    }
#pragma unroll
  for (int mi = 0; mi < 2; ++mi)
#pragma unroll
    for (int n = 0; n < 4; ++n) {
      const int d = d0 + 64 * kh + 32 * wm + 16 * mi + 4 * g, tok = t0 + 64 * wn + 16 * n + cl;
      *(f32x4*)(a.out + (size_t)tok * D + d) = acc[mi][n];
    }
  STAMP(13);
}

extern "C" void kernel_launch(void* const* d_in, const int* in_sizes, int n_in, void* d_out, int out_size, void* d_ws, size_t ws_size,
                              hipStream_t stream) {
  (void)hipFuncSetAttribute((const void*)fused, hipFuncAttributeMaxDynamicSharedMemorySize, LDS_BYTES);
  Args a;
  a.x = (const u16*)d_in[0]; a.Lre = (const u16*)d_in[1]; a.Lim = (const u16*)d_in[2]; a.Bre = (const u16*)d_in[3]; a.Bim = (const u16*)d_in[4];
  a.Cre = (const u16*)d_in[5]; a.Cim = (const u16*)d_in[6]; a.logdt = (const u16*)d_in[7]; a.Dsk = (const u16*)d_in[8];
  a.w1 = (const u16*)d_in[10]; a.b1 = (const u16*)d_in[11]; a.w2 = (const u16*)d_in[12]; a.b2 = (const u16*)d_in[13];
  a.out = (float*)d_out; a.ws = (char*)d_ws;
  fused<<<256, 512, LDS_BYTES, stream>>>(a);
}
```

```cpp
#include <hip/hip_runtime.h>
#include <stdint.h>

typedef unsigned short u16;
typedef unsigned long long u64;
typedef __bf16 bf16x8 __attribute__((ext_vector_type(8)));
typedef short s16x4 __attribute__((ext_vector_type(4)));
typedef short s16x8 __attribute__((ext_vector_type(8)));
typedef float f32x2 __attribute__((ext_vector_type(2)));
typedef float f32x4 __attribute__((ext_vector_type(4)));
typedef unsigned u32x4 __attribute__((ext_vector_type(4)));
typedef unsigned u32x2 __attribute__((ext_vector_type(2)));
#define LAS __attribute__((address_space(3)))
#define GAS __attribute__((address_space(1)))
typedef GAS unsigned long long gu64;
typedef __amdgpu_buffer_rsrc_t rsrc_t;
#define RLX_AGENT __ATOMIC_RELAXED, __HIP_MEMORY_SCOPE_AGENT

constexpr int S = 2048, D = 1024, P = 1024, F = 3584;
constexpr size_t MB = 1024 * 1024;
constexpr size_t WS_HL = 4 * MB, WS_HT = 8 * MB, WS_U = 16 * MB, WS_XB = 24 * MB, WS_SYNC = 32 * MB;
constexpr int SY_LN = 64, SY_H = 320, SY_U = 448, SY_X = 576, SY_ST = 1024, SY_SB = 3072, SY_GR = 5120;
constexpr unsigned SYNC_BYTES = (SY_GR + 8 * P * 2) * 8;
constexpr unsigned SPIN_CAP = 1u << 17;
#define NBA 22
#define NAB 4
constexpr unsigned CODE_PF_BYTES = 36 * 1024;
#define PF_NST 11
#define PF_ST0 1
#define PF_N1 13
#define PF_NST2 20
#define PF_WB 4
#define A1_RS 2
#define PSTAMP(w, i)
#define STAMP_INIT()
#define STAMP_DUMP()
#define TRACE(e)
#define TRACE_DUMP()
#define STAMP(i) PSTAMP(0, i)

__device__ __forceinline__ float bf2f(u16 v) { return __uint_as_float(((unsigned)v) << 16); }
__device__ __forceinline__ float bflo(unsigned v) { return __uint_as_float(v << 16); }
__device__ __forceinline__ float bfhi(unsigned v) { return __uint_as_float(v & 0xffff0000u); }
__device__ __forceinline__ unsigned pk_bf16(float lo, float hi) {
  typedef __bf16 bf16x2 __attribute__((ext_vector_type(2)));
  f32x2 v = {lo, hi};
  bf16x2 r = __builtin_convertvector(v, bf16x2);
  return __builtin_bit_cast(unsigned, r);
}
__device__ __forceinline__ float rbf(float f) { return bflo(pk_bf16(f, f)); }
__device__ __forceinline__ float dot2(unsigned a, unsigned b, float c) {
  typedef __bf16 bf16x2 __attribute__((ext_vector_type(2)));
  return __builtin_amdgcn_fdot2_f32_bf16(__builtin_bit_cast(bf16x2, a), __builtin_bit_cast(bf16x2, b), c, false);
}
__device__ __forceinline__ f32x2 fma2(f32x2 a, f32x2 b, f32x2 c) { return __builtin_elementwise_fma(a, b, c); }
__device__ __forceinline__ f32x2 bc2(float v) { return (f32x2){v, v}; }

__device__ __forceinline__ rsrc_t mk_rsrc(const void* p, unsigned bytes) { return __builtin_amdgcn_make_buffer_rsrc((void*)p, 0, bytes, 0x00020000); }
__device__ __forceinline__ void dma16(rsrc_t rs, unsigned voff, unsigned soff, LAS char* l) { __builtin_amdgcn_raw_ptr_buffer_load_lds(rs, (LAS void*)l, 16, voff, soff, 0, 0); }
__device__ __forceinline__ void dma16c(rsrc_t rs, unsigned voff, unsigned soff, LAS char* l) { __builtin_amdgcn_raw_ptr_buffer_load_lds(rs, (LAS void*)l, 16, voff, soff, 0, 16); }
__device__ __forceinline__ void dma4(rsrc_t rs, unsigned voff, unsigned soff, LAS char* l) { __builtin_amdgcn_raw_ptr_buffer_load_lds(rs, (LAS void*)l, 4, voff, soff, 0, 0); }
__device__ __forceinline__ void st16_wt(rsrc_t rs, unsigned voff, unsigned soff, u32x4 v) { __builtin_amdgcn_raw_buffer_store_b128(v, rs, voff + soff, 0, 16); }
__device__ __forceinline__ void st8_wt(rsrc_t rs, unsigned voff, unsigned soff, u32x2 v) { __builtin_amdgcn_raw_buffer_store_b64(v, rs, voff + soff, 0, 16); }
template <int N> __device__ __forceinline__ void wait_vm() { asm volatile("s_waitcnt vmcnt(%0)" ::"n"(N) : "memory"); }
__device__ __forceinline__ void wait_lgkm0() { asm volatile("" ::: "memory"); __builtin_amdgcn_s_waitcnt(0xC07F); asm volatile("" ::: "memory"); }
__device__ __forceinline__ void bar() {
  asm volatile("" ::: "memory");
  __builtin_amdgcn_s_barrier();
  asm volatile("" ::: "memory");
}
#define SCHED_FENCE() __builtin_amdgcn_sched_barrier(0)
__device__ __forceinline__ bf16x8 lds_rd128(unsigned p) { return *(LAS bf16x8*)(size_t)p; }
__device__ __forceinline__ s16x4 lds_rdtr(unsigned p) { return __builtin_amdgcn_ds_read_tr16_b64_v4i16((LAS s16x4*)(size_t)p); }
struct TrFrag { s16x4 h[2]; };
__device__ __forceinline__ bf16x8 frag(const bf16x8& f) { return f; }
__device__ __forceinline__ bf16x8 frag(const TrFrag& f) { s16x8 ab = __builtin_shufflevector(f.h[0], f.h[1], 0, 1, 2, 3, 4, 5, 6, 7); return __builtin_bit_cast(bf16x8, ab); }
#define MFMA(a, b, c) __builtin_amdgcn_mfma_f32_16x16x32_bf16(frag(a), frag(b), (c), 0, 0, 0)
template <int NM, class FA, class FB, class Side>
__device__ __forceinline__ void phase(f32x4 (*acc)[4], const FA* fa, const FB* fb, Side side) {
#pragma unroll
  for (int i = 0; i < NM; ++i) {
    acc[i >> 2][i & 3] = MFMA(fa[i >> 2], fb[i & 3], acc[i >> 2][i & 3]);
    side(i);
    SCHED_FENCE();
  }
}

template <int NL> __device__ __forceinline__ void wait_flags(gu64* p, bool active, u64 tag) {
#pragma unroll 1
  for (unsigned spins = 0; spins < SPIN_CAP; ++spins) {
    bool ok = true;
    if (active) {
#pragma unroll
      for (int k = 0; k < NL; ++k) ok &= __hip_atomic_load(p + 64 * k, RLX_AGENT) == tag;
    }
    if (__all(ok)) break;
    __builtin_amdgcn_s_sleep(2);
  }
  __builtin_amdgcn_fence(__ATOMIC_ACQUIRE, "wavefront");
}

__device__ __forceinline__ void lds_wait(unsigned addr, unsigned want) {
#pragma unroll 1
  for (unsigned spins = 0; spins < (SPIN_CAP << 4); ++spins) {
    if (__builtin_amdgcn_readfirstlane(__hip_atomic_load((LAS unsigned*)(size_t)addr, __ATOMIC_RELAXED, __HIP_MEMORY_SCOPE_WORKGROUP)) >= want) break;
    __builtin_amdgcn_s_sleep(1);
  }
  asm volatile("" ::: "memory");
}
__device__ __forceinline__ void publish(unsigned cnt, gu64* flag, u64 tag, int lane) {
  wait_vm<0>();
  wait_lgkm0();
  if (lane == 0 && __hip_atomic_fetch_add((LAS unsigned*)(size_t)cnt, 1u, __ATOMIC_RELAXED, __HIP_MEMORY_SCOPE_WORKGROUP) == 3u) __hip_atomic_store(flag, tag, RLX_AGENT);
}

template <class Issue> __device__ __forceinline__ void prod_fill(int n, Issue issue) {
#pragma unroll 1
  for (int t = 0; t < n; ++t) issue();
}
template <int L, int NS, class Issue> __device__ __forceinline__ void prod_run(int n, Issue issue) {
#pragma unroll 1
  for (int t = 0; t < n; ++t) { wait_vm<(NS - 2) * L>(); bar(); issue(); }
}
template <int L, int K, int X = 0> __device__ __forceinline__ void prod_tail() {
  wait_vm<K * L + X>();
  bar();
  if constexpr (K > 0) prod_tail<L, K - 1, X>();
}

__device__ __forceinline__ float wave_sum(float v) {
  v += __builtin_bit_cast(float, __builtin_amdgcn_update_dpp(0, __builtin_bit_cast(int, v), 0xB1, 0xf, 0xf, true));
  v += __builtin_bit_cast(float, __builtin_amdgcn_update_dpp(0, __builtin_bit_cast(int, v), 0x4E, 0xf, 0xf, true));
  v += __builtin_bit_cast(float, __builtin_amdgcn_update_dpp(0, __builtin_bit_cast(int, v), 0x141, 0xf, 0xf, true));
  v += __builtin_bit_cast(float, __builtin_amdgcn_update_dpp(0, __builtin_bit_cast(int, v), 0x140, 0xf, 0xf, true));
  const int iv = __builtin_bit_cast(int, v);
  const float a = __builtin_bit_cast(float, __builtin_amdgcn_readlane(iv, 0)), b = __builtin_bit_cast(float, __builtin_amdgcn_readlane(iv, 16));
  const float c = __builtin_bit_cast(float, __builtin_amdgcn_readlane(iv, 32)), d = __builtin_bit_cast(float, __builtin_amdgcn_readlane(iv, 48));
  return (a + b) + (c + d);
}

struct Args {
  const u16 *x, *Lre, *Lim, *Bre, *Bim, *Cre, *Cim, *logdt, *Dsk, *w1, *b1, *w2, *b2;
  float* out;
  char* ws;
};

constexpr int A1_NS = 3, A1_STAGE = 49152, A1_NT = D / 64;
constexpr int B1_NS = 7, B1_ABYTES = 32 * 448, B1_STAGE = B1_ABYTES + 32 * 256, B1_NT = S / 32;
constexpr int P2_NS = 4, P2_STAGE = 32768;
constexpr int LDS_BYTES = 160 * 1024;
constexpr int LDS_COEF = A1_NS * A1_STAGE;
constexpr int LDS_TOT = LDS_COEF + 14 * 256;
constexpr int LDS_SB = LDS_TOT + 512;
constexpr int LDS_STAT = LDS_SB + 512;
constexpr int LDS_RAW = LDS_STAT + 8 * 272;
constexpr int LDS_RES = P2_NS * P2_STAGE;
constexpr int LDS_CNT = LDS_BYTES - 256 - 2048 - 64;
constexpr int LDS_FLG = LDS_BYTES - 256 - 2048;
constexpr int LDS_DUMMY = LDS_BYTES - 256;
static_assert(LDS_RAW + 3072 + 768 <= LDS_CNT && B1_NS * B1_STAGE <= LDS_CNT && LDS_RES + 17920 <= LDS_CNT && P2_NS * P2_STAGE <= LDS_COEF && LDS_RAW % 16 == 0, "lds");

__device__ __forceinline__ f32x2 gelu_tanh2(f32x2 x) {
  const f32x2 z = x * fma2(x * x, bc2(-0.10294323967f), bc2(-2.30220819813f));
  const f32x2 d = (f32x2){__builtin_amdgcn_exp2f(z.x), __builtin_amdgcn_exp2f(z.y)} + bc2(1.0f);
  return x * (f32x2){__builtin_amdgcn_rcpf(d.x), __builtin_amdgcn_rcpf(d.y)};
}
#define CMUL(or_, oi_, ar_, ai_, br_, bi_) do { const float t_ = (ar_) * (br_) - (ai_) * (bi_); oi_ = (ar_) * (bi_) + (ai_) * (br_); or_ = t_; } while (0)

__global__ __launch_bounds__(512, 2) void fused(Args a) {
  extern __shared__ __attribute__((aligned(16))) char smem[];
  LAS char* lds = (LAS char*)smem;
  u64 pc0;
  asm volatile("s_getpc_b64 %0" : "=s"(pc0));
  const int wid = __builtin_amdgcn_readfirstlane(threadIdx.x >> 6), lane = threadIdx.x & 63;
  const int bid = blockIdx.x, xc = bid & 7, idx = bid >> 3, xg = xc >> 1;
  const bool roleA = !(xc & 1);
  const int ri = bid >> 1;
  gu64* sy = (gu64*)(a.ws + WS_SYNC);
  u64 tag;
#define LOAD_TAG() do { const __attribute__((address_space(4))) u64* ep_ = (const __attribute__((address_space(4))) u64*)(a.ws + WS_SYNC); \
    asm volatile("" : "+s"(ep_)::"memory"); tag = *ep_ + 1ull; } while (0)
#define HOLD_TAG() asm volatile("" ::"s"(tag))
#define tag32 ((unsigned)tag)
  const int chunk = 4 * (xg & 1) + (idx & 3), cb = 8 * (xg >> 1) + (idx >> 2);
  const int ft = 4 * xg + (idx & 3), dt1 = idx >> 2;
  const int tt = 4 * xg + (idx & 3), dt = idx >> 2;
  const int t0 = tt * 128, d0 = dt * 128;
  const int nC = roleA ? 32 - NAB : NAB, kc0 = roleA ? 0 : 32 - NAB, ku0 = roleA ? 0 : NBA, nt2 = nC + (roleA ? NBA : 56 - NBA);
  const rsrc_t rsy = mk_rsrc(a.ws + WS_SYNC, SYNC_BYTES);
  STAMP_INIT();
  STAMP(0);

  auto touch = [&](const u16* base, unsigned bytes, int u0, int n) {
    const rsrc_t rs = mk_rsrc(base, bytes);
#pragma unroll 1
    for (int k = 0; k < n; ++k) dma4(rs, (unsigned)(lane * 128), (unsigned)(u0 + k) * 8192u, lds + LDS_DUMMY);
  };
  auto w1_voff = [&](int piece) {
    const int ch = piece * 64 + lane, k = ch / 28, pos = ch - k * 28;
    return (unsigned)(k * F + ft * 224 + (pos ^ (3 * ((k >> 3) & 1))) * 8) * 2u;
  };
  if (!roleA) {
    const int row = ri * 16 + wid * 2;
    u32x4 r[2][2], brow[2][2];
    {
      const u16* xp = a.x + (size_t)row * D;
      const u16* bp = (ri < 64 ? a.Bre : a.Bim) + (size_t)(row & 1023) * D;
      const unsigned lo = (unsigned)lane * 16u;
      asm volatile("global_load_dwordx4 %0, %4, %5\n\tglobal_load_dwordx4 %1, %4, %5 offset:1024\n\tglobal_load_dwordx4 %2, %4, %5 offset:2048\n\tglobal_load_dwordx4 %3, %4, %5 offset:3072"
                   : "=&v"(r[0][0]), "=&v"(r[0][1]), "=&v"(r[1][0]), "=&v"(r[1][1]) : "v"(lo), "s"(xp) : "memory");
      asm volatile("global_load_dwordx4 %0, %4, %5\n\tglobal_load_dwordx4 %1, %4, %5 offset:1024\n\tglobal_load_dwordx4 %2, %4, %5 offset:2048\n\tglobal_load_dwordx4 %3, %4, %5 offset:3072"
                   : "=&v"(brow[0][0]), "=&v"(brow[0][1]), "=&v"(brow[1][0]), "=&v"(brow[1][1]) : "v"(lo), "s"(bp) : "memory");
    }
    LOAD_TAG();
    bar();
    {
      const rsrc_t rs = mk_rsrc(a.w1, S * F * 2);
#pragma unroll
      for (int j = 0; j < 2; ++j) {
        const int piece = j == 0 ? wid : (wid < 6 ? wid + 8 : wid);
        const unsigned vo = w1_voff(piece);
#pragma unroll
        for (int t = 0; t < B1_NS - 1; ++t) dma16(rs, vo, t * (32 * F * 2), lds + t * B1_STAGE + piece * 1024);
      }
    }
    wait_vm<16>();
    STAMP(20);
    asm volatile("" : "+v"(r[0][0]), "+v"(r[0][1]), "+v"(r[1][0]), "+v"(r[1][1])::"memory");
    const rsrc_t rx = mk_rsrc(a.ws + WS_HL, S * D * 2);
#pragma unroll
    for (int pass = 0; pass < 2; ++pass) {
      float s1[2], s2[2];
#pragma unroll
      for (int i = 0; i < 2; ++i) {
        float a1 = 0.f, a2 = 0.f;
#pragma unroll
        for (int k = 0; k < 8; ++k) { const unsigned w = r[i][k >> 2][k & 3]; a1 = dot2(w, 0x3f803f80u, a1); a2 = dot2(w, w, a2); }
        s1[i] = a1; s2[i] = a2;
      }
#pragma unroll
      for (int i = 0; i < 2; ++i) { s1[i] = wave_sum(s1[i]); s2[i] = wave_sum(s2[i]); }
#pragma unroll
      for (int i = 0; i < 2; ++i) {
        const float mean = s1[i] * (1.0f / D), var = fmaxf(fmaf(-mean, mean, s2[i] * (1.0f / D)), 0.f);
        const unsigned ms = pk_bf16(mean, __builtin_amdgcn_sqrtf(rbf(rbf(var) + 1e-5f)));
        const float rs_ = __builtin_amdgcn_rcpf(bfhi(ms)), mb = bflo(ms);
#pragma unroll
        for (int k = 0; k < 8; ++k) {
          const unsigned w = r[i][k >> 2][k & 3];
          r[i][k >> 2][k & 3] = pk_bf16((bflo(w) - mb) * rs_, (bfhi(w) - mb) * rs_);
        }
        if (pass == 0) {
          if (lane == 0) __hip_atomic_store(sy + SY_ST + row + i, ((u64)tag32 << 32) | ms, RLX_AGENT);
        } else {
          st16_wt(rx, (unsigned)lane * 16u, (unsigned)((row + i) * D) * 2u, r[i][0]);
          st16_wt(rx, (unsigned)lane * 16u + 1024u, (unsigned)((row + i) * D) * 2u, r[i][1]);
        }
      }
    }
    STAMP(1);
    asm volatile("s_waitcnt vmcnt(0)" : "+v"(brow[0][0]), "+v"(brow[0][1]), "+v"(brow[1][0]), "+v"(brow[1][1])::"memory");
    bar();
    if (wid == 0 && lane == 0) __hip_atomic_store(sy + SY_LN + ri, tag, RLX_AGENT);
    STAMP(2);
    {
      float sums[2];
#pragma unroll
      for (int i = 0; i < 2; ++i) {
        float s = 0.f;
#pragma unroll
        for (int k = 0; k < 8; ++k) { const unsigned w = brow[i][k >> 2][k & 3]; s = dot2(w, 0x3f803f80u, s); }
        sums[i] = wave_sum(s);
      }
      if (lane < 2) __hip_atomic_store(sy + SY_SB + row + lane, ((u64)tag32 << 32) | __float_as_uint(lane ? sums[1] : sums[0]), RLX_AGENT);
    }
  }

  if (wid >= 4) {
    const int pw = wid - 4;
    if (pw == 3) {
      auto flags_fetch = [&]() {
        dma16c(rsy, (unsigned)lane * 16u, SY_LN * 8, lds + LDS_FLG);
        dma16c(rsy, (unsigned)lane * 16u, SY_LN * 8 + 1024, lds + LDS_FLG + 1024);
      };
      auto flags_check = [&]() {
        bool ok = true;
#pragma unroll
        for (int k = 0; k < 4; ++k) ok &= *(LAS u64*)(lds + LDS_FLG + k * 512 + lane * 8) == tag;
        if (!__all(ok)) wait_flags<4>(sy + SY_LN + lane, true, tag);
      };
      if (lane < 4) *(LAS unsigned*)(size_t)(LDS_CNT + lane * 4) = 0u;
      dma4(mk_rsrc((const void*)(pc0 & ~255ull), CODE_PF_BYTES), (unsigned)(lane * 128), (unsigned)((idx % 5) * 8192), lds + LDS_DUMMY);
      if (roleA) {
        st16_wt(rsy, (unsigned)lane * 16u, (unsigned)(SY_GR * 8 + (chunk * P + cb * 64) * 16), (u32x4){0u, 0u, 0u, 0u});
        dma4(mk_rsrc(a.Lre, P * 2), (unsigned)(lane & 31) * 4u, cb * 128, lds + LDS_RAW + 3072);
        dma4(mk_rsrc(a.Lim, P * 2), (unsigned)(lane & 31) * 4u, cb * 128, lds + LDS_RAW + 3072 + 256);
        dma4(mk_rsrc(a.logdt, P * 2), (unsigned)(lane & 31) * 4u, cb * 128, lds + LDS_RAW + 3072 + 512);
        LOAD_TAG(); HOLD_TAG();
        const int bst = PF_ST0 + ((ri * PF_NST) >> 7);
        auto step = [&](int t) {
          bar();
          if (t == bst) touch(a.w1, S * F * 2, 128 + PF_N1 * ri, PF_N1);
          if (t == bst + PF_NST) touch(a.w1, S * F * 2, 128 + 128 * PF_N1 + (13 - PF_N1) * ri, 13 - PF_N1);
        };
        step(0); step(1); step(2);
        if (bst <= 2) wait_vm<PF_N1>(); else wait_vm<0>();
        if (lane == 0) __hip_atomic_store(sy + SY_LN + 128 + ri, tag, RLX_AGENT);
        LAS float* cf = (LAS float*)(lds + LDS_COEF) + lane;
        const float lr = bf2f(*(LAS u16*)(lds + LDS_RAW + 3072 + lane * 2)), li = bf2f(*(LAS u16*)(lds + LDS_RAW + 3072 + 256 + lane * 2));
        const float dtt = rbf(expf(bf2f(*(LAS u16*)(lds + LDS_RAW + 3072 + 512 + lane * 2)))), er = expf(dtt * lr);
        float arg = dtt * li;
        asm volatile("" : "+v"(arg));
        step(3);
        float sn, cs;
        sincosf(arg, &sn, &cs);
        float pr = er * cs, pi = er * sn;
        asm volatile("" : "+v"(pr), "+v"(pi));
        step(4);
        const float nr = pr - 1.0f, den = lr * lr + li * li;
        cf[0] = pr; cf[64] = pi;
        cf[128] = 2.0f * (nr * lr + pi * li) / den; cf[192] = 2.0f * (pi * lr - nr * li) / den;
        step(5);
#pragma unroll
        for (int k = 0; k < 8; ++k) {
          const float t = pr * pr - pi * pi; pi = 2.0f * pr * pi; pr = t;
          if (k >= 3) { cf[(4 + 2 * (k - 3)) * 64] = pr; cf[(5 + 2 * (k - 3)) * 64] = pi; }
        }
#pragma unroll 1
        for (int t = 6; t < 12; ++t) step(t);
        const unsigned sbo = (unsigned)(SY_SB * 8 + cb * 512 + (lane >> 5) * (P * 8) + (lane & 31) * 16);
#pragma unroll 1
        for (unsigned spins = 0;; ++spins) {
          dma16c(rsy, (unsigned)lane * 16u, (unsigned)(SY_ST * 8 + chunk * 2048), lds + LDS_RAW);
          dma16c(rsy, (unsigned)lane * 16u, (unsigned)(SY_ST * 8 + chunk * 2048 + 1024), lds + LDS_RAW + 1024);
          dma16c(rsy, sbo, 0, lds + LDS_RAW + 2048);
          if (spins == 0) { flags_fetch(); step(12); step(13); }
          wait_vm<0>();
          u32x2 gs[4], gb[2];
          bool ok = true;
#pragma unroll
          for (int k = 0; k < 4; ++k) { gs[k] = *(LAS u32x2*)(lds + LDS_RAW + k * 512 + lane * 8); ok &= gs[k][1] == tag32; }
#pragma unroll
          for (int k = 0; k < 2; ++k) { gb[k] = *(LAS u32x2*)(lds + LDS_RAW + 2048 + k * 512 + lane * 8); ok &= gb[k][1] == tag32; }
          if (__all(ok) || spins > SPIN_CAP) {
#pragma unroll
            for (int k = 0; k < 4; ++k) {
              LAS float* e = (LAS float*)(lds + LDS_STAT + (2 * k + (lane >> 5)) * 272 + (lane & 15) * 16 + ((lane >> 4) & 1) * 4);
              e[0] = -bflo(gs[k][0]); e[2] = __builtin_amdgcn_rcpf(bfhi(gs[k][0]));
            }
#pragma unroll
            for (int k = 0; k < 2; ++k) *(LAS unsigned*)(lds + LDS_SB + k * 256 + lane * 4) = gb[k][0];
            break;
          }
          __builtin_amdgcn_s_sleep(2);
        }
        bar();
        flags_check();
        wait_lgkm0();
        step(15);
      } else {
#pragma unroll 1
        for (int t = 0; t < 13; ++t) bar();
        flags_fetch();
#pragma unroll 1
        for (int t = 13; t < 20; ++t) bar();
        wait_vm<0>();
        flags_check();
        if (bid == 255 && lane == 0) __hip_atomic_store(sy, tag, RLX_AGENT);
#pragma unroll 1
        for (int t = 20; t < B1_NT; ++t) bar();
      }
    } else if (roleA) {
      const int T0 = chunk * 256, p0 = cb * 64;
      if (pw < 2) {
        unsigned voff[16];
#pragma unroll
        for (int j = 0; j < 16; ++j) {
          const int rho = 8 * j + (lane >> 3), m = rho >> 4, i = rho & 15;
          const int tok = T0 + 128 * pw + 32 * (i >> 2) + 16 * (i & 1) + 2 * m + ((i >> 1) & 1);
          const int c = (lane & 7) ^ ((rho >> 1) & 7);
          voff[j] = (unsigned)(tok * D + c * 8) * 2u;
        }
        const rsrc_t rs = mk_rsrc(a.x, S * D * 2);
        unsigned so = 0, lo = pw * 16384;
        auto issue = [&]() {
#pragma unroll
          for (int j = 0; j < 16; ++j) dma16(rs, voff[j], so, lds + lo + j * 1024);
          so += 128; lo = lo >= (A1_NS - 1) * A1_STAGE ? lo - (A1_NS - 1) * A1_STAGE : lo + A1_STAGE;
        };
        prod_fill(A1_NS - 1, issue);
        LOAD_TAG(); HOLD_TAG();
        prod_run<16, A1_NS>(A1_NT - (A1_NS - 1), issue);
        prod_tail<16, A1_NS - 2>();
      } else {
        unsigned voff[8];
#pragma unroll
        for (int j = 0; j < 8; ++j) {
          const int wn = j >> 2, q = (j >> 1) & 1, hf = j & 1, i = hf * 8 + (lane >> 3);
          const int st = p0 + 32 * wn + 16 * q + i;
          const int c = (lane & 7) ^ ((i >> 1) & 7);
          voff[j] = (unsigned)(st * D + c * 8) * 2u;
        }
        const rsrc_t rre = mk_rsrc(a.Bre, P * D * 2), rim = mk_rsrc(a.Bim, P * D * 2);
        unsigned so = 0, lo = 32768;
        auto issue = [&]() {
#pragma unroll
          for (int j = 0; j < 8; ++j) {
            dma16(rre, voff[j], so, lds + lo + (j >> 1) * 4096 + (j & 1) * 1024);
            dma16(rim, voff[j], so, lds + lo + (j >> 1) * 4096 + (j & 1) * 1024 + 2048);
          }
          so += 128; lo = lo >= (A1_NS - 1) * A1_STAGE ? lo - (A1_NS - 1) * A1_STAGE : lo + A1_STAGE;
        };
        prod_fill(A1_NS - 1, issue);
        LOAD_TAG(); HOLD_TAG();
        prod_run<16, A1_NS>(A1_NT - (A1_NS - 1), issue);
        prod_tail<16, A1_NS - 2>();
      }
    } else {
      const int dd0 = dt1 * 128;
      if (pw < 2) {
        unsigned voff[7];
#pragma unroll
        for (int j = 0; j < 7; ++j) voff[j] = w1_voff(pw * 7 + j);
        const rsrc_t rs = mk_rsrc(a.w1, S * F * 2);
        unsigned so = (B1_NS - 1) * (32 * F * 2), lo = (B1_NS - 1) * B1_STAGE + pw * 7168;
        auto issue = [&]() {
#pragma unroll
          for (int j = 0; j < 7; ++j) dma16(rs, voff[j], so, lds + lo + j * 1024);
          so += 32 * F * 2; lo = lo >= (B1_NS - 1) * B1_STAGE ? lo - (B1_NS - 1) * B1_STAGE : lo + B1_STAGE;
        };
        prod_run<7, B1_NS>(B1_NT - (B1_NS - 1), issue);
        prod_tail<7, B1_NS - 2>();
      } else {
        unsigned voff[2];
#pragma unroll
        for (int j = 0; j < 2; ++j) {
          const int k = j * 8 + (lane >> 4), pos = lane & 15;
          const int c = pos ^ (((k & 3) << 1) | (((k >> 3) & 1) << 3));
          voff[j] = (unsigned)((lane >> 4) * D + dd0 + c * 8) * 2u;
        }
        const rsrc_t rs = mk_rsrc(a.ws + WS_HL, S * D * 2);
        unsigned so = 0, lo = B1_ABYTES;
        auto issue = [&]() {
#pragma unroll
          for (int j = 0; j < 8; ++j) dma16c(rs, voff[(j >> 1) & 1], so + j * 4 * (D * 2), lds + lo + j * 1024);
          so += 32 * D * 2; lo = lo >= (B1_NS - 1) * B1_STAGE ? lo - (B1_NS - 1) * B1_STAGE : lo + B1_STAGE;
        };
        wait_flags<1>(sy + SY_LN + lane, true, tag);
        PSTAMP(6, 16);
        prod_fill(B1_NS - 1, issue);
        prod_run<8, B1_NS>(B1_NT - (B1_NS - 1), issue);
        prod_tail<8, B1_NS - 2>();
      }
    }
    bar();
    const int nU = nt2 - nC;
    if (pw == 0 || pw == 3) {
      const int dh = d0 + (((pw ? 1 : 0) ^ (roleA ? 0 : 1)) << 6), r4 = lane >> 4;
      const unsigned vce = (unsigned)(dh + (lane >> 3)) * (P * 2) + (unsigned)(((lane & 7) ^ (lane >> 4)) * 16), vco = vce ^ 64u;
      const unsigned vu = (unsigned)((dh + (lane >> 2)) * 64 + (((lane & 3) ^ (((r4 & 1) << 1) ^ ((r4 >> 1) * 3))) * 16));
      const rsrc_t ru = mk_rsrc(a.ws + WS_U, D * F * 2);
      rsrc_t rc = mk_rsrc(roleA ? a.Cre : a.Cim, D * P * 2);
      unsigned so = (kc0 & 15) * 128, lo = pw ? 8192 : 0;
      auto issueC = [&]() {
#pragma unroll
        for (int j = 0; j < 8; ++j) dma16(rc, (j & 1) ? vco : vce, so + j * 8 * (P * 2), lds + lo + j * 1024);
        so += 128; lo = (lo + P2_STAGE) & (P2_NS * P2_STAGE - 1);
      };
      auto issueU = [&]() {
#pragma unroll
        for (int j = 0; j < 8; ++j) dma16c(ru, vu, so + (j & 1) * (D * 64) + (j >> 1) * 1024, lds + lo + j * 1024);
        so += 2 * D * 64; lo = (lo + P2_STAGE) & (P2_NS * P2_STAGE - 1);
      };
      prod_fill(P2_NS - 1, issueC);
      constexpr int UA = NBA * 32, WB = PF_WB * 128 / 2;
      constexpr int RA = (UA - WB + 127) / 128, RB = (1792 - UA - WB + 127) / 128;
      if (roleA) {
        if (pw == 3) { touch(a.Cre, D * P * 2, 2 * ri, 2); touch(a.Cim, D * P * 2, 2 * ri, 2); }
        const int bst = 1 + ((ri * PF_NST2) >> 7);
#pragma unroll 1
        for (int sg = 0; sg < 2; ++sg) {
          const int b = sg ? 13 : 0, e = sg ? nC - 3 : 13, m = bst < b ? b : bst > e ? e : bst;
          prod_run<8, P2_NS>(m - b, issueC);
          if (pw == 3 && bst > b && bst <= e) { touch(a.w2, F * S * 2, UA + WB + RB * ri, RB); touch(a.w2, F * S * 2, WB + RA * ri, RA); }
          prod_run<8, P2_NS>(e - m, issueC);
          rc = mk_rsrc(a.Cim, D * P * 2); so = 0;
        }
      } else {
        if (pw == 3) { touch(a.w2, F * S * 2, UA + (PF_WB / 2) * ri, PF_WB / 2); touch(a.w2, F * S * 2, (PF_WB / 2) * ri, PF_WB / 2); }
        prod_run<8, P2_NS>(nC - 3, issueC);
      }
      so = ku0 * (2 * D * 64);
      wait_vm<16>();
      bar();
      wait_flags<1>(sy + SY_U + dt * 16 + (lane & 15), true, tag);
      PSTAMP(4, 19);
      issueU();
      prod_run<8, P2_NS>(nU - 1, issueU);
    } else {
      unsigned voff[2];
#pragma unroll
      for (int j = 0; j < 2; ++j) {
        const int k = j * 8 + (lane >> 4), pos = lane & 15;
        const int c = pos ^ (((k & 3) << 1) | (((k >> 3) & 1) << 3));
        voff[j] = (unsigned)(((pw - 1) * 32 + (lane >> 4)) * S + t0 + c * 8) * 2u;
      }
      const unsigned vh = (unsigned)(lane * 16 + (pw - 1) * 8192);
      const rsrc_t rh = mk_rsrc(a.ws + WS_HT, 2 * P * S * 2), rw = mk_rsrc(a.w2, F * S * 2);
      unsigned so = (tt * 32 + kc0) * 16384, lo = 16384 + (pw - 1) * 8192;
      auto issueH = [&]() {
#pragma unroll
        for (int j = 0; j < 8; ++j) dma16c(rh, vh, so + j * 1024, lds + lo + j * 1024);
        so += 16384; lo = (lo + P2_STAGE) & (P2_NS * P2_STAGE - 1);
      };
      auto issueW = [&]() {
#pragma unroll
        for (int j = 0; j < 8; ++j) dma16(rw, voff[(j >> 1) & 1], so + j * 4 * (S * 2), lds + lo + j * 1024);
        so += 64 * S * 2; lo = (lo + P2_STAGE) & (P2_NS * P2_STAGE - 1);
      };
      if (roleA) lds_wait(LDS_CNT, 4u);
      wait_flags<1>(sy + SY_H + (tt >> 1) * 16 + (lane & 15), true, tag);
      PSTAMP(6, 17);
      prod_fill(P2_NS - 1, issueH);
      prod_run<8, P2_NS>(nC - 3, issueH);
      so = ku0 * (64 * S * 2);
      prod_run<8, P2_NS>(nU, issueW);
    }
    {
      const rsrc_t rxx = mk_rsrc(a.x, S * D * 2);
      const int dk = d0 + (roleA ? 0 : 64);
#pragma unroll
      for (int k = 0; k < 4; ++k) dma16(rxx, (unsigned)((lane >> 3) * (D * 2) + ((lane & 7) ^ (lane >> 3)) * 16), (unsigned)((t0 + 32 * pw + 8 * k) * D + dk) * 2u, lds + LDS_RES + (4 * pw + k) * 1024);
      if (pw == 0) dma16c(rsy, (unsigned)lane * 16u, (unsigned)(SY_ST + t0) * 8u, lds + LDS_RES + 16384);
      else dma4(mk_rsrc(pw == 1 ? a.b2 : a.Dsk, pw == 1 ? S * 2 : D * 2), (unsigned)lane * 4u, (unsigned)(pw == 1 ? t0 : dk) * 2u, lds + LDS_RES + (pw == 1 ? 17408 : 17664));
      prod_tail<8, P2_NS - 2, 5>();
      wait_vm<0>();
      bar();
    }
    return;
  }

  const int wm = wid >> 1, wn = wid & 1;
  int cl = lane & 15, g = lane >> 4, q4 = cl >> 2, p4 = lane & 3, ln = lane;
#define RELANE() do { ln = __builtin_amdgcn_mbcnt_hi(~0u, __builtin_amdgcn_mbcnt_lo(~0u, 0u)); asm volatile("" : "+v"(ln)); cl = ln & 15; g = ln >> 4; q4 = cl >> 2; p4 = ln & 3; } while (0)
  const int kh = roleA ? 0 : 1, pa = bid ^ 1;

  if (roleA) {
    LOAD_TAG(); HOLD_TAG();
    RELANE();
    const int p0 = cb * 64;
    const unsigned rb = (unsigned)(cl * 128 + ((g ^ (cl >> 1)) * 16));
    unsigned pa0 = rb + wm * 16384, pa1 = pa0 ^ 64u, pb0 = rb + 32768 + wn * 8192, pb1 = pb0 ^ 64u;
    f32x4 acc[8][4];
#pragma unroll
    for (int m = 0; m < 8; ++m)
#pragma unroll
      for (int n = 0; n < 4; ++n) acc[m][n] = (f32x4){0.f, 0.f, 0.f, 0.f};
    bf16x8 xa[8], xb[4], ya[8], yb[4];
    auto rd = [&](bf16x8* fa, bf16x8* fb, unsigned pa_, unsigned pb_, int r) {
      if (r == 0) fa[0] = lds_rd128(pa_); else if (r < 5) fb[r - 1] = lds_rd128(pb_ + (r - 1) * 2048); else fa[r - 4] = lds_rd128(pa_ + (r - 4) * 2048);
    };
    int slot = 0, dl = 0;
    bar();
    STAMP(3);
#pragma unroll
    for (int r = 0; r < 12; ++r) rd(xa, xb, pa0, pb0, r);
#pragma unroll 1
    for (int t = 0; t < A1_NT; ++t) {
      SCHED_FENCE(); wait_lgkm0(); SCHED_FENCE();
      phase<32>(acc, xa, xb, [&](int i) {
        if (i % A1_RS == 0 && i / A1_RS < 12) rd(ya, yb, pa1, pb1, i / A1_RS);
        if (i == 25) { dl = (slot == A1_NS - 1) ? -(A1_NS - 1) * A1_STAGE : A1_STAGE; slot = (slot == A1_NS - 1) ? 0 : slot + 1; }
        if (i == 27) { pa0 += dl; pb0 += dl; asm volatile("" : "+v"(pa0), "+v"(pb0)); }
        if (i == 29) { pa1 += dl; pb1 += dl; asm volatile("" : "+v"(pa1), "+v"(pb1)); }
      });
      wait_lgkm0();
      bar();
      SCHED_FENCE();
      phase<32>(acc, ya, yb, [&](int i) { if (i % A1_RS == 0 && i / A1_RS < 12) rd(xa, xb, pa0, pb0, i / A1_RS); });
    }
    RELANE();
    STAMP(4);
    float cf[2][14], sb[4];
#pragma unroll
    for (int q = 0; q < 2; ++q) {
#pragma unroll
      for (int k = 0; k < 14; ++k) cf[q][k] = ((LAS float*)(lds + LDS_COEF))[k * 64 + 32 * wn + 16 * q + cl];
      sb[2 * q] = ((LAS float*)(lds + LDS_SB))[32 * wn + 16 * q + cl]; sb[2 * q + 1] = ((LAS float*)(lds + LDS_SB))[64 + 32 * wn + 16 * q + cl];
    }
    f32x2 er[2], ei[2];
#pragma unroll
    for (int q = 0; q < 2; ++q) { er[q] = bc2(0.f); ei[q] = bc2(0.f); }
#pragma unroll
    for (int m = 0; m < 8; ++m)
#pragma unroll
      for (int jj = 0; jj < 2; ++jj) {
        const f32x4 st = *(LAS f32x4*)(lds + LDS_STAT + (4 * wm + g) * 272 + (2 * m + jj) * 16);
        const f32x2 c2 = {st[0], st[1]}, r2 = {st[2], st[3]};
#pragma unroll
        for (int q = 0; q < 2; ++q) {
          const f32x2 ur = fma2(c2, bc2(sb[2 * q]), (f32x2){acc[m][2 * q][2 * jj], acc[m][2 * q][2 * jj + 1]}) * r2;
          const f32x2 ui = fma2(c2, bc2(sb[2 * q + 1]), (f32x2){acc[m][2 * q + 1][2 * jj], acc[m][2 * q + 1][2 * jj + 1]}) * r2;
          acc[m][2 * q][2 * jj] = ur.x; acc[m][2 * q][2 * jj + 1] = ur.y; acc[m][2 * q + 1][2 * jj] = ui.x; acc[m][2 * q + 1][2 * jj + 1] = ui.y;
          const f32x2 nr = fma2(bc2(cf[q][0]), er[q], fma2(bc2(-cf[q][1]), ei[q], ur));
          const f32x2 ni = fma2(bc2(cf[q][0]), ei[q], fma2(bc2(cf[q][1]), er[q], ui));
          er[q] = nr; ei[q] = ni;
        }
        if (jj == 1 && (m & 1)) asm volatile("" : "+v"(er[0]), "+v"(ei[0]), "+v"(er[1]), "+v"(ei[1]));
      }
    float sr[2], si[2], pr_[2], pi_[2];
    LAS float* tot = (LAS float*)(lds + LDS_TOT);
#pragma unroll
    for (int q = 0; q < 2; ++q) {
      float tr, ti;
      CMUL(tr, ti, cf[q][4], cf[q][5], er[q].x, ei[q].x);
      sr[q] = tr + er[q].y; si[q] = ti + ei[q].y;
      float vr = __shfl_up(sr[q], 16), vi = __shfl_up(si[q], 16);
      CMUL(tr, ti, cf[q][6], cf[q][7], vr, vi);
      if (g >= 1) { sr[q] += tr; si[q] += ti; }
      vr = __shfl_up(sr[q], 32); vi = __shfl_up(si[q], 32);
      CMUL(tr, ti, cf[q][8], cf[q][9], vr, vi);
      if (g >= 2) { sr[q] += tr; si[q] += ti; }
      vr = __shfl_up(sr[q], 16); vi = __shfl_up(si[q], 16);
      pr_[q] = g >= 1 ? vr : 0.f; pi_[q] = g >= 1 ? vi : 0.f;
      if (wm == 0 && g == 3) *(LAS f32x2*)(tot + (32 * wn + 16 * q + cl) * 2) = (f32x2){sr[q], si[q]};
    }
    if (wm == 0) {
      wait_lgkm0();
      if (ln == 0) __hip_atomic_store((LAS unsigned*)(size_t)(LDS_CNT + 4 + 4 * wn), 1u, __ATOMIC_RELAXED, __HIP_MEMORY_SCOPE_WORKGROUP);
    } else {
      lds_wait(LDS_CNT + 4 + 4 * wn, 1u);
    }
    f32x2 tq[2];
    gu64* gran = sy + SY_GR;
#pragma unroll
    for (int q = 0; q < 2; ++q) {
      tq[q] = *(LAS f32x2*)(tot + (32 * wn + 16 * q + cl) * 2);
      if (wm == 1 && g == 3 && chunk < 7) {
        float tr, ti;
        CMUL(tr, ti, cf[q][10], cf[q][11], tq[q].x, tq[q].y);
        gu64* gp = gran + ((size_t)chunk * P + p0 + 32 * wn + 16 * q + cl) * 2;
        __hip_atomic_store(gp, (1ull << 32) | __float_as_uint(tr + sr[q]), RLX_AGENT);
        __hip_atomic_store(gp + 1, (1ull << 32) | __float_as_uint(ti + si[q]), RLX_AGENT);
      }
    }
    STAMP(5);
#pragma unroll
    for (int m = 0; m < 8; ++m)
#pragma unroll
      for (int q = 0; q < 2; ++q) {
        const f32x4 ur = acc[m][2 * q], ui = acc[m][2 * q + 1];
        acc[m][2 * q] = ur * cf[q][2] - ui * cf[q][3];
        acc[m][2 * q + 1] = ui * (-cf[q][2]) - ur * cf[q][3];
        asm volatile("" : "+v"(acc[m][2 * q]), "+v"(acc[m][2 * q + 1]));
      }
    float hr[2] = {0.f, 0.f}, hi[2] = {0.f, 0.f};
    if (chunk > 0) {
      float gr[2][2], gi[2][2];
#pragma unroll 1
      for (unsigned spins = 0;; ++spins) {
        bool ok = true;
#pragma unroll
        for (int q = 0; q < 2; ++q)
#pragma unroll
          for (int k = 0; k < 2; ++k) {
            const int cc = (g + 4 * k < chunk) ? g + 4 * k : 0;
            gu64* gp = gran + ((size_t)cc * P + p0 + 32 * wn + 16 * q + cl) * 2;
            const u64 vr = __hip_atomic_load(gp, RLX_AGENT), vi = __hip_atomic_load(gp + 1, RLX_AGENT);
            ok &= ((vr >> 32) == 1ull) & ((vi >> 32) == 1ull);
            gr[q][k] = __uint_as_float((unsigned)vr); gi[q][k] = __uint_as_float((unsigned)vi);
          }
        if (__all(ok) || spins > SPIN_CAP) break;
        __builtin_amdgcn_s_sleep(1);
      }
#pragma unroll
      for (int q = 0; q < 2; ++q) {
        float ar_ = 0.f, ai_ = 0.f;
#pragma unroll
        for (int k = 0; k < 2; ++k) {
          float vr = gr[q][k], vi = gi[q][k];
          const int e = chunk - 1 - (g + 4 * k);
#pragma unroll
          for (int i = 0; i < 6 - 4 * k; ++i) {
            float tr, ti;
            CMUL(tr, ti, cf[q][12], cf[q][13], vr, vi);
            vr = i < e ? tr : vr; vi = i < e ? ti : vi;
          }
          ar_ += e >= 0 ? vr : 0.f; ai_ += e >= 0 ? vi : 0.f;
        }
        ar_ += __shfl_xor(ar_, 16); ai_ += __shfl_xor(ai_, 16);
        ar_ += __shfl_xor(ar_, 32); ai_ += __shfl_xor(ai_, 32);
        hr[q] = ar_; hi[q] = ai_;
      }
    }
    STAMP(6);
    f32x2 wr[2], wi[2];
#pragma unroll
    for (int q = 0; q < 2; ++q) {
      float tr, ti;
      if (wm == 1) { CMUL(tr, ti, cf[q][10], cf[q][11], hr[q], hi[q]); hr[q] = tr + tq[q].x; hi[q] = ti + tq[q].y; }
      CMUL(tr, ti, cf[q][6], cf[q][7], hr[q], hi[q]);
      if (g & 1) { hr[q] = tr; hi[q] = ti; }
      CMUL(tr, ti, cf[q][8], cf[q][9], hr[q], hi[q]);
      if (g & 2) { hr[q] = tr; hi[q] = ti; }
      hr[q] += pr_[q]; hi[q] += pi_[q];
      CMUL(tr, ti, cf[q][4], cf[q][5], hr[q], hi[q]);
      const f32x2 h0r = {hr[q], tr + er[q].x}, h0i = {hi[q], ti + ei[q].x};
      wr[q] = h0r * cf[q][2] - h0i * cf[q][3];
      wi[q] = h0i * (-cf[q][2]) - h0r * cf[q][3];
    }
    LAS char* scr = lds + wid * P2_STAGE + 16384;
    const unsigned fsw = (unsigned)(((cl & 3) << 1) | (((cl >> 3) & 1) << 3));
    const rsrc_t rh = mk_rsrc(a.ws + WS_HT, 2 * P * S * 2);
#pragma unroll
    for (int q = 0; q < 2; ++q) {
      const f32x2 ar2 = bc2(cf[q][0]), ai2 = bc2(cf[q][1]);
#pragma unroll
      for (int mh = 0; mh < 2; ++mh) {
        u32x4 o[2][2];
#pragma unroll
        for (int mq = 0; mq < 4; ++mq) {
          const int m = 4 * mh + mq;
          const f32x2 u0r = {acc[m][2 * q][0], acc[m][2 * q][1]}, u0i = {acc[m][2 * q + 1][0], acc[m][2 * q + 1][1]};
          const f32x2 u1r = {acc[m][2 * q][2], acc[m][2 * q][3]}, u1i = {acc[m][2 * q + 1][2], acc[m][2 * q + 1][3]};
          const f32x2 n0r = fma2(ar2, wr[q], fma2(ai2, wi[q], u0r)), n0i = fma2(ar2, wi[q], fma2(-ai2, wr[q], u0i));
          const f32x2 n1r = fma2(ar2, n0r, fma2(ai2, n0i, u1r)), n1i = fma2(ar2, n0i, fma2(-ai2, n0r, u1i));
          wr[q] = n1r; wi[q] = n1i;
          o[0][0][mq] = pk_bf16(n0r.x, n1r.x); o[0][1][mq] = pk_bf16(n0r.y, n1r.y);
          o[1][0][mq] = pk_bf16(n0i.x, n1i.x); o[1][1][mq] = pk_bf16(n0i.y, n1i.y);
        }
#pragma unroll
        for (int part = 0; part < 2; ++part)
#pragma unroll
          for (int run = 0; run < 2; ++run)
            *(LAS u32x4*)(scr + (part * 32 + 16 * q + cl) * 256 + (((unsigned)(4 * g + 2 * run + mh) ^ fsw) * 16)) = o[part][run];
      }
#pragma unroll
      for (int i = 0; i < 8; ++i) {
        const int part = i >> 2, pc = 4 * q + (i & 3);
        const u32x4 v = *(LAS u32x4*)(scr + part * 8192 + pc * 1024 + ln * 16);
        st16_wt(rh, (unsigned)(ln * 16 + pc * 1024), (unsigned)(((2 * chunk + wm) * 32 + part * 16 + cb) * 16384 + wn * 8192), v);
      }
      SCHED_FENCE();
    }
    STAMP(7);
    publish(LDS_CNT, sy + SY_H + chunk * 16 + cb, tag, ln);
    STAMP(8);
  } else {
    RELANE();
    const int f0 = ft * 224, dd0 = dt1 * 128;
    const int k0 = 8 * g + q4;
    unsigned pa0 = (unsigned)(k0 * 448 + ((12 * wm + p4) ^ (3 * (g & 1))) * 16 + (p4 & 1) * 8);
    unsigned pa1 = (unsigned)(k0 * 448 + ((12 * wm + p4) ^ (3 * (g & 1))) * 16 + ((p4 & 1) ^ 1) * 8);
    unsigned pa2 = (unsigned)(k0 * 448 + ((24 + 2 * wm + (p4 >> 1)) ^ (3 * (g & 1))) * 16 + (p4 & 1) * 8);
    unsigned pb[4];
#pragma unroll
    for (int n = 0; n < 4; ++n) pb[n] = (unsigned)(B1_ABYTES + k0 * 256 + ((8 * wn + 2 * n + (p4 >> 1)) ^ ((q4 << 1) | ((g & 1) << 3))) * 16 + (p4 & 1) * 8);
    f32x4 acc[7][4];
#pragma unroll
    for (int m = 0; m < 7; ++m) {
      const u32x2 bv = *(const u32x2*)(a.b1 + f0 + (m < 6 ? 96 * wm + 32 * (m >> 1) + 8 * g + 4 * ((m & 1) ^ (g & 1)) : 192 + 16 * wm + 4 * g));
#pragma unroll
      for (int n = 0; n < 4; ++n) acc[m][n] = (f32x4){bflo(bv[0]), bfhi(bv[0]), bflo(bv[1]), bfhi(bv[1])};
    }
    TrFrag xa[7], xb[4], ya[7], yb[4];
    int slot = 0, dl = 0;
    auto side = [&](TrFrag* fa, TrFrag* fb, int i) {
      const int f = i >> 1, h = i & 1;
      if (f == 0) fa[0].h[h] = lds_rdtr(pa0 + h * 1792);
      else if (f < 5) fb[f - 1].h[h] = lds_rdtr(pb[f - 1] + h * 1024);
      else if (f < 10) fa[f - 4].h[h] = lds_rdtr((((f - 4) & 1) ? pa1 : pa0) + ((f - 4) >> 1) * 64 + h * 1792);
      else if (f == 10) fa[6].h[h] = lds_rdtr(pa2 + h * 1792);
      else if (i == 22) { dl = (slot == B1_NS - 1) ? -(B1_NS - 1) * B1_STAGE : B1_STAGE; slot = (slot == B1_NS - 1) ? 0 : slot + 1; }
      else if (i == 23) { pa0 += dl; pb[0] += dl; asm volatile("" : "+v"(pa0), "+v"(pb[0])); }
      else if (i == 24) { pb[1] += dl; pb[2] += dl; asm volatile("" : "+v"(pb[1]), "+v"(pb[2])); }
      else if (i == 25) { pb[3] += dl; pa1 += dl; asm volatile("" : "+v"(pb[3]), "+v"(pa1)); }
      else if (i == 26) { pa2 += dl; asm volatile("" : "+v"(pa2)); }
    };
    STAMP(15);
    bar();
    STAMP(3);
#pragma unroll
    for (int i = 0; i < 28; ++i) side(xa, xb, i);
#pragma unroll 1
    for (int t = 0; t < B1_NT; t += 2) {
      SCHED_FENCE(); wait_lgkm0(); bar(); SCHED_FENCE();
      phase<28>(acc, xa, xb, [&](int i) { side(ya, yb, i); });
      wait_lgkm0(); bar(); SCHED_FENCE();
      phase<28>(acc, ya, yb, [&](int i) { side(xa, xb, i); });
    }
    RELANE();
    STAMP(4);
    const rsrc_t ru = mk_rsrc(a.ws + WS_U, D * F * 2);
#pragma unroll
    for (int n = 0; n < 4; ++n) {
      const unsigned vo = (unsigned)((dd0 + 64 * wn + 16 * n + cl) * 64);
      u32x2 w[7];
#pragma unroll
      for (int m = 0; m < 7; ++m) {
        const f32x2 v0 = gelu_tanh2((f32x2){acc[m][n][0], acc[m][n][1]}), v1 = gelu_tanh2((f32x2){acc[m][n][2], acc[m][n][3]});
        w[m][0] = pk_bf16(v0.x, v0.y); w[m][1] = pk_bf16(v1.x, v1.y);
      }
#pragma unroll
      for (int mm = 0; mm < 3; ++mm) {
        const bool sw = g & 1;
        const u32x2 lo = sw ? w[2 * mm + 1] : w[2 * mm], hi = sw ? w[2 * mm] : w[2 * mm + 1];
        st16_wt(ru, vo + 16 * g, (unsigned)((ft * 7 + 3 * wm + mm) * D * 64), (u32x4){lo[0], lo[1], hi[0], hi[1]});
      }
      st8_wt(ru, vo + 8 * g, (unsigned)((ft * 7 + 6) * D * 64 + 32 * wm), w[6]);
    }
    STAMP(7);
    publish(LDS_CNT, sy + SY_U + dt1 * 16 + ft, tag, ln);
    STAMP(8);
  }

  f32x4 acc[4][4];
#pragma unroll
  for (int m = 0; m < 4; ++m)
#pragma unroll
    for (int n = 0; n < 4; ++n) acc[m][n] = (f32x4){0.f, 0.f, 0.f, 0.f};
  const unsigned au0 = (unsigned)(wm * 4096 + cl * 64 + ((g ^ (((q4 & 1) << 1) ^ ((q4 >> 1) * 3))) * 16));
  unsigned pa0 = (unsigned)((wm * 32 + cl) * 128 + ((g ^ (cl >> 1)) * 16)), pa1 = pa0 ^ 64u;
  const unsigned sw0 = pa0 ^ au0 ^ 65536u, sw1 = pa1 ^ (au0 + 1024u) ^ 65536u;
  unsigned pb[4];
#pragma unroll
  for (int n = 0; n < 4; ++n) pb[n] = (unsigned)(16384 + (8 * g + q4) * 256 + ((8 * wn + 2 * n + (p4 >> 1)) ^ ((q4 << 1) | ((g & 1) << 3))) * 16 + (p4 & 1) * 8);
  bf16x8 xa[4], ya[4];
  TrFrag xb[4], yb[4];
  static_assert(P2_NS == 4 && NAB % 2 == 0 && NBA % 2 == 0 && NAB >= P2_NS && NAB <= 16 && PF_NST2 < 29 - NAB, "phase-2 loop: two stages per trip, 64-KB windows");
  auto rd = [&](bf16x8* fa, TrFrag* fb, int so, int ks, int r) {
    const unsigned pa_ = (ks ? pa1 : pa0) + so;
    if (r == 0) fa[0] = lds_rd128(pa_);
    else if (r < 9) fb[(r - 1) >> 1].h[(r - 1) & 1] = lds_rdtr(pb[(r - 1) >> 1] + so + ks * 8192 + ((r - 1) & 1) * 1024);
    else if (r < 12) fa[r - 8] = lds_rd128(pa_ + ((r - 8) >> 1) * 8192 + ((r - 8) & 1) * 2048);
  };
  STAMP(18);
  bar();
  STAMP(9);
#pragma unroll
  for (int r = 0; r < 12; ++r) rd(xa, xb, 0, 0, r);
#pragma unroll 1
  for (int t = 0; t < nt2; t += 2) {
    unsigned x0, x1;
    SCHED_FENCE(); wait_lgkm0(); SCHED_FENCE();
    phase<16>(acc, xa, xb, [&](int i) { rd(ya, yb, 0, 1, i); });
    wait_lgkm0(); bar(); SCHED_FENCE();
    phase<16>(acc, ya, yb, [&](int i) {
      rd(xa, xb, P2_STAGE, 0, i);
      if (i == 13) { const bool sw = (t + 2 == nC); x0 = sw ? sw0 : 65536u; x1 = sw ? sw1 : 65536u; asm volatile("" : "+v"(x0), "+v"(x1)); }
      if (i == 14) { pa0 ^= x0; asm volatile("" : "+v"(pa0)); }
    });
    wait_lgkm0(); SCHED_FENCE();
    phase<16>(acc, xa, xb, [&](int i) {
      rd(ya, yb, P2_STAGE, 1, i);
      if (i == 12) { pb[0] ^= 65536u; pb[1] ^= 65536u; asm volatile("" : "+v"(pb[0]), "+v"(pb[1])); }
      if (i == 13) { pb[2] ^= 65536u; pb[3] ^= 65536u; asm volatile("" : "+v"(pb[2]), "+v"(pb[3])); }
      if (i == 14) { pa1 ^= x1; asm volatile("" : "+v"(pa1)); }
    });
    wait_lgkm0(); bar(); SCHED_FENCE();
    phase<16>(acc, ya, yb, [&](int i) { rd(xa, xb, 0, 0, i); });
  }
  STAMP(10);
  const rsrc_t rxb = mk_rsrc(a.ws + WS_XB, 256 * 32768);
  const unsigned xo = (unsigned)(wid * 8192 + ln * 16);
#pragma unroll
  for (int mi = 0; mi < 2; ++mi)
#pragma unroll
    for (int n = 0; n < 4; ++n) st16_wt(rxb, xo + (mi * 4 + n) * 1024, bid * 32768, __builtin_bit_cast(u32x4, acc[2 + mi][n]));
#pragma unroll
  for (int n = 0; n < 4; ++n) {
    const int tl = 64 * wn + 16 * n + cl;
    const unsigned ms = *(LAS unsigned*)(lds + LDS_RES + 16384 + tl * 8);
    const float mb = bflo(ms), rs_ = __builtin_amdgcn_rcpf(bfhi(ms)), b2 = bf2f(*(LAS u16*)(lds + LDS_RES + 17408 + tl * 2));
#pragma unroll
    for (int mi = 0; mi < 2; ++mi) {
      const int c8 = 4 * wm + 2 * mi + (g >> 1);
      const u32x2 xv = *(LAS u32x2*)(lds + LDS_RES + tl * 128 + ((c8 ^ (tl & 7)) * 16) + (g & 1) * 8);
      const u32x2 dk = *(LAS u32x2*)(lds + LDS_RES + 17664 + c8 * 16 + (g & 1) * 8);
#pragma unroll
      for (int h = 0; h < 2; ++h) {
        const float x0 = bflo(xv[h]), x1 = bfhi(xv[h]);
        const unsigned xn = pk_bf16((x0 - mb) * rs_, (x1 - mb) * rs_);
        acc[mi][n][2 * h] += fmaf(bflo(xn), bflo(dk[h]), x0 + b2); acc[mi][n][2 * h + 1] += fmaf(bfhi(xn), bfhi(dk[h]), x1 + b2);
      }
      asm volatile("" : "+v"(acc[mi][n]));
    }
  }
  wait_vm<0>();
  bar();
  STAMP(11);
  if (wid == 0 && ln == 0) __hip_atomic_store(sy + SY_X + bid, tag, RLX_AGENT);
  if (wid == 0) {
#pragma unroll 1
    for (unsigned spins = 0; spins < SPIN_CAP; ++spins) {
      if (__all(__hip_atomic_load(sy + SY_X + pa, RLX_AGENT) == tag)) break;
      __builtin_amdgcn_s_sleep(1);
    }
  }
  STAMP(12);
  bar();
#pragma unroll
  for (int mi = 0; mi < 2; ++mi)
#pragma unroll
    for (int n = 0; n < 4; ++n) {
      const u32x4 r = __builtin_amdgcn_raw_buffer_load_b128(rxb, xo + (mi * 4 + n) * 1024, pa * 32768, 16);
      acc[mi][n] += __builtin_bit_cast(f32x4, r);
    }
#pragma unroll
  for (int mi = 0; mi < 2; ++mi)
#pragma unroll
    for (int n = 0; n < 4; ++n) {
      const int d = d0 + 64 * kh + 32 * wm + 16 * mi + 4 * g, tok = t0 + 64 * wn + 16 * n + cl;
      __builtin_amdgcn_raw_buffer_store_b128(__builtin_bit_cast(u32x4, acc[mi][n]), mk_rsrc(a.out, S * D * 4), (unsigned)(tok * D + d) * 4u, 0, 16);
    }
  STAMP(13);
}

extern "C" void kernel_launch(void* const* d_in, const int* in_sizes, int n_in, void* d_out, int out_size, void* d_ws, size_t ws_size,
                              hipStream_t stream) {
  (void)hipFuncSetAttribute((const void*)fused, hipFuncAttributeMaxDynamicSharedMemorySize, LDS_BYTES);
  Args a;
  a.x = (const u16*)d_in[0]; a.Lre = (const u16*)d_in[1]; a.Lim = (const u16*)d_in[2]; a.Bre = (const u16*)d_in[3]; a.Bim = (const u16*)d_in[4];
  a.Cre = (const u16*)d_in[5]; a.Cim = (const u16*)d_in[6]; a.logdt = (const u16*)d_in[7]; a.Dsk = (const u16*)d_in[8];
  a.w1 = (const u16*)d_in[10]; a.b1 = (const u16*)d_in[11]; a.w2 = (const u16*)d_in[12]; a.b2 = (const u16*)d_in[13];
  a.out = (float*)d_out; a.ws = (char*)d_ws;
  fused<<<256, 512, LDS_BYTES, stream>>>(a);
}
```

```cpp
#include <hip/hip_runtime.h>
#include <stdint.h>

typedef unsigned short u16;
typedef unsigned long long u64;
typedef __bf16 bf16x8 __attribute__((ext_vector_type(8)));
typedef short s16x4 __attribute__((ext_vector_type(4)));
typedef short s16x8 __attribute__((ext_vector_type(8)));
typedef float f32x2 __attribute__((ext_vector_type(2)));
typedef float f32x4 __attribute__((ext_vector_type(4)));
typedef unsigned u32x4 __attribute__((ext_vector_type(4)));
typedef unsigned u32x2 __attribute__((ext_vector_type(2)));
#define LAS __attribute__((address_space(3)))
#define GAS __attribute__((address_space(1)))
typedef GAS unsigned long long gu64;
typedef __amdgpu_buffer_rsrc_t rsrc_t;
#define RLX_AGENT __ATOMIC_RELAXED, __HIP_MEMORY_SCOPE_AGENT

constexpr int S = 2048, D = 1024, P = 1024, F = 3584;
constexpr size_t MB = 1024 * 1024;
constexpr size_t WS_HL = 4 * MB, WS_HT = 8 * MB, WS_U = 16 * MB, WS_XB = 24 * MB, WS_SYNC = 32 * MB;
constexpr int SY_LN = 64, SY_H = 320, SY_U = 448, SY_X = 576, SY_ST = 1024, SY_SB = 3072, SY_GR = 5120;
constexpr unsigned SYNC_BYTES = (SY_GR + 8 * P * 2) * 8;
constexpr unsigned SPIN_CAP = 1u << 17;
#define NBA 24
#define NAB 6
constexpr unsigned CODE_PF_BYTES = 36 * 1024;
constexpr unsigned long long CB_ORDER = 0x80F7E6D5C491B3A2ull;
#define XB_BF16 1
#define PF_NST 11
#define PF_ST0 1
#define PF_N1 13
#define PF_NST2 20
#define PF_WB 4
#define A1_RS 2
#define PSTAMP(w, i)
#define STAMP_INIT()
#define STAMP_DUMP()
#define TRACE(e)
#define TRACE_DUMP()
#define STAMP(i) PSTAMP(0, i)

__device__ __forceinline__ float bf2f(u16 v) { return __uint_as_float(((unsigned)v) << 16); }
__device__ __forceinline__ float bflo(unsigned v) { return __uint_as_float(v << 16); }
__device__ __forceinline__ float bfhi(unsigned v) { return __uint_as_float(v & 0xffff0000u); }
__device__ __forceinline__ unsigned pk_bf16(float lo, float hi) {
  typedef __bf16 bf16x2 __attribute__((ext_vector_type(2)));
  f32x2 v = {lo, hi};
  bf16x2 r = __builtin_convertvector(v, bf16x2);
  return __builtin_bit_cast(unsigned, r);
}
__device__ __forceinline__ float rbf(float f) { return bflo(pk_bf16(f, f)); }
__device__ __forceinline__ float dot2(unsigned a, unsigned b, float c) {
  typedef __bf16 bf16x2 __attribute__((ext_vector_type(2)));
  return __builtin_amdgcn_fdot2_f32_bf16(__builtin_bit_cast(bf16x2, a), __builtin_bit_cast(bf16x2, b), c, false);
}
__device__ __forceinline__ f32x2 fma2(f32x2 a, f32x2 b, f32x2 c) { return __builtin_elementwise_fma(a, b, c); }
__device__ __forceinline__ f32x2 bc2(float v) { return (f32x2){v, v}; }

__device__ __forceinline__ rsrc_t mk_rsrc(const void* p, unsigned bytes) { return __builtin_amdgcn_make_buffer_rsrc((void*)p, 0, bytes, 0x00020000); }
__device__ __forceinline__ void dma16(rsrc_t rs, unsigned voff, unsigned soff, LAS char* l) { __builtin_amdgcn_raw_ptr_buffer_load_lds(rs, (LAS void*)l, 16, voff, soff, 0, 0); }
__device__ __forceinline__ void dma16c(rsrc_t rs, unsigned voff, unsigned soff, LAS char* l) { __builtin_amdgcn_raw_ptr_buffer_load_lds(rs, (LAS void*)l, 16, voff, soff, 0, 16); }
__device__ __forceinline__ void dma4(rsrc_t rs, unsigned voff, unsigned soff, LAS char* l) { __builtin_amdgcn_raw_ptr_buffer_load_lds(rs, (LAS void*)l, 4, voff, soff, 0, 0); }
__device__ __forceinline__ void st16_wt(rsrc_t rs, unsigned voff, unsigned soff, u32x4 v) { __builtin_amdgcn_raw_buffer_store_b128(v, rs, voff + soff, 0, 16); }
__device__ __forceinline__ void st8_wt(rsrc_t rs, unsigned voff, unsigned soff, u32x2 v) { __builtin_amdgcn_raw_buffer_store_b64(v, rs, voff + soff, 0, 16); }
template <int N> __device__ __forceinline__ void wait_vm() { asm volatile("s_waitcnt vmcnt(%0)" ::"n"(N) : "memory"); }
__device__ __forceinline__ void wait_lgkm0() { asm volatile("" ::: "memory"); __builtin_amdgcn_s_waitcnt(0xC07F); asm volatile("" ::: "memory"); }
__device__ __forceinline__ void bar() {
  asm volatile("" ::: "memory");
  __builtin_amdgcn_s_barrier();
  asm volatile("" ::: "memory");
}
#define SCHED_FENCE() __builtin_amdgcn_sched_barrier(0)
__device__ __forceinline__ bf16x8 lds_rd128(unsigned p) { return *(LAS bf16x8*)(size_t)p; }
__device__ __forceinline__ s16x4 lds_rdtr(unsigned p) { return __builtin_amdgcn_ds_read_tr16_b64_v4i16((LAS s16x4*)(size_t)p); }
struct TrFrag { s16x4 h[2]; };
__device__ __forceinline__ bf16x8 frag(const bf16x8& f) { return f; }
__device__ __forceinline__ bf16x8 frag(const TrFrag& f) { s16x8 ab = __builtin_shufflevector(f.h[0], f.h[1], 0, 1, 2, 3, 4, 5, 6, 7); return __builtin_bit_cast(bf16x8, ab); }
#define MFMA(a, b, c) __builtin_amdgcn_mfma_f32_16x16x32_bf16(frag(a), frag(b), (c), 0, 0, 0)
template <int NM, class FA, class FB, class Side>
__device__ __forceinline__ void phase(f32x4 (*acc)[4], const FA* fa, const FB* fb, Side side) {
#pragma unroll
  for (int i = 0; i < NM; ++i) {
    acc[i >> 2][i & 3] = MFMA(fa[i >> 2], fb[i & 3], acc[i >> 2][i & 3]);
    side(i);
    SCHED_FENCE();
  }
}

template <int NL> __device__ __forceinline__ void wait_flags(gu64* p, bool active, u64 tag) {
#pragma unroll 1
  for (unsigned spins = 0; spins < SPIN_CAP; ++spins) {
    bool ok = true;
    if (active) {
#pragma unroll
      for (int k = 0; k < NL; ++k) ok &= __hip_atomic_load(p + 64 * k, RLX_AGENT) == tag;
    }
    if (__all(ok)) break;
    __builtin_amdgcn_s_sleep(2);
  }
  __builtin_amdgcn_fence(__ATOMIC_ACQUIRE, "wavefront");
}

template <int NL> __device__ __forceinline__ int flag_prefix(gu64* p, int n, int need, u64 tag, int lane) {
  int pre = 0;
#pragma unroll 1
  for (unsigned spins = 0; spins < SPIN_CAP; ++spins) {
    const u64 m0 = ~__ballot(lane >= n || __hip_atomic_load(p, RLX_AGENT) == tag);
    const u64 m1 = NL == 2 ? ~__ballot(__hip_atomic_load(p + 64, RLX_AGENT) == tag) : 0ull;
    pre = m0 ? __builtin_ctzll(m0) : m1 ? 64 + __builtin_ctzll(m1) : 128;
    if (pre >= need) break;
    __builtin_amdgcn_s_sleep(1);
  }
  __builtin_amdgcn_fence(__ATOMIC_ACQUIRE, "wavefront");
  return pre < need ? 128 : pre;
}
__device__ __forceinline__ void lds_wait(unsigned addr, unsigned want) {
#pragma unroll 1
  for (unsigned spins = 0; spins < (SPIN_CAP << 4); ++spins) {
    if (__builtin_amdgcn_readfirstlane(__hip_atomic_load((LAS unsigned*)(size_t)addr, __ATOMIC_RELAXED, __HIP_MEMORY_SCOPE_WORKGROUP)) >= want) break;
    __builtin_amdgcn_s_sleep(1);
  }
  asm volatile("" ::: "memory");
}
__device__ __forceinline__ void publish(unsigned cnt, gu64* flag, u64 tag, int lane) {
  wait_vm<0>();
  wait_lgkm0();
  if (lane == 0 && __hip_atomic_fetch_add((LAS unsigned*)(size_t)cnt, 1u, __ATOMIC_RELAXED, __HIP_MEMORY_SCOPE_WORKGROUP) == 3u) __hip_atomic_store(flag, tag, RLX_AGENT);
}

template <class Issue> __device__ __forceinline__ void prod_fill(int n, Issue issue) {
#pragma unroll 1
  for (int t = 0; t < n; ++t) issue();
}
template <int L, int NS, class Issue> __device__ __forceinline__ void prod_run(int n, Issue issue) {
#pragma unroll 1
  for (int t = 0; t < n; ++t) { wait_vm<(NS - 2) * L>(); bar(); issue(); }
}
template <int L, int K, int X = 0> __device__ __forceinline__ void prod_tail() {
  wait_vm<K * L + X>();
  bar();
  if constexpr (K > 0) prod_tail<L, K - 1, X>();
}

__device__ __forceinline__ float wave_sum(float v) {
  v += __builtin_bit_cast(float, __builtin_amdgcn_update_dpp(0, __builtin_bit_cast(int, v), 0xB1, 0xf, 0xf, true));
  v += __builtin_bit_cast(float, __builtin_amdgcn_update_dpp(0, __builtin_bit_cast(int, v), 0x4E, 0xf, 0xf, true));
  v += __builtin_bit_cast(float, __builtin_amdgcn_update_dpp(0, __builtin_bit_cast(int, v), 0x141, 0xf, 0xf, true));
  v += __builtin_bit_cast(float, __builtin_amdgcn_update_dpp(0, __builtin_bit_cast(int, v), 0x140, 0xf, 0xf, true));
  const int iv = __builtin_bit_cast(int, v);
  const float a = __builtin_bit_cast(float, __builtin_amdgcn_readlane(iv, 0)), b = __builtin_bit_cast(float, __builtin_amdgcn_readlane(iv, 16));
  const float c = __builtin_bit_cast(float, __builtin_amdgcn_readlane(iv, 32)), d = __builtin_bit_cast(float, __builtin_amdgcn_readlane(iv, 48));
  return (a + b) + (c + d);
}

struct Args {
  const u16 *x, *Lre, *Lim, *Bre, *Bim, *Cre, *Cim, *logdt, *Dsk, *w1, *b1, *w2, *b2;
  float* out;
  char* ws;
};

constexpr int A1_NS = 3, A1_STAGE = 49152, A1_NT = D / 64;
constexpr int B1_NS = 7, B1_ABYTES = 32 * 448, B1_STAGE = B1_ABYTES + 32 * 256, B1_NT = S / 32;
constexpr int P2_NS = 4, P2_STAGE = 32768;
constexpr int LDS_BYTES = 160 * 1024;
constexpr int LDS_COEF = A1_NS * A1_STAGE;
constexpr int LDS_TOT = LDS_COEF + 14 * 256;
constexpr int LDS_SB = LDS_TOT + 512;
constexpr int LDS_STAT = LDS_SB + 512;
constexpr int LDS_RAW = LDS_STAT + 8 * 272;
constexpr int LDS_RES = P2_NS * P2_STAGE;
constexpr int LDS_RES2 = 158208;
constexpr int LDS_CNT = LDS_BYTES - 256 - 2048 - 64;
constexpr int LDS_FLG = LDS_BYTES - 256 - 2048;
constexpr int LDS_DUMMY = LDS_BYTES - 256;
static_assert(LDS_RAW + 3072 + 768 <= LDS_CNT && B1_NS * B1_STAGE <= LDS_CNT && LDS_RES + 16384 <= LDS_COEF && LDS_RAW + 3840 <= LDS_RES2 && LDS_RES2 + 1536 <= LDS_CNT && P2_NS * P2_STAGE <= LDS_COEF && LDS_RAW % 16 == 0, "lds");

__device__ __forceinline__ f32x2 gelu_tanh2(f32x2 x) {
  const f32x2 z = x * fma2(x * x, bc2(-0.10294323967f), bc2(-2.30220819813f));
  const f32x2 d = (f32x2){__builtin_amdgcn_exp2f(z.x), __builtin_amdgcn_exp2f(z.y)} + bc2(1.0f);
  return x * (f32x2){__builtin_amdgcn_rcpf(d.x), __builtin_amdgcn_rcpf(d.y)};
}
#define CMUL(or_, oi_, ar_, ai_, br_, bi_) do { const float t_ = (ar_) * (br_) - (ai_) * (bi_); oi_ = (ar_) * (bi_) + (ai_) * (br_); or_ = t_; } while (0)

__global__ __launch_bounds__(512, 2) void fused(Args a) {
  extern __shared__ __attribute__((aligned(16))) char smem[];
  LAS char* lds = (LAS char*)smem;
  u64 pc0;
  asm volatile("s_getpc_b64 %0" : "=s"(pc0));
  const int wid = __builtin_amdgcn_readfirstlane(threadIdx.x >> 6), lane = threadIdx.x & 63;
  const int bid = blockIdx.x, xc = bid & 7, idx = bid >> 3, xg = xc >> 1;
  const bool roleA = !(xc & 1);
  const int ri = bid >> 1;
  gu64* sy = (gu64*)(a.ws + WS_SYNC);
  u64 tag;
#define LOAD_TAG() do { const __attribute__((address_space(4))) u64* ep_ = (const __attribute__((address_space(4))) u64*)(a.ws + WS_SYNC); \
    asm volatile("" : "+s"(ep_)::"memory"); tag = *ep_ + 1ull; } while (0)
#define HOLD_TAG() asm volatile("" ::"s"(tag))
#define tag32 ((unsigned)tag)
  const int chunk = 4 * (xg & 1) + (idx & 3), cb = 8 * (xg >> 1) + (idx >> 2);
  const int ft = 4 * xg + (idx & 3), dt1 = idx >> 2;
  const int tt = 4 * xg + (idx & 3), dt = idx >> 2;
  const int t0 = tt * 128, d0 = dt * 128;
  u64 cbs = roleA ? CB_ORDER : CB_ORDER >> (2 * (32 - NAB));
  const int nC = roleA ? 32 - NAB : NAB, ku0 = roleA ? 0 : NBA, nt2 = nC + (roleA ? NBA : 56 - NBA);
  const rsrc_t rsy = mk_rsrc(a.ws + WS_SYNC, SYNC_BYTES);
  STAMP_INIT();
  STAMP(0);

  auto touch = [&](const u16* base, unsigned bytes, int u0, int n) {
    const rsrc_t rs = mk_rsrc(base, bytes);
#pragma unroll 1
    for (int k = 0; k < n; ++k) dma4(rs, (unsigned)(lane * 128), (unsigned)(u0 + k) * 8192u, lds + LDS_DUMMY);
  };
  auto w1_voff = [&](int piece) {
    const int ch = piece * 64 + lane, k = ch / 28, pos = ch - k * 28;
    return (unsigned)(k * F + ft * 224 + (pos ^ (3 * ((k >> 3) & 1))) * 8) * 2u;
  };
  if (!roleA) {
    const int row = ri * 16 + wid * 2;
    u32x4 r[2][2], brow[2][2];
    {
      const u16* xp = a.x + (size_t)row * D;
      const u16* bp = (ri < 64 ? a.Bre : a.Bim) + (size_t)(row & 1023) * D;
      const unsigned lo = (unsigned)lane * 16u;
      asm volatile("global_load_dwordx4 %0, %4, %5\n\tglobal_load_dwordx4 %1, %4, %5 offset:1024\n\tglobal_load_dwordx4 %2, %4, %5 offset:2048\n\tglobal_load_dwordx4 %3, %4, %5 offset:3072"
                   : "=&v"(r[0][0]), "=&v"(r[0][1]), "=&v"(r[1][0]), "=&v"(r[1][1]) : "v"(lo), "s"(xp) : "memory");
      asm volatile("global_load_dwordx4 %0, %4, %5\n\tglobal_load_dwordx4 %1, %4, %5 offset:1024\n\tglobal_load_dwordx4 %2, %4, %5 offset:2048\n\tglobal_load_dwordx4 %3, %4, %5 offset:3072"
                   : "=&v"(brow[0][0]), "=&v"(brow[0][1]), "=&v"(brow[1][0]), "=&v"(brow[1][1]) : "v"(lo), "s"(bp) : "memory");
    }
    LOAD_TAG();
    bar();
    {
      const rsrc_t rs = mk_rsrc(a.w1, S * F * 2);
#pragma unroll
      for (int j = 0; j < 2; ++j) {
        const int piece = j == 0 ? wid : (wid < 6 ? wid + 8 : wid);
        const unsigned vo = w1_voff(piece);
#pragma unroll
        for (int t = 0; t < B1_NS - 1; ++t) dma16(rs, vo, t * (32 * F * 2), lds + t * B1_STAGE + piece * 1024);
      }
    }
    wait_vm<16>();
    STAMP(20);
    asm volatile("" : "+v"(r[0][0]), "+v"(r[0][1]), "+v"(r[1][0]), "+v"(r[1][1])::"memory");
    const rsrc_t rx = mk_rsrc(a.ws + WS_HL, S * D * 2);
#pragma unroll
    for (int pass = 0; pass < 2; ++pass) {
      float s1[2], s2[2];
#pragma unroll
      for (int i = 0; i < 2; ++i) {
        float a1 = 0.f, a2 = 0.f;
#pragma unroll
        for (int k = 0; k < 8; ++k) { const unsigned w = r[i][k >> 2][k & 3]; a1 = dot2(w, 0x3f803f80u, a1); a2 = dot2(w, w, a2); }
        s1[i] = a1; s2[i] = a2;
      }
#pragma unroll
      for (int i = 0; i < 2; ++i) { s1[i] = wave_sum(s1[i]); s2[i] = wave_sum(s2[i]); }
#pragma unroll
      for (int i = 0; i < 2; ++i) {
        const float mean = s1[i] * (1.0f / D), var = fmaxf(fmaf(-mean, mean, s2[i] * (1.0f / D)), 0.f);
        const unsigned ms = pk_bf16(mean, __builtin_amdgcn_sqrtf(rbf(rbf(var) + 1e-5f)));
        const float rs_ = __builtin_amdgcn_rcpf(bfhi(ms)), mb = bflo(ms);
#pragma unroll
        for (int k = 0; k < 8; ++k) {
          const unsigned w = r[i][k >> 2][k & 3];
          r[i][k >> 2][k & 3] = pk_bf16((bflo(w) - mb) * rs_, (bfhi(w) - mb) * rs_);
        }
        if (pass == 0) {
          if (lane == 0) __hip_atomic_store(sy + SY_ST + row + i, ((u64)tag32 << 32) | ms, RLX_AGENT);
        } else {
          st16_wt(rx, (unsigned)lane * 16u, (unsigned)((row + i) * D) * 2u, r[i][0]);
          st16_wt(rx, (unsigned)lane * 16u + 1024u, (unsigned)((row + i) * D) * 2u, r[i][1]);
        }
      }
    }
    STAMP(1);
    asm volatile("s_waitcnt vmcnt(0)" : "+v"(brow[0][0]), "+v"(brow[0][1]), "+v"(brow[1][0]), "+v"(brow[1][1])::"memory");
    bar();
    if (wid == 0 && lane == 0) __hip_atomic_store(sy + SY_LN + ri, tag, RLX_AGENT);
    STAMP(2);
    {
      float sums[2];
#pragma unroll
      for (int i = 0; i < 2; ++i) {
        float s = 0.f;
#pragma unroll
        for (int k = 0; k < 8; ++k) { const unsigned w = brow[i][k >> 2][k & 3]; s = dot2(w, 0x3f803f80u, s); }
        sums[i] = wave_sum(s);
      }
      if (lane < 2) __hip_atomic_store(sy + SY_SB + row + lane, ((u64)tag32 << 32) | __float_as_uint(lane ? sums[1] : sums[0]), RLX_AGENT);
    }
  }

  if (wid >= 4) {
    const int pw = wid - 4;
    if (pw == 3) {
      auto flags_fetch = [&]() {
        dma16c(rsy, (unsigned)lane * 16u, SY_LN * 8, lds + LDS_FLG);
        dma16c(rsy, (unsigned)lane * 16u, SY_LN * 8 + 1024, lds + LDS_FLG + 1024);
      };
      auto flags_check = [&]() {
        bool ok = true;
#pragma unroll
        for (int k = 0; k < 4; ++k) ok &= *(LAS u64*)(lds + LDS_FLG + k * 512 + lane * 8) == tag;
        if (!__all(ok)) wait_flags<4>(sy + SY_LN + lane, true, tag);
      };
      if (lane < 4) *(LAS unsigned*)(size_t)(LDS_CNT + lane * 4) = 0u;
      dma4(mk_rsrc((const void*)(pc0 & ~255ull), CODE_PF_BYTES), (unsigned)(lane * 128), (unsigned)((idx % 5) * 8192), lds + LDS_DUMMY);
      if (roleA) {
        st16_wt(rsy, (unsigned)lane * 16u, (unsigned)(SY_GR * 8 + (chunk * P + cb * 64) * 16), (u32x4){0u, 0u, 0u, 0u});
        dma4(mk_rsrc(a.Lre, P * 2), (unsigned)(lane & 31) * 4u, cb * 128, lds + LDS_RAW + 3072);
        dma4(mk_rsrc(a.Lim, P * 2), (unsigned)(lane & 31) * 4u, cb * 128, lds + LDS_RAW + 3072 + 256);
        dma4(mk_rsrc(a.logdt, P * 2), (unsigned)(lane & 31) * 4u, cb * 128, lds + LDS_RAW + 3072 + 512);
        LOAD_TAG(); HOLD_TAG();
        const int bst = PF_ST0 + ((ri * PF_NST) >> 7);
        auto step = [&](int t) {
          bar();
          if (t == bst) touch(a.w1, S * F * 2, 128 + PF_N1 * ri, PF_N1);
          if (t == bst + PF_NST) touch(a.w1, S * F * 2, 128 + 128 * PF_N1 + (13 - PF_N1) * ri, 13 - PF_N1);
        };
        PSTAMP(7, 24);
        step(0); step(1); step(2);
        if (bst <= 2) wait_vm<PF_N1>(); else wait_vm<0>();
        if (lane == 0) __hip_atomic_store(sy + SY_LN + 128 + ri, tag, RLX_AGENT);
        LAS float* cf = (LAS float*)(lds + LDS_COEF) + lane;
        const float lr = bf2f(*(LAS u16*)(lds + LDS_RAW + 3072 + lane * 2)), li = bf2f(*(LAS u16*)(lds + LDS_RAW + 3072 + 256 + lane * 2));
        const float dtt = rbf(expf(bf2f(*(LAS u16*)(lds + LDS_RAW + 3072 + 512 + lane * 2)))), er = expf(dtt * lr);
        float arg = dtt * li;
        asm volatile("" : "+v"(arg));
        step(3);
        float sn, cs;
        sincosf(arg, &sn, &cs);
        float pr = er * cs, pi = er * sn;
        asm volatile("" : "+v"(pr), "+v"(pi));
        step(4);
        const float nr = pr - 1.0f, den = lr * lr + li * li;
        cf[0] = pr; cf[64] = pi;
        cf[128] = 2.0f * (nr * lr + pi * li) / den; cf[192] = 2.0f * (pi * lr - nr * li) / den;
        step(5);
#pragma unroll
        for (int k = 0; k < 8; ++k) {
          const float t = pr * pr - pi * pi; pi = 2.0f * pr * pi; pr = t;
          if (k >= 3) { cf[(4 + 2 * (k - 3)) * 64] = pr; cf[(5 + 2 * (k - 3)) * 64] = pi; }
        }
#pragma unroll 1
        for (int t = 6; t < 12; ++t) step(t);
        const unsigned sbo = (unsigned)(SY_SB * 8 + cb * 512 + (lane >> 5) * (P * 8) + (lane & 31) * 16);
#pragma unroll 1
        for (unsigned spins = 0;; ++spins) {
          dma16c(rsy, (unsigned)lane * 16u, (unsigned)(SY_ST * 8 + chunk * 2048), lds + LDS_RAW);
          dma16c(rsy, (unsigned)lane * 16u, (unsigned)(SY_ST * 8 + chunk * 2048 + 1024), lds + LDS_RAW + 1024);
          dma16c(rsy, sbo, 0, lds + LDS_RAW + 2048);
          if (spins == 0) { flags_fetch(); step(12); step(13); }
          wait_vm<0>();
          u32x2 gs[4], gb[2];
          bool ok = true;
#pragma unroll
          for (int k = 0; k < 4; ++k) { gs[k] = *(LAS u32x2*)(lds + LDS_RAW + k * 512 + lane * 8); ok &= gs[k][1] == tag32; }
#pragma unroll
          for (int k = 0; k < 2; ++k) { gb[k] = *(LAS u32x2*)(lds + LDS_RAW + 2048 + k * 512 + lane * 8); ok &= gb[k][1] == tag32; }
          if (__all(ok) || spins > SPIN_CAP) {
#pragma unroll
            for (int k = 0; k < 4; ++k) {
              LAS float* e = (LAS float*)(lds + LDS_STAT + (2 * k + (lane >> 5)) * 272 + (lane & 15) * 16 + ((lane >> 4) & 1) * 4);
              e[0] = -bflo(gs[k][0]); e[2] = __builtin_amdgcn_rcpf(bfhi(gs[k][0]));
            }
#pragma unroll
            for (int k = 0; k < 2; ++k) *(LAS unsigned*)(lds + LDS_SB + k * 256 + lane * 4) = gb[k][0];
            break;
          }
          __builtin_amdgcn_s_sleep(2);
        }
        bar();
        flags_check();
        wait_lgkm0();
        step(15);
      } else {
#pragma unroll 1
        for (int t = 0; t < 13; ++t) bar();
        flags_fetch();
#pragma unroll 1
        for (int t = 13; t < 20; ++t) bar();
        wait_vm<0>();
        flags_check();
        if (bid == 255 && lane == 0) __hip_atomic_store(sy, tag, RLX_AGENT);
#pragma unroll 1
        for (int t = 20; t < B1_NT; ++t) bar();
      }
    } else if (roleA) {
      const int T0 = chunk * 256, p0 = cb * 64;
      if (pw < 2) {
        const int i3 = lane >> 3;
        const unsigned ve = (unsigned)((T0 + 128 * pw + 32 * (i3 >> 2) + 16 * (i3 & 1) + ((i3 >> 1) & 1)) * D * 2 + ((lane & 7) ^ (lane >> 4)) * 16);
        const unsigned vo = (ve ^ 64u) + 64 * D * 2;
        const rsrc_t rs = mk_rsrc(a.x, S * D * 2);
        unsigned so = 0, lo = pw * 16384;
        auto issue = [&]() {
#pragma unroll
          for (int j = 0; j < 16; ++j) dma16(rs, (j & 1) ? vo : ve, so + (j >> 1) * (2 * D * 2), lds + lo + j * 1024);
          so += 128; lo = lo >= (A1_NS - 1) * A1_STAGE ? lo - (A1_NS - 1) * A1_STAGE : lo + A1_STAGE;
        };
        PSTAMP(4, 21);
        prod_fill(A1_NS - 1, issue);
        PSTAMP(4, 22);
        LOAD_TAG(); HOLD_TAG();
        prod_run<16, A1_NS>(A1_NT - (A1_NS - 1), issue);
        prod_tail<16, A1_NS - 2>();
      } else {
        const unsigned ve = (unsigned)((p0 + (lane >> 3)) * D * 2 + ((lane & 7) ^ (lane >> 4)) * 16), vo = ve ^ 64u;
        const rsrc_t rre = mk_rsrc(a.Bre, P * D * 2), rim = mk_rsrc(a.Bim, P * D * 2);
        unsigned so = 0, lo = 32768;
        auto issue = [&]() {
#pragma unroll
          for (int j = 0; j < 8; ++j) {
            dma16(rre, (j & 1) ? vo : ve, so + j * (8 * D * 2), lds + lo + (j >> 1) * 4096 + (j & 1) * 1024);
            dma16(rim, (j & 1) ? vo : ve, so + j * (8 * D * 2), lds + lo + (j >> 1) * 4096 + (j & 1) * 1024 + 2048);
          }
          so += 128; lo = lo >= (A1_NS - 1) * A1_STAGE ? lo - (A1_NS - 1) * A1_STAGE : lo + A1_STAGE;
        };
        prod_fill(A1_NS - 1, issue);
        LOAD_TAG(); HOLD_TAG();
        prod_run<16, A1_NS>(A1_NT - (A1_NS - 1), issue);
        prod_tail<16, A1_NS - 2>();
      }
    } else {
      const int dd0 = dt1 * 128;
      if (pw < 2) {
        unsigned voff[7];
#pragma unroll
        for (int j = 0; j < 7; ++j) voff[j] = w1_voff(pw * 7 + j);
        const rsrc_t rs = mk_rsrc(a.w1, S * F * 2);
        unsigned so = (B1_NS - 1) * (32 * F * 2), lo = (B1_NS - 1) * B1_STAGE + pw * 7168;
        auto issue = [&]() {
#pragma unroll
          for (int j = 0; j < 7; ++j) dma16(rs, voff[j], so, lds + lo + j * 1024);
          so += 32 * F * 2; lo = lo >= (B1_NS - 1) * B1_STAGE ? lo - (B1_NS - 1) * B1_STAGE : lo + B1_STAGE;
        };
        prod_run<7, B1_NS>(B1_NT - (B1_NS - 1), issue);
        prod_tail<7, B1_NS - 2>();
      } else {
        unsigned voff[2];
#pragma unroll
        for (int j = 0; j < 2; ++j) {
          const int k = j * 8 + (lane >> 4), pos = lane & 15;
          const int c = pos ^ (((k & 3) << 1) | (((k >> 3) & 1) << 3));
          voff[j] = (unsigned)((lane >> 4) * D + dd0 + c * 8) * 2u;
        }
        const rsrc_t rs = mk_rsrc(a.ws + WS_HL, S * D * 2);
        unsigned so = 0, lo = B1_ABYTES;
        auto issue = [&]() {
#pragma unroll
          for (int j = 0; j < 8; ++j) dma16c(rs, voff[(j >> 1) & 1], so + j * 4 * (D * 2), lds + lo + j * 1024);
          so += 32 * D * 2; lo = lo >= (B1_NS - 1) * B1_STAGE ? lo - (B1_NS - 1) * B1_STAGE : lo + B1_STAGE;
        };
        int done = B1_NS - 1;
        wait_flags<1>(sy + SY_LN + lane, lane < 2 * done, tag);
        PSTAMP(6, 16);
        prod_fill(B1_NS - 1, issue);
        wait_vm<(B1_NS - 2) * 8>();
        bar();
        int pre = flag_prefix<2>(sy + SY_LN + lane, 64, 2 * done + 2, tag, lane);
        issue();
        ++done;
#pragma unroll 1
        while (pre < 128 && done < B1_NT) {
          const int n = pre / 2 - done;
          prod_run<8, B1_NS>(n, issue);
          done += n;
          wait_vm<(B1_NS - 2) * 8>();
          bar();
          pre = flag_prefix<2>(sy + SY_LN + lane, 64, 2 * done + 2, tag, lane);
          issue();
          ++done;
        }
        prod_run<8, B1_NS>(B1_NT - done, issue);
        prod_tail<8, B1_NS - 2>();
      }
    }
    bar();
    const int nU = nt2 - nC;
    if (pw == 1 || pw == 2) {
      const rsrc_t rxx = mk_rsrc(a.x, S * D * 2);
      const int dk = d0 + (roleA ? 0 : 64);
#pragma unroll
      for (int k = 0; k < 8; ++k) dma16(rxx, (unsigned)((lane >> 3) * (D * 2) + ((lane & 7) ^ (lane >> 3)) * 16), (unsigned)((t0 + 64 * (pw - 1) + 8 * k) * D + dk) * 2u, lds + LDS_RES + (8 * (pw - 1) + k) * 1024);
      if (pw == 1) dma16c(rsy, (unsigned)lane * 16u, (unsigned)(SY_ST + t0) * 8u, lds + LDS_RES2);
      else { dma4(mk_rsrc(a.b2, S * 2), (unsigned)lane * 4u, (unsigned)t0 * 2u, lds + LDS_RES2 + 1024); dma4(mk_rsrc(a.Dsk, D * 2), (unsigned)lane * 4u, (unsigned)dk * 2u, lds + LDS_RES2 + 1280); }
      wait_vm<0>();
      if (lane == 0) __hip_atomic_fetch_add((LAS unsigned*)(size_t)(LDS_CNT + 12), 1u, __ATOMIC_RELAXED, __HIP_MEMORY_SCOPE_WORKGROUP);
    }
    if (pw == 0 || pw == 3) {
      const int dh = d0 + (((pw ? 1 : 0) ^ (roleA ? 0 : 1)) << 6), r4 = lane >> 4;
      const unsigned vce = (unsigned)(dh + (lane >> 3)) * (P * 2) + (unsigned)(((lane & 7) ^ (lane >> 4)) * 16), vco = vce ^ 64u;
      const unsigned vu = (unsigned)((dh + (lane >> 2)) * 64 + (((lane & 3) ^ (((r4 & 1) << 1) ^ ((r4 >> 1) * 3))) * 16));
      const rsrc_t ru = mk_rsrc(a.ws + WS_U, D * F * 2);
      rsrc_t rc = mk_rsrc(a.Cre, D * P * 2), rc2 = mk_rsrc(a.Cim, D * P * 2);
      unsigned so = 0, lo = pw ? 8192 : 0, sh = 0;
      auto issueC = [&]() {
        so = ((unsigned)cbs & 15u) * 128;
#pragma unroll
        for (int j = 0; j < 8; ++j) dma16(rc, (j & 1) ? vco : vce, so + j * 8 * (P * 2), lds + lo + j * 1024);
        const rsrc_t t_ = rc; rc = rc2; rc2 = t_;
        cbs >>= sh; sh ^= 4; lo = (lo + P2_STAGE) & (P2_NS * P2_STAGE - 1);
      };
      auto issueU = [&]() {
#pragma unroll
        for (int j = 0; j < 8; ++j) dma16c(ru, vu, so + (j & 1) * (D * 64) + (j >> 1) * 1024, lds + lo + j * 1024);
        so += 2 * D * 64; lo = (lo + P2_STAGE) & (P2_NS * P2_STAGE - 1);
      };
      prod_fill(P2_NS - 1, issueC);
      constexpr int UA = NBA * 32, WB = PF_WB * 128 / 2;
      constexpr int RA = (UA - WB + 127) / 128, RB = (1792 - UA - WB + 127) / 128;
      if (roleA) {
        if (pw == 3) { touch(a.Cre, D * P * 2, 2 * ri, 2); touch(a.Cim, D * P * 2, 2 * ri, 2); }
        const int bst = 1 + ((ri * PF_NST2) >> 7);
        prod_run<8, P2_NS>(bst, issueC);
        if (pw == 3) { touch(a.w2, F * S * 2, UA + WB + RB * ri, RB); touch(a.w2, F * S * 2, WB + RA * ri, RA); }
        prod_run<8, P2_NS>(nC - 3 - bst, issueC);
      } else {
        if (pw == 3) { touch(a.w2, F * S * 2, UA + (PF_WB / 2) * ri, PF_WB / 2); touch(a.w2, F * S * 2, (PF_WB / 2) * ri, PF_WB / 2); }
        prod_run<8, P2_NS>(nC - 3, issueC);
      }
      so = ku0 * (2 * D * 64);
      wait_vm<16>();
      bar();
      wait_flags<1>(sy + SY_U + dt * 16 + (lane & 15), true, tag);
      PSTAMP(4, 19);
      issueU();
      prod_run<8, P2_NS>(nU - 1, issueU);
    } else {
      unsigned voff[2];
#pragma unroll
      for (int j = 0; j < 2; ++j) {
        const int k = j * 8 + (lane >> 4), pos = lane & 15;
        const int c = pos ^ (((k & 3) << 1) | (((k >> 3) & 1) << 3));
        voff[j] = (unsigned)(((pw - 1) * 32 + (lane >> 4)) * S + t0 + c * 8) * 2u;
      }
      const unsigned vh = (unsigned)(lane * 16 + (pw - 1) * 8192);
      const rsrc_t rh = mk_rsrc(a.ws + WS_HT, 2 * P * S * 2), rw = mk_rsrc(a.w2, F * S * 2);
      unsigned so = 0, lo = 16384 + (pw - 1) * 8192, sh = 0;
      gu64* hf = sy + SY_H + (tt >> 1) * 16 + ((unsigned)(cbs >> (4 * (lane & 15))) & 15u);
      auto issueH = [&]() {
        so = (tt * 32 + sh * 4 + ((unsigned)cbs & 15u)) * 16384;
#pragma unroll
        for (int j = 0; j < 8; ++j) dma16c(rh, vh, so + j * 1024, lds + lo + j * 1024);
        cbs >>= sh; sh ^= 4; lo = (lo + P2_STAGE) & (P2_NS * P2_STAGE - 1);
      };
      auto issueW = [&]() {
#pragma unroll
        for (int j = 0; j < 8; ++j) dma16(rw, voff[(j >> 1) & 1], so + j * 4 * (S * 2), lds + lo + j * 1024);
        so += 64 * S * 2; lo = (lo + P2_STAGE) & (P2_NS * P2_STAGE - 1);
      };
      if (roleA) lds_wait(LDS_CNT, 4u);
      int done = P2_NS - 1, pre = flag_prefix<1>(hf, nC / 2, 2, tag, lane);
      PSTAMP(6, 17);
      prod_fill(P2_NS - 1, issueH);
#pragma unroll 1
      while (pre < 128 && done < nC) {
        const int n = (2 * pre < nC ? 2 * pre : nC) - done;
        prod_run<8, P2_NS>(n, issueH);
        done += n;
        if (done < nC) {
          wait_vm<16>();
          bar();
          pre = flag_prefix<1>(hf, nC / 2, done / 2 + 1, tag, lane);
          issueH();
          ++done;
        }
      }
      prod_run<8, P2_NS>(nC - done, issueH);
      so = ku0 * (64 * S * 2);
      prod_run<8, P2_NS>(nU, issueW);
    }
    prod_tail<8, P2_NS - 2>();
    return;
  }

  const int wm = wid >> 1, wn = wid & 1;
  int cl = lane & 15, g = lane >> 4, q4 = cl >> 2, p4 = lane & 3, ln = lane;
#define RELANE() do { ln = __builtin_amdgcn_mbcnt_hi(~0u, __builtin_amdgcn_mbcnt_lo(~0u, 0u)); asm volatile("" : "+v"(ln)); cl = ln & 15; g = ln >> 4; q4 = cl >> 2; p4 = ln & 3; } while (0)
  const int kh = roleA ? 0 : 1, pa = bid ^ 1;
  f32x4 res[2][4];
  auto residual = [&]() {
    lds_wait(LDS_CNT + 12, 2u);
#pragma unroll
    for (int n = 0; n < 4; ++n) {
      const int tl = 64 * wn + 16 * n + cl;
      const unsigned ms = *(LAS unsigned*)(lds + LDS_RES2 + tl * 8);
      const float rs_ = __builtin_amdgcn_rcpf(bfhi(ms)), mb = bflo(ms), b2 = bf2f(*(LAS u16*)(lds + LDS_RES2 + 1024 + tl * 2));
#pragma unroll
      for (int mi = 0; mi < 2; ++mi) {
        const int c8 = 4 * wm + 2 * mi + (g >> 1);
        const u32x2 xv = *(LAS u32x2*)(lds + LDS_RES + tl * 128 + ((c8 ^ (tl & 7)) * 16) + (g & 1) * 8);
        const u32x2 dk = *(LAS u32x2*)(lds + LDS_RES2 + 1280 + c8 * 16 + (g & 1) * 8);
#pragma unroll
        for (int h = 0; h < 2; ++h) {
          const float x0 = bflo(xv[h]), x1 = bfhi(xv[h]);
          const unsigned xn = pk_bf16((x0 - mb) * rs_, (x1 - mb) * rs_);
          res[mi][n][2 * h] = fmaf(bflo(xn), bflo(dk[h]), x0 + b2); res[mi][n][2 * h + 1] = fmaf(bfhi(xn), bfhi(dk[h]), x1 + b2);
        }
        asm volatile("" : "+v"(res[mi][n]));
      }
    }
  };

  if (roleA) {
    LOAD_TAG(); HOLD_TAG();
    RELANE();
    const int p0 = cb * 64;
    const unsigned rb = (unsigned)(cl * 128 + ((g ^ (cl >> 1)) * 16));
    unsigned pa0 = rb + wm * 16384, pa1 = pa0 ^ 64u, pb0 = rb + 32768 + wn * 8192, pb1 = pb0 ^ 64u;
    f32x4 acc[8][4];
#pragma unroll
    for (int m = 0; m < 8; ++m)
#pragma unroll
      for (int n = 0; n < 4; ++n) acc[m][n] = (f32x4){0.f, 0.f, 0.f, 0.f};
    bf16x8 xa[8], xb[4], ya[8], yb[4];
    auto rd = [&](bf16x8* fa, bf16x8* fb, unsigned pa_, unsigned pb_, int r) {
      if (r == 0) fa[0] = lds_rd128(pa_); else if (r < 5) fb[r - 1] = lds_rd128(pb_ + (r - 1) * 2048); else fa[r - 4] = lds_rd128(pa_ + (r - 4) * 2048);
    };
    int slot = 0, dl = 0;
    STAMP(23);
    bar();
    STAMP(3);
#pragma unroll
    for (int r = 0; r < 12; ++r) rd(xa, xb, pa0, pb0, r);
#pragma unroll 1
    for (int t = 0; t < A1_NT; ++t) {
      SCHED_FENCE(); wait_lgkm0(); SCHED_FENCE();
      phase<32>(acc, xa, xb, [&](int i) {
        if (i % A1_RS == 0 && i / A1_RS < 12) rd(ya, yb, pa1, pb1, i / A1_RS);
        if (i == 25) { dl = (slot == A1_NS - 1) ? -(A1_NS - 1) * A1_STAGE : A1_STAGE; slot = (slot == A1_NS - 1) ? 0 : slot + 1; }
        if (i == 27) { pa0 += dl; pb0 += dl; asm volatile("" : "+v"(pa0), "+v"(pb0)); }
        if (i == 29) { pa1 += dl; pb1 += dl; asm volatile("" : "+v"(pa1), "+v"(pb1)); }
      });
      wait_lgkm0();
      bar();
      SCHED_FENCE();
      phase<32>(acc, ya, yb, [&](int i) { if (i % A1_RS == 0 && i / A1_RS < 12) rd(xa, xb, pa0, pb0, i / A1_RS); });
    }
    RELANE();
    STAMP(4);
    float cf[2][14], sb[4];
#pragma unroll
    for (int q = 0; q < 2; ++q) {
#pragma unroll
      for (int k = 0; k < 14; ++k) cf[q][k] = ((LAS float*)(lds + LDS_COEF))[k * 64 + 32 * wn + 16 * q + cl];
      sb[2 * q] = ((LAS float*)(lds + LDS_SB))[32 * wn + 16 * q + cl]; sb[2 * q + 1] = ((LAS float*)(lds + LDS_SB))[64 + 32 * wn + 16 * q + cl];
    }
    f32x2 er[2], ei[2];
#pragma unroll
    for (int q = 0; q < 2; ++q) { er[q] = bc2(0.f); ei[q] = bc2(0.f); }
#pragma unroll
    for (int m = 0; m < 8; ++m)
#pragma unroll
      for (int jj = 0; jj < 2; ++jj) {
        const f32x4 st = *(LAS f32x4*)(lds + LDS_STAT + (4 * wm + g) * 272 + (2 * m + jj) * 16);
        const f32x2 c2 = {st[0], st[1]}, r2 = {st[2], st[3]};
#pragma unroll
        for (int q = 0; q < 2; ++q) {
          const f32x2 ur = fma2(c2, bc2(sb[2 * q]), (f32x2){acc[m][2 * q][2 * jj], acc[m][2 * q][2 * jj + 1]}) * r2;
          const f32x2 ui = fma2(c2, bc2(sb[2 * q + 1]), (f32x2){acc[m][2 * q + 1][2 * jj], acc[m][2 * q + 1][2 * jj + 1]}) * r2;
          acc[m][2 * q][2 * jj] = ur.x; acc[m][2 * q][2 * jj + 1] = ur.y; acc[m][2 * q + 1][2 * jj] = ui.x; acc[m][2 * q + 1][2 * jj + 1] = ui.y;
          const f32x2 nr = fma2(bc2(cf[q][0]), er[q], fma2(bc2(-cf[q][1]), ei[q], ur));
          const f32x2 ni = fma2(bc2(cf[q][0]), ei[q], fma2(bc2(cf[q][1]), er[q], ui));
          er[q] = nr; ei[q] = ni;
        }
        if (jj == 1 && (m & 1)) asm volatile("" : "+v"(er[0]), "+v"(ei[0]), "+v"(er[1]), "+v"(ei[1]));
      }
    float sr[2], si[2], pr_[2], pi_[2];
    LAS float* tot = (LAS float*)(lds + LDS_TOT);
#pragma unroll
    for (int q = 0; q < 2; ++q) {
      float tr, ti;
      CMUL(tr, ti, cf[q][4], cf[q][5], er[q].x, ei[q].x);
      sr[q] = tr + er[q].y; si[q] = ti + ei[q].y;
      float vr = __shfl_up(sr[q], 16), vi = __shfl_up(si[q], 16);
      CMUL(tr, ti, cf[q][6], cf[q][7], vr, vi);
      if (g >= 1) { sr[q] += tr; si[q] += ti; }
      vr = __shfl_up(sr[q], 32); vi = __shfl_up(si[q], 32);
      CMUL(tr, ti, cf[q][8], cf[q][9], vr, vi);
      if (g >= 2) { sr[q] += tr; si[q] += ti; }
      vr = __shfl_up(sr[q], 16); vi = __shfl_up(si[q], 16);
      pr_[q] = g >= 1 ? vr : 0.f; pi_[q] = g >= 1 ? vi : 0.f;
      if (wm == 0 && g == 3) *(LAS f32x2*)(tot + (32 * wn + 16 * q + cl) * 2) = (f32x2){sr[q], si[q]};
    }
    if (wm == 0) {
      wait_lgkm0();
      if (ln == 0) __hip_atomic_store((LAS unsigned*)(size_t)(LDS_CNT + 4 + 4 * wn), 1u, __ATOMIC_RELAXED, __HIP_MEMORY_SCOPE_WORKGROUP);
    } else {
      lds_wait(LDS_CNT + 4 + 4 * wn, 1u);
    }
    f32x2 tq[2];
    gu64* gran = sy + SY_GR;
#pragma unroll
    for (int q = 0; q < 2; ++q) {
      tq[q] = *(LAS f32x2*)(tot + (32 * wn + 16 * q + cl) * 2);
      if (wm == 1 && g == 3 && chunk < 7) {
        float tr, ti;
        CMUL(tr, ti, cf[q][10], cf[q][11], tq[q].x, tq[q].y);
        gu64* gp = gran + ((size_t)chunk * P + p0 + 32 * wn + 16 * q + cl) * 2;
        __hip_atomic_store(gp, (1ull << 32) | __float_as_uint(tr + sr[q]), RLX_AGENT);
        __hip_atomic_store(gp + 1, (1ull << 32) | __float_as_uint(ti + si[q]), RLX_AGENT);
      }
    }
    STAMP(5);
#pragma unroll
    for (int m = 0; m < 8; ++m)
#pragma unroll
      for (int q = 0; q < 2; ++q) {
        const f32x4 ur = acc[m][2 * q], ui = acc[m][2 * q + 1];
        acc[m][2 * q] = ur * cf[q][2] - ui * cf[q][3];
        acc[m][2 * q + 1] = ui * (-cf[q][2]) - ur * cf[q][3];
        asm volatile("" : "+v"(acc[m][2 * q]), "+v"(acc[m][2 * q + 1]));
      }
    float hr[2] = {0.f, 0.f}, hi[2] = {0.f, 0.f};
    if (chunk > 0) {
      float gr[2][2], gi[2][2];
#pragma unroll 1
      for (unsigned spins = 0;; ++spins) {
        bool ok = true;
#pragma unroll
        for (int q = 0; q < 2; ++q)
#pragma unroll
          for (int k = 0; k < 2; ++k) {
            const int cc = (g + 4 * k < chunk) ? g + 4 * k : 0;
            gu64* gp = gran + ((size_t)cc * P + p0 + 32 * wn + 16 * q + cl) * 2;
            const u64 vr = __hip_atomic_load(gp, RLX_AGENT), vi = __hip_atomic_load(gp + 1, RLX_AGENT);
            ok &= ((vr >> 32) == 1ull) & ((vi >> 32) == 1ull);
            gr[q][k] = __uint_as_float((unsigned)vr); gi[q][k] = __uint_as_float((unsigned)vi);
          }
        if (__all(ok) || spins > SPIN_CAP) break;
        __builtin_amdgcn_s_sleep(1);
      }
#pragma unroll
      for (int q = 0; q < 2; ++q) {
        float ar_ = 0.f, ai_ = 0.f;
#pragma unroll
        for (int k = 0; k < 2; ++k) {
          float vr = gr[q][k], vi = gi[q][k];
          const int e = chunk - 1 - (g + 4 * k);
#pragma unroll
          for (int i = 0; i < 6 - 4 * k; ++i) {
            float tr, ti;
            CMUL(tr, ti, cf[q][12], cf[q][13], vr, vi);
            vr = i < e ? tr : vr; vi = i < e ? ti : vi;
          }
          ar_ += e >= 0 ? vr : 0.f; ai_ += e >= 0 ? vi : 0.f;
        }
        ar_ += __shfl_xor(ar_, 16); ai_ += __shfl_xor(ai_, 16);
        ar_ += __shfl_xor(ar_, 32); ai_ += __shfl_xor(ai_, 32);
        hr[q] = ar_; hi[q] = ai_;
      }
    }
    STAMP(6);
    f32x2 wr[2], wi[2];
#pragma unroll
    for (int q = 0; q < 2; ++q) {
      float tr, ti;
      if (wm == 1) { CMUL(tr, ti, cf[q][10], cf[q][11], hr[q], hi[q]); hr[q] = tr + tq[q].x; hi[q] = ti + tq[q].y; }
      CMUL(tr, ti, cf[q][6], cf[q][7], hr[q], hi[q]);
      if (g & 1) { hr[q] = tr; hi[q] = ti; }
      CMUL(tr, ti, cf[q][8], cf[q][9], hr[q], hi[q]);
      if (g & 2) { hr[q] = tr; hi[q] = ti; }
      hr[q] += pr_[q]; hi[q] += pi_[q];
      CMUL(tr, ti, cf[q][4], cf[q][5], hr[q], hi[q]);
      const f32x2 h0r = {hr[q], tr + er[q].x}, h0i = {hi[q], ti + ei[q].x};
      wr[q] = h0r * cf[q][2] - h0i * cf[q][3];
      wi[q] = h0i * (-cf[q][2]) - h0r * cf[q][3];
    }
    LAS char* scr = lds + wid * P2_STAGE + 16384;
    const unsigned fsw = (unsigned)(((cl & 3) << 1) | (((cl >> 3) & 1) << 3));
    const rsrc_t rh = mk_rsrc(a.ws + WS_HT, 2 * P * S * 2);
#pragma unroll
    for (int q = 0; q < 2; ++q) {
      const f32x2 ar2 = bc2(cf[q][0]), ai2 = bc2(cf[q][1]);
#pragma unroll
      for (int mh = 0; mh < 2; ++mh) {
        u32x4 o[2][2];
#pragma unroll
        for (int mq = 0; mq < 4; ++mq) {
          const int m = 4 * mh + mq;
          const f32x2 u0r = {acc[m][2 * q][0], acc[m][2 * q][1]}, u0i = {acc[m][2 * q + 1][0], acc[m][2 * q + 1][1]};
          const f32x2 u1r = {acc[m][2 * q][2], acc[m][2 * q][3]}, u1i = {acc[m][2 * q + 1][2], acc[m][2 * q + 1][3]};
          const f32x2 n0r = fma2(ar2, wr[q], fma2(ai2, wi[q], u0r)), n0i = fma2(ar2, wi[q], fma2(-ai2, wr[q], u0i));
          const f32x2 n1r = fma2(ar2, n0r, fma2(ai2, n0i, u1r)), n1i = fma2(ar2, n0i, fma2(-ai2, n0r, u1i));
          wr[q] = n1r; wi[q] = n1i;
          o[0][0][mq] = pk_bf16(n0r.x, n1r.x); o[0][1][mq] = pk_bf16(n0r.y, n1r.y);
          o[1][0][mq] = pk_bf16(n0i.x, n1i.x); o[1][1][mq] = pk_bf16(n0i.y, n1i.y);
        }
#pragma unroll
        for (int part = 0; part < 2; ++part)
#pragma unroll
          for (int run = 0; run < 2; ++run)
            *(LAS u32x4*)(scr + (part * 32 + 16 * q + cl) * 256 + (((unsigned)(4 * g + 2 * run + mh) ^ fsw) * 16)) = o[part][run];
      }
#pragma unroll
      for (int i = 0; i < 8; ++i) {
        const int part = i >> 2, pc = 4 * q + (i & 3);
        const u32x4 v = *(LAS u32x4*)(scr + part * 8192 + pc * 1024 + ln * 16);
        st16_wt(rh, (unsigned)(ln * 16 + pc * 1024), (unsigned)(((2 * chunk + wm) * 32 + part * 16 + cb) * 16384 + wn * 8192), v);
      }
      SCHED_FENCE();
    }
    STAMP(7);
    publish(LDS_CNT, sy + SY_H + chunk * 16 + cb, tag, ln);
    STAMP(8);
    residual();
  } else {
    RELANE();
    const int f0 = ft * 224, dd0 = dt1 * 128;
    const int k0 = 8 * g + q4;
    unsigned pa0 = (unsigned)(k0 * 448 + ((12 * wm + p4) ^ (3 * (g & 1))) * 16 + (p4 & 1) * 8);
    unsigned pa1 = (unsigned)(k0 * 448 + ((12 * wm + p4) ^ (3 * (g & 1))) * 16 + ((p4 & 1) ^ 1) * 8);
    unsigned pa2 = (unsigned)(k0 * 448 + ((24 + 2 * wm + (p4 >> 1)) ^ (3 * (g & 1))) * 16 + (p4 & 1) * 8);
    unsigned pb[4];
#pragma unroll
    for (int n = 0; n < 4; ++n) pb[n] = (unsigned)(B1_ABYTES + k0 * 256 + ((8 * wn + 2 * n + (p4 >> 1)) ^ ((q4 << 1) | ((g & 1) << 3))) * 16 + (p4 & 1) * 8);
    f32x4 acc[7][4];
#pragma unroll
    for (int m = 0; m < 7; ++m) {
      const u32x2 bv = *(const u32x2*)(a.b1 + f0 + (m < 6 ? 96 * wm + 32 * (m >> 1) + 8 * g + 4 * ((m & 1) ^ (g & 1)) : 192 + 16 * wm + 4 * g));
#pragma unroll
      for (int n = 0; n < 4; ++n) acc[m][n] = (f32x4){bflo(bv[0]), bfhi(bv[0]), bflo(bv[1]), bfhi(bv[1])};
    }
    TrFrag xa[7], xb[4], ya[7], yb[4];
    int slot = 0, dl = 0;
    auto side = [&](TrFrag* fa, TrFrag* fb, int i) {
      const int f = i >> 1, h = i & 1;
      if (f == 0) fa[0].h[h] = lds_rdtr(pa0 + h * 1792);
      else if (f < 5) fb[f - 1].h[h] = lds_rdtr(pb[f - 1] + h * 1024);
      else if (f < 10) fa[f - 4].h[h] = lds_rdtr((((f - 4) & 1) ? pa1 : pa0) + ((f - 4) >> 1) * 64 + h * 1792);
      else if (f == 10) fa[6].h[h] = lds_rdtr(pa2 + h * 1792);
      else if (i == 22) { dl = (slot == B1_NS - 1) ? -(B1_NS - 1) * B1_STAGE : B1_STAGE; slot = (slot == B1_NS - 1) ? 0 : slot + 1; }
      else if (i == 23) { pa0 += dl; pb[0] += dl; asm volatile("" : "+v"(pa0), "+v"(pb[0])); }
      else if (i == 24) { pb[1] += dl; pb[2] += dl; asm volatile("" : "+v"(pb[1]), "+v"(pb[2])); }
      else if (i == 25) { pb[3] += dl; pa1 += dl; asm volatile("" : "+v"(pb[3]), "+v"(pa1)); }
      else if (i == 26) { pa2 += dl; asm volatile("" : "+v"(pa2)); }
    };
    STAMP(15);
    bar();
    STAMP(3);
#pragma unroll
    for (int i = 0; i < 28; ++i) side(xa, xb, i);
#pragma unroll 1
    for (int t = 0; t < B1_NT; t += 2) {
      SCHED_FENCE(); wait_lgkm0(); bar(); SCHED_FENCE();
      phase<28>(acc, xa, xb, [&](int i) { side(ya, yb, i); });
      wait_lgkm0(); bar(); SCHED_FENCE();
      phase<28>(acc, ya, yb, [&](int i) { side(xa, xb, i); });
    }
    RELANE();
    STAMP(4);
    const rsrc_t ru = mk_rsrc(a.ws + WS_U, D * F * 2);
#pragma unroll
    for (int n = 0; n < 4; ++n) {
      const unsigned vo = (unsigned)((dd0 + 64 * wn + 16 * n + cl) * 64);
      u32x2 w[7];
#pragma unroll
      for (int m = 0; m < 7; ++m) {
        const f32x2 v0 = gelu_tanh2((f32x2){acc[m][n][0], acc[m][n][1]}), v1 = gelu_tanh2((f32x2){acc[m][n][2], acc[m][n][3]});
        w[m][0] = pk_bf16(v0.x, v0.y); w[m][1] = pk_bf16(v1.x, v1.y);
      }
#pragma unroll
      for (int mm = 0; mm < 3; ++mm) {
        const bool sw = g & 1;
        const u32x2 lo = sw ? w[2 * mm + 1] : w[2 * mm], hi = sw ? w[2 * mm] : w[2 * mm + 1];
        st16_wt(ru, vo + 16 * g, (unsigned)((ft * 7 + 3 * wm + mm) * D * 64), (u32x4){lo[0], lo[1], hi[0], hi[1]});
      }
      st8_wt(ru, vo + 8 * g, (unsigned)((ft * 7 + 6) * D * 64 + 32 * wm), w[6]);
    }
    STAMP(7);
    residual();
    publish(LDS_CNT, sy + SY_U + dt1 * 16 + ft, tag, ln);
    STAMP(8);
  }

  f32x4 acc[4][4];
#pragma unroll
  for (int n = 0; n < 4; ++n) { acc[0][n] = res[0][n]; acc[1][n] = res[1][n]; acc[2][n] = (f32x4){0.f, 0.f, 0.f, 0.f}; acc[3][n] = (f32x4){0.f, 0.f, 0.f, 0.f}; }
  const unsigned au0 = (unsigned)(wm * 4096 + cl * 64 + ((g ^ (((q4 & 1) << 1) ^ ((q4 >> 1) * 3))) * 16));
  unsigned pa0 = (unsigned)((wm * 32 + cl) * 128 + ((g ^ (cl >> 1)) * 16)), pa1 = pa0 ^ 64u;
  const unsigned sw0 = pa0 ^ au0 ^ 65536u, sw1 = pa1 ^ (au0 + 1024u) ^ 65536u;
  unsigned pb[4];
#pragma unroll
  for (int n = 0; n < 4; ++n) pb[n] = (unsigned)(16384 + (8 * g + q4) * 256 + ((8 * wn + 2 * n + (p4 >> 1)) ^ ((q4 << 1) | ((g & 1) << 3))) * 16 + (p4 & 1) * 8);
  bf16x8 xa[4], ya[4];
  TrFrag xb[4], yb[4];
  static_assert(P2_NS == 4 && NAB % 2 == 0 && NBA % 2 == 0 && NAB >= P2_NS && NAB <= 16 && PF_NST2 < 29 - NAB, "phase-2 loop: two stages per trip, 64-KB windows");
  auto rd = [&](bf16x8* fa, TrFrag* fb, int so, int ks, int r) {
    const unsigned pa_ = (ks ? pa1 : pa0) + so;
    if (r == 0) fa[0] = lds_rd128(pa_);
    else if (r < 9) fb[(r - 1) >> 1].h[(r - 1) & 1] = lds_rdtr(pb[(r - 1) >> 1] + so + ks * 8192 + ((r - 1) & 1) * 1024);
    else if (r < 12) fa[r - 8] = lds_rd128(pa_ + ((r - 8) >> 1) * 8192 + ((r - 8) & 1) * 2048);
  };
  STAMP(18);
  bar();
  STAMP(9);
#pragma unroll
  for (int r = 0; r < 12; ++r) rd(xa, xb, 0, 0, r);
#pragma unroll 1
  for (int t = 0; t < nt2; t += 2) {
    unsigned x0, x1;
    SCHED_FENCE(); wait_lgkm0(); SCHED_FENCE();
    phase<16>(acc, xa, xb, [&](int i) { rd(ya, yb, 0, 1, i); });
    wait_lgkm0(); bar(); SCHED_FENCE();
    phase<16>(acc, ya, yb, [&](int i) {
      rd(xa, xb, P2_STAGE, 0, i);
      if (i == 13) { const bool sw = (t + 2 == nC); x0 = sw ? sw0 : 65536u; x1 = sw ? sw1 : 65536u; asm volatile("" : "+v"(x0), "+v"(x1)); }
      if (i == 14) { pa0 ^= x0; asm volatile("" : "+v"(pa0)); }
    });
    wait_lgkm0(); SCHED_FENCE();
    phase<16>(acc, xa, xb, [&](int i) {
      rd(ya, yb, P2_STAGE, 1, i);
      if (i == 12) { pb[0] ^= 65536u; pb[1] ^= 65536u; asm volatile("" : "+v"(pb[0]), "+v"(pb[1])); }
      if (i == 13) { pb[2] ^= 65536u; pb[3] ^= 65536u; asm volatile("" : "+v"(pb[2]), "+v"(pb[3])); }
      if (i == 14) { pa1 ^= x1; asm volatile("" : "+v"(pa1)); }
    });
    wait_lgkm0(); bar(); SCHED_FENCE();
    phase<16>(acc, ya, yb, [&](int i) { rd(xa, xb, 0, 0, i); });
  }
  STAMP(10);
  const rsrc_t rxb = mk_rsrc(a.ws + WS_XB, 256 * 32768);
  const unsigned xo = (unsigned)(wid * 8192 + ln * 16);
#pragma unroll
  for (int n = 0; n < 4; ++n)
    st16_wt(rxb, xo + n * 1024, bid * 32768, (u32x4){pk_bf16(acc[2][n][0], acc[2][n][1]), pk_bf16(acc[2][n][2], acc[2][n][3]), pk_bf16(acc[3][n][0], acc[3][n][1]), pk_bf16(acc[3][n][2], acc[3][n][3])});
  wait_vm<0>();
  bar();
  STAMP(11);
  if (wid == 0 && ln == 0) __hip_atomic_store(sy + SY_X + bid, tag, RLX_AGENT);
  if (wid == 0) {
#pragma unroll 1
    for (unsigned spins = 0; spins < SPIN_CAP; ++spins) {
      if (__all(__hip_atomic_load(sy + SY_X + pa, RLX_AGENT) == tag)) break;
      __builtin_amdgcn_s_sleep(1);
    }
  }
  STAMP(12);
  bar();
#pragma unroll
  for (int n = 0; n < 4; ++n) {
    const u32x4 r = __builtin_amdgcn_raw_buffer_load_b128(rxb, xo + n * 1024, pa * 32768, 16);
    acc[0][n] += (f32x4){bflo(r[0]), bfhi(r[0]), bflo(r[1]), bfhi(r[1])};
    acc[1][n] += (f32x4){bflo(r[2]), bfhi(r[2]), bflo(r[3]), bfhi(r[3])};
  }
#pragma unroll
  for (int n = 0; n < 4; ++n)
#pragma unroll
    for (int mi = 0; mi < 2; ++mi) {
      const int d = d0 + 64 * kh + 32 * wm + 16 * mi + 4 * g, tok = t0 + 64 * wn + 16 * n + cl;
      __builtin_amdgcn_raw_buffer_store_b128(__builtin_bit_cast(u32x4, acc[mi][n]), mk_rsrc(a.out, S * D * 4), (unsigned)(tok * D + d) * 4u, 0, 16);
    }
  STAMP(13);
}

extern "C" void kernel_launch(void* const* d_in, const int* in_sizes, int n_in, void* d_out, int out_size, void* d_ws, size_t ws_size,
                              hipStream_t stream) {
  (void)hipFuncSetAttribute((const void*)fused, hipFuncAttributeMaxDynamicSharedMemorySize, LDS_BYTES);
  Args a;
  a.x = (const u16*)d_in[0]; a.Lre = (const u16*)d_in[1]; a.Lim = (const u16*)d_in[2]; a.Bre = (const u16*)d_in[3]; a.Bim = (const u16*)d_in[4];
  a.Cre = (const u16*)d_in[5]; a.Cim = (const u16*)d_in[6]; a.logdt = (const u16*)d_in[7]; a.Dsk = (const u16*)d_in[8];
  a.w1 = (const u16*)d_in[10]; a.b1 = (const u16*)d_in[11]; a.w2 = (const u16*)d_in[12]; a.b2 = (const u16*)d_in[13];
  a.out = (float*)d_out; a.ws = (char*)d_ws;
  fused<<<256, 512, LDS_BYTES, stream>>>(a);
}
```

```cpp
#include <hip/hip_runtime.h>
#include <stdint.h>

typedef unsigned short u16;
typedef unsigned long long u64;
typedef __bf16 bf16x8 __attribute__((ext_vector_type(8)));
typedef short s16x4 __attribute__((ext_vector_type(4)));
typedef short s16x8 __attribute__((ext_vector_type(8)));
typedef float f32x2 __attribute__((ext_vector_type(2)));
typedef float f32x4 __attribute__((ext_vector_type(4)));
typedef unsigned u32x4 __attribute__((ext_vector_type(4)));
typedef unsigned u32x2 __attribute__((ext_vector_type(2)));
#define LAS __attribute__((address_space(3)))
#define GAS __attribute__((address_space(1)))
typedef GAS unsigned long long gu64;
typedef __amdgpu_buffer_rsrc_t rsrc_t;
#define RLX_AGENT __ATOMIC_RELAXED, __HIP_MEMORY_SCOPE_AGENT

constexpr int S = 2048, D = 1024, P = 1024, F = 3584;
constexpr size_t MB = 1024 * 1024;
constexpr size_t WS_HL = 4 * MB, WS_HT = 8 * MB, WS_U = 16 * MB, WS_XB = 24 * MB, WS_SYNC = 32 * MB;
constexpr int SY_LN = 64, SY_H = 320, SY_U = 448, SY_ST = 1024, SY_SB = 3072, SY_GR = 5120;
constexpr unsigned SYNC_BYTES = (SY_GR + 8 * P * 2) * 8;
constexpr unsigned SPIN_CAP = 1u << 17;
#define NBA 24
#define NAB 6
constexpr unsigned CODE_PF_BYTES = 36 * 1024;
constexpr unsigned long long CB_ORDER = 0x80F7E6D5C491B3A2ull;
#define XB_SLEEP 2
#define PF_NST 11
#define PF_ST0 1
#define PF_N1 13
#define PF_NST2 20
#define PF_WA 0
#define PF_BT 5
#define PF_WB 4
#define A1_RS 2
#define PSTAMP(w, i)
#define STAMP_INIT()
#define STAMP_DUMP()
#define TRACE(e)
#define TRACE_DUMP()
#define STAMP(i) PSTAMP(0, i)

__device__ __forceinline__ float bf2f(u16 v) { return __uint_as_float(((unsigned)v) << 16); }
__device__ __forceinline__ float bflo(unsigned v) { return __uint_as_float(v << 16); }
__device__ __forceinline__ float bfhi(unsigned v) { return __uint_as_float(v & 0xffff0000u); }
__device__ __forceinline__ unsigned pk_bf16(float lo, float hi) {
  typedef __bf16 bf16x2 __attribute__((ext_vector_type(2)));
  f32x2 v = {lo, hi};
  bf16x2 r = __builtin_convertvector(v, bf16x2);
  return __builtin_bit_cast(unsigned, r);
}
__device__ __forceinline__ float rbf(float f) { return bflo(pk_bf16(f, f)); }
__device__ __forceinline__ float dot2(unsigned a, unsigned b, float c) {
  typedef __bf16 bf16x2 __attribute__((ext_vector_type(2)));
  return __builtin_amdgcn_fdot2_f32_bf16(__builtin_bit_cast(bf16x2, a), __builtin_bit_cast(bf16x2, b), c, false);
}
__device__ __forceinline__ f32x2 fma2(f32x2 a, f32x2 b, f32x2 c) { return __builtin_elementwise_fma(a, b, c); }
__device__ __forceinline__ f32x2 bc2(float v) { return (f32x2){v, v}; }

__device__ __forceinline__ rsrc_t mk_rsrc(const void* p, unsigned bytes) { return __builtin_amdgcn_make_buffer_rsrc((void*)p, 0, bytes, 0x00020000); }
__device__ __forceinline__ void dma16(rsrc_t rs, unsigned voff, unsigned soff, LAS char* l) { __builtin_amdgcn_raw_ptr_buffer_load_lds(rs, (LAS void*)l, 16, voff, soff, 0, 0); }
__device__ __forceinline__ void dma16c(rsrc_t rs, unsigned voff, unsigned soff, LAS char* l) { __builtin_amdgcn_raw_ptr_buffer_load_lds(rs, (LAS void*)l, 16, voff, soff, 0, 16); }
__device__ __forceinline__ void dma4(rsrc_t rs, unsigned voff, unsigned soff, LAS char* l) { __builtin_amdgcn_raw_ptr_buffer_load_lds(rs, (LAS void*)l, 4, voff, soff, 0, 0); }
__device__ __forceinline__ void st16_wt(rsrc_t rs, unsigned voff, unsigned soff, u32x4 v) { __builtin_amdgcn_raw_buffer_store_b128(v, rs, voff + soff, 0, 16); }
__device__ __forceinline__ void st8_wt(rsrc_t rs, unsigned voff, unsigned soff, u32x2 v) { __builtin_amdgcn_raw_buffer_store_b64(v, rs, voff + soff, 0, 16); }
template <int N> __device__ __forceinline__ void wait_vm() { asm volatile("s_waitcnt vmcnt(%0)" ::"n"(N) : "memory"); }
__device__ __forceinline__ void wait_lgkm0() { asm volatile("" ::: "memory"); __builtin_amdgcn_s_waitcnt(0xC07F); asm volatile("" ::: "memory"); }
__device__ __forceinline__ void bar() {
  asm volatile("" ::: "memory");
  __builtin_amdgcn_s_barrier();
  asm volatile("" ::: "memory");
}
#define SCHED_FENCE() __builtin_amdgcn_sched_barrier(0)
__device__ __forceinline__ bf16x8 lds_rd128(unsigned p) { return *(LAS bf16x8*)(size_t)p; }
__device__ __forceinline__ s16x4 lds_rdtr(unsigned p) { return __builtin_amdgcn_ds_read_tr16_b64_v4i16((LAS s16x4*)(size_t)p); }
struct TrFrag { s16x4 h[2]; };
__device__ __forceinline__ bf16x8 frag(const bf16x8& f) { return f; }
__device__ __forceinline__ bf16x8 frag(const TrFrag& f) { s16x8 ab = __builtin_shufflevector(f.h[0], f.h[1], 0, 1, 2, 3, 4, 5, 6, 7); return __builtin_bit_cast(bf16x8, ab); }
#define MFMA(a, b, c) __builtin_amdgcn_mfma_f32_16x16x32_bf16(frag(a), frag(b), (c), 0, 0, 0)
template <int NM, class FA, class FB, class Side>
__device__ __forceinline__ void phase(f32x4 (*acc)[4], const FA* fa, const FB* fb, Side side) {
#pragma unroll
  for (int i = 0; i < NM; ++i) {
    acc[i >> 2][i & 3] = MFMA(fa[i >> 2], fb[i & 3], acc[i >> 2][i & 3]);
    side(i);
    SCHED_FENCE();
  }
}

template <int NL> __device__ __forceinline__ void wait_flags(gu64* p, bool active, u64 tag) {
#pragma unroll 1
  for (unsigned spins = 0; spins < SPIN_CAP; ++spins) {
    bool ok = true;
    if (active) {
#pragma unroll
      for (int k = 0; k < NL; ++k) ok &= __hip_atomic_load(p + 64 * k, RLX_AGENT) == tag;
    }
    if (__all(ok)) break;
    __builtin_amdgcn_s_sleep(2);
  }
  __builtin_amdgcn_fence(__ATOMIC_ACQUIRE, "wavefront");
}

template <int NL> __device__ __forceinline__ int flag_prefix(gu64* p, int n, int need, u64 tag, int lane) {
  int pre = 0;
#pragma unroll 1
  for (unsigned spins = 0; spins < SPIN_CAP; ++spins) {
    const u64 m0 = ~__ballot(lane >= n || __hip_atomic_load(p, RLX_AGENT) == tag);
    const u64 m1 = NL == 2 ? ~__ballot(__hip_atomic_load(p + 64, RLX_AGENT) == tag) : 0ull;
    pre = m0 ? __builtin_ctzll(m0) : m1 ? 64 + __builtin_ctzll(m1) : 128;
    if (pre >= need) break;
    __builtin_amdgcn_s_sleep(1);
  }
  __builtin_amdgcn_fence(__ATOMIC_ACQUIRE, "wavefront");
  return pre < need ? 128 : pre;
}
__device__ __forceinline__ void lds_wait(unsigned addr, unsigned want) {
#pragma unroll 1
  for (unsigned spins = 0; spins < (SPIN_CAP << 4); ++spins) {
    if (__builtin_amdgcn_readfirstlane(__hip_atomic_load((LAS unsigned*)(size_t)addr, __ATOMIC_RELAXED, __HIP_MEMORY_SCOPE_WORKGROUP)) >= want) break;
    __builtin_amdgcn_s_sleep(1);
  }
  asm volatile("" ::: "memory");
}
__device__ __forceinline__ void publish(unsigned cnt, gu64* flag, u64 tag, int lane) {
  wait_vm<0>();
  wait_lgkm0();
  if (lane == 0 && __hip_atomic_fetch_add((LAS unsigned*)(size_t)cnt, 1u, __ATOMIC_RELAXED, __HIP_MEMORY_SCOPE_WORKGROUP) == 3u) __hip_atomic_store(flag, tag, RLX_AGENT);
}

template <class Issue> __device__ __forceinline__ void prod_fill(int n, Issue issue) {
#pragma unroll 1
  for (int t = 0; t < n; ++t) issue();
}
template <int L, int NS, class Issue> __device__ __forceinline__ void prod_run(int n, Issue issue) {
#pragma unroll 1
  for (int t = 0; t < n; ++t) { wait_vm<(NS - 2) * L>(); bar(); issue(); }
}
template <int L, int K, int X = 0> __device__ __forceinline__ void prod_tail() {
  wait_vm<K * L + X>();
  bar();
  if constexpr (K > 0) prod_tail<L, K - 1, X>();
}

__device__ __forceinline__ float wave_sum(float v) {
  v += __builtin_bit_cast(float, __builtin_amdgcn_update_dpp(0, __builtin_bit_cast(int, v), 0xB1, 0xf, 0xf, true));
  v += __builtin_bit_cast(float, __builtin_amdgcn_update_dpp(0, __builtin_bit_cast(int, v), 0x4E, 0xf, 0xf, true));
  v += __builtin_bit_cast(float, __builtin_amdgcn_update_dpp(0, __builtin_bit_cast(int, v), 0x141, 0xf, 0xf, true));
  v += __builtin_bit_cast(float, __builtin_amdgcn_update_dpp(0, __builtin_bit_cast(int, v), 0x140, 0xf, 0xf, true));
  const int iv = __builtin_bit_cast(int, v);
  const float a = __builtin_bit_cast(float, __builtin_amdgcn_readlane(iv, 0)), b = __builtin_bit_cast(float, __builtin_amdgcn_readlane(iv, 16));
  const float c = __builtin_bit_cast(float, __builtin_amdgcn_readlane(iv, 32)), d = __builtin_bit_cast(float, __builtin_amdgcn_readlane(iv, 48));
  return (a + b) + (c + d);
}

struct Args {
  const u16 *x, *Lre, *Lim, *Bre, *Bim, *Cre, *Cim, *logdt, *Dsk, *w1, *b1, *w2, *b2;
  float* out;
  char* ws;
};

constexpr int A1_NS = 3, A1_STAGE = 49152, A1_NT = D / 64;
constexpr int B1_NS = 7, B1_ABYTES = 32 * 448, B1_STAGE = B1_ABYTES + 32 * 256, B1_NT = S / 32;
constexpr int P2_NS = 4, P2_STAGE = 32768;
constexpr int LDS_BYTES = 160 * 1024;
constexpr int LDS_COEF = A1_NS * A1_STAGE;
constexpr int LDS_TOT = LDS_COEF + 14 * 256;
constexpr int LDS_SB = LDS_TOT + 512;
constexpr int LDS_STAT = LDS_SB + 512;
constexpr int LDS_RAW = LDS_STAT + 8 * 272;
constexpr int LDS_RES = P2_NS * P2_STAGE;
constexpr int LDS_RES2 = 158208;
constexpr int LDS_CNT = LDS_BYTES - 256 - 2048 - 64;
constexpr int LDS_FLG = LDS_BYTES - 256 - 2048;
constexpr int LDS_DUMMY = LDS_BYTES - 256;
static_assert(LDS_RAW + 3072 + 768 <= LDS_CNT && B1_NS * B1_STAGE <= LDS_CNT && LDS_RES + 16384 <= LDS_COEF && LDS_RAW + 3840 <= LDS_RES2 && LDS_RES2 + 1536 <= LDS_CNT && P2_NS * P2_STAGE <= LDS_COEF && LDS_RAW % 16 == 0, "lds");

constexpr int UA = NBA * 32, WA = PF_WA * 128 / 2, WB = PF_WB * 128 / 2;
constexpr int RA = (UA - WA - WB + 127) / 128, RB = (1792 - UA - WA - WB + 127) / 128;

__device__ __forceinline__ f32x2 gelu_tanh2(f32x2 x) {
  const f32x2 z = x * fma2(x * x, bc2(-0.10294323967f), bc2(-2.30220819813f));
  const f32x2 d = (f32x2){__builtin_amdgcn_exp2f(z.x), __builtin_amdgcn_exp2f(z.y)} + bc2(1.0f);
  return x * (f32x2){__builtin_amdgcn_rcpf(d.x), __builtin_amdgcn_rcpf(d.y)};
}
#define CMUL(or_, oi_, ar_, ai_, br_, bi_) do { const float t_ = (ar_) * (br_) - (ai_) * (bi_); oi_ = (ar_) * (bi_) + (ai_) * (br_); or_ = t_; } while (0)

__global__ __launch_bounds__(512, 2) void fused(Args a) {
  extern __shared__ __attribute__((aligned(16))) char smem[];
  LAS char* lds = (LAS char*)smem;
  u64 pc0;
  asm volatile("s_getpc_b64 %0" : "=s"(pc0));
  const int wid = __builtin_amdgcn_readfirstlane(threadIdx.x >> 6), lane = threadIdx.x & 63;
  const int bid = blockIdx.x, xc = bid & 7, idx = bid >> 3, xg = xc >> 1;
  const bool roleA = !(xc & 1);
  const int ri = bid >> 1;
  gu64* sy = (gu64*)(a.ws + WS_SYNC);
  u64 tag;
#define LOAD_TAG() do { const __attribute__((address_space(4))) u64* ep_ = (const __attribute__((address_space(4))) u64*)(a.ws + WS_SYNC); \
    asm volatile("" : "+s"(ep_)::"memory"); tag = *ep_ + 1ull; } while (0)
#define HOLD_TAG() asm volatile("" ::"s"(tag))
#define tag32 ((unsigned)tag)
  const int chunk = 4 * (xg & 1) + (idx & 3), cb = 8 * (xg >> 1) + (idx >> 2);
  const int ft = 4 * xg + (idx & 3), dt1 = idx >> 2;
  const int tt = 4 * xg + (idx & 3), dt = idx >> 2;
  const int t0 = tt * 128, d0 = dt * 128;
  u64 cbs = roleA ? CB_ORDER : CB_ORDER >> (2 * (32 - NAB));
  const int nC = roleA ? 32 - NAB : NAB, ku0 = roleA ? 0 : NBA, nt2 = nC + (roleA ? NBA : 56 - NBA);
  const rsrc_t rsy = mk_rsrc(a.ws + WS_SYNC, SYNC_BYTES);
  STAMP_INIT();
  STAMP(0);

  auto touch = [&](const u16* base, unsigned bytes, int u0, int n) {
    const rsrc_t rs = mk_rsrc(base, bytes);
#pragma unroll 1
    for (int k = 0; k < n; ++k) dma4(rs, (unsigned)(lane * 128), (unsigned)(u0 + k) * 8192u, lds + LDS_DUMMY);
  };
  auto w1_voff = [&](int piece) {
    const int ch = piece * 64 + lane, k = ch / 28, pos = ch - k * 28;
    return (unsigned)(k * F + ft * 224 + (pos ^ (3 * ((k >> 3) & 1))) * 8) * 2u;
  };
  if (!roleA) {
    const int row = ri * 16 + wid * 2;
    u32x4 r[2][2], brow[2][2];
    {
      const u16* xp = a.x + (size_t)row * D;
      const u16* bp = (ri < 64 ? a.Bre : a.Bim) + (size_t)(row & 1023) * D;
      const unsigned lo = (unsigned)lane * 16u;
      asm volatile("global_load_dwordx4 %0, %4, %5\n\tglobal_load_dwordx4 %1, %4, %5 offset:1024\n\tglobal_load_dwordx4 %2, %4, %5 offset:2048\n\tglobal_load_dwordx4 %3, %4, %5 offset:3072"
                   : "=&v"(r[0][0]), "=&v"(r[0][1]), "=&v"(r[1][0]), "=&v"(r[1][1]) : "v"(lo), "s"(xp) : "memory");
      asm volatile("global_load_dwordx4 %0, %4, %5\n\tglobal_load_dwordx4 %1, %4, %5 offset:1024\n\tglobal_load_dwordx4 %2, %4, %5 offset:2048\n\tglobal_load_dwordx4 %3, %4, %5 offset:3072"
                   : "=&v"(brow[0][0]), "=&v"(brow[0][1]), "=&v"(brow[1][0]), "=&v"(brow[1][1]) : "v"(lo), "s"(bp) : "memory");
    }
    LOAD_TAG();
    bar();
    {
      const rsrc_t rs = mk_rsrc(a.w1, S * F * 2);
#pragma unroll
      for (int j = 0; j < 2; ++j) {
        const int piece = j == 0 ? wid : (wid < 6 ? wid + 8 : wid);
        const unsigned vo = w1_voff(piece);
#pragma unroll
        for (int t = 0; t < B1_NS - 1; ++t) dma16(rs, vo, t * (32 * F * 2), lds + t * B1_STAGE + piece * 1024);
      }
    }
    wait_vm<16>();
    STAMP(20);
    asm volatile("" : "+v"(r[0][0]), "+v"(r[0][1]), "+v"(r[1][0]), "+v"(r[1][1])::"memory");
    const rsrc_t rx = mk_rsrc(a.ws + WS_HL, S * D * 2);
#pragma unroll
    for (int pass = 0; pass < 2; ++pass) {
      float s1[2], s2[2];
#pragma unroll
      for (int i = 0; i < 2; ++i) {
        float a1 = 0.f, a2 = 0.f;
#pragma unroll
        for (int k = 0; k < 8; ++k) { const unsigned w = r[i][k >> 2][k & 3]; a1 = dot2(w, 0x3f803f80u, a1); a2 = dot2(w, w, a2); }
        s1[i] = a1; s2[i] = a2;
      }
#pragma unroll
      for (int i = 0; i < 2; ++i) { s1[i] = wave_sum(s1[i]); s2[i] = wave_sum(s2[i]); }
#pragma unroll
      for (int i = 0; i < 2; ++i) {
        const float mean = s1[i] * (1.0f / D), var = fmaxf(fmaf(-mean, mean, s2[i] * (1.0f / D)), 0.f);
        const unsigned ms = pk_bf16(mean, __builtin_amdgcn_sqrtf(rbf(rbf(var) + 1e-5f)));
        const float rs_ = __builtin_amdgcn_rcpf(bfhi(ms)), mb = bflo(ms);
#pragma unroll
        for (int k = 0; k < 8; ++k) {
          const unsigned w = r[i][k >> 2][k & 3];
          r[i][k >> 2][k & 3] = pk_bf16((bflo(w) - mb) * rs_, (bfhi(w) - mb) * rs_);
        }
        if (pass == 0) {
          if (lane == 0) __hip_atomic_store(sy + SY_ST + row + i, ((u64)tag32 << 32) | ms, RLX_AGENT);
        } else {
          st16_wt(rx, (unsigned)lane * 16u, (unsigned)((row + i) * D) * 2u, r[i][0]);
          st16_wt(rx, (unsigned)lane * 16u + 1024u, (unsigned)((row + i) * D) * 2u, r[i][1]);
        }
      }
    }
    STAMP(1);
    asm volatile("s_waitcnt vmcnt(0)" : "+v"(brow[0][0]), "+v"(brow[0][1]), "+v"(brow[1][0]), "+v"(brow[1][1])::"memory");
    bar();
    if (wid == 0 && lane == 0) __hip_atomic_store(sy + SY_LN + ri, tag, RLX_AGENT);
    STAMP(2);
    {
      float sums[2];
#pragma unroll
      for (int i = 0; i < 2; ++i) {
        float s = 0.f;
#pragma unroll
        for (int k = 0; k < 8; ++k) { const unsigned w = brow[i][k >> 2][k & 3]; s = dot2(w, 0x3f803f80u, s); }
        sums[i] = wave_sum(s);
      }
      if (lane < 2) __hip_atomic_store(sy + SY_SB + row + lane, ((u64)tag32 << 32) | __float_as_uint(lane ? sums[1] : sums[0]), RLX_AGENT);
    }
  }

  if (wid >= 4) {
    const int pw = wid - 4;
    if (pw == 3) {
      auto flags_fetch = [&]() {
        dma16c(rsy, (unsigned)lane * 16u, SY_LN * 8, lds + LDS_FLG);
        dma16c(rsy, (unsigned)lane * 16u, SY_LN * 8 + 1024, lds + LDS_FLG + 1024);
      };
      auto flags_check = [&]() {
        bool ok = true;
#pragma unroll
        for (int k = 0; k < 4; ++k) ok &= *(LAS u64*)(lds + LDS_FLG + k * 512 + lane * 8) == tag;
        if (!__all(ok)) wait_flags<4>(sy + SY_LN + lane, true, tag);
      };
      if (lane < 4) *(LAS unsigned*)(size_t)(LDS_CNT + lane * 4) = 0u;
      dma4(mk_rsrc((const void*)(pc0 & ~255ull), CODE_PF_BYTES), (unsigned)(lane * 128), (unsigned)((idx % 5) * 8192), lds + LDS_DUMMY);
      if (roleA) {
        st16_wt(rsy, (unsigned)lane * 16u, (unsigned)(SY_GR * 8 + (chunk * P + cb * 64) * 16), (u32x4){0u, 0u, 0u, 0u});
        dma4(mk_rsrc(a.Lre, P * 2), (unsigned)(lane & 31) * 4u, cb * 128, lds + LDS_RAW + 3072);
        dma4(mk_rsrc(a.Lim, P * 2), (unsigned)(lane & 31) * 4u, cb * 128, lds + LDS_RAW + 3072 + 256);
        dma4(mk_rsrc(a.logdt, P * 2), (unsigned)(lane & 31) * 4u, cb * 128, lds + LDS_RAW + 3072 + 512);
        LOAD_TAG(); HOLD_TAG();
        const int bst = PF_ST0 + ((ri * PF_NST) >> 7);
        auto step = [&](int t) {
          bar();
          if (t == bst) touch(a.w1, S * F * 2, 128 + PF_N1 * ri, PF_N1);
          if (t == bst + PF_NST) touch(a.w1, S * F * 2, 128 + 128 * PF_N1 + (13 - PF_N1) * ri, 13 - PF_N1);
        };
        PSTAMP(7, 24);
        step(0); step(1); step(2);
        if (bst <= 2) wait_vm<PF_N1>(); else wait_vm<0>();
        if (lane == 0) __hip_atomic_store(sy + SY_LN + 128 + ri, tag, RLX_AGENT);
        LAS float* cf = (LAS float*)(lds + LDS_COEF) + lane;
        const float lr = bf2f(*(LAS u16*)(lds + LDS_RAW + 3072 + lane * 2)), li = bf2f(*(LAS u16*)(lds + LDS_RAW + 3072 + 256 + lane * 2));
        const float dtt = rbf(expf(bf2f(*(LAS u16*)(lds + LDS_RAW + 3072 + 512 + lane * 2)))), er = expf(dtt * lr);
        float arg = dtt * li;
        asm volatile("" : "+v"(arg));
        step(3);
        float sn, cs;
        sincosf(arg, &sn, &cs);
        float pr = er * cs, pi = er * sn;
        asm volatile("" : "+v"(pr), "+v"(pi));
        step(4);
        const float nr = pr - 1.0f, den = lr * lr + li * li;
        cf[0] = pr; cf[64] = pi;
        cf[128] = 2.0f * (nr * lr + pi * li) / den; cf[192] = 2.0f * (pi * lr - nr * li) / den;
        step(5);
#pragma unroll
        for (int k = 0; k < 8; ++k) {
          const float t = pr * pr - pi * pi; pi = 2.0f * pr * pi; pr = t;
          if (k >= 3) { cf[(4 + 2 * (k - 3)) * 64] = pr; cf[(5 + 2 * (k - 3)) * 64] = pi; }
        }
#pragma unroll 1
        for (int t = 6; t < 12; ++t) step(t);
        const unsigned sbo = (unsigned)(SY_SB * 8 + cb * 512 + (lane >> 5) * (P * 8) + (lane & 31) * 16);
#pragma unroll 1
        for (unsigned spins = 0;; ++spins) {
          dma16c(rsy, (unsigned)lane * 16u, (unsigned)(SY_ST * 8 + chunk * 2048), lds + LDS_RAW);
          dma16c(rsy, (unsigned)lane * 16u, (unsigned)(SY_ST * 8 + chunk * 2048 + 1024), lds + LDS_RAW + 1024);
          dma16c(rsy, sbo, 0, lds + LDS_RAW + 2048);
          if (spins == 0) { flags_fetch(); step(12); step(13); }
          wait_vm<0>();
          u32x2 gs[4], gb[2];
          bool ok = true;
#pragma unroll
          for (int k = 0; k < 4; ++k) { gs[k] = *(LAS u32x2*)(lds + LDS_RAW + k * 512 + lane * 8); ok &= gs[k][1] == tag32; }
#pragma unroll
          for (int k = 0; k < 2; ++k) { gb[k] = *(LAS u32x2*)(lds + LDS_RAW + 2048 + k * 512 + lane * 8); ok &= gb[k][1] == tag32; }
          if (__all(ok) || spins > SPIN_CAP) {
#pragma unroll
            for (int k = 0; k < 4; ++k) {
              LAS float* e = (LAS float*)(lds + LDS_STAT + (2 * k + (lane >> 5)) * 272 + (lane & 15) * 16 + ((lane >> 4) & 1) * 4);
              e[0] = -bflo(gs[k][0]); e[2] = __builtin_amdgcn_rcpf(bfhi(gs[k][0]));
            }
#pragma unroll
            for (int k = 0; k < 2; ++k) *(LAS unsigned*)(lds + LDS_SB + k * 256 + lane * 4) = gb[k][0];
            break;
          }
          __builtin_amdgcn_s_sleep(2);
        }
        touch(a.Cre, D * P * 2, 2 * ri, 2); touch(a.Cim, D * P * 2, 2 * ri, 2);
        touch(a.w2, F * S * 2, UA + (PF_WA / 2) * ri, PF_WA / 2); touch(a.w2, F * S * 2, (PF_WA / 2) * ri, PF_WA / 2);
        bar();
        flags_check();
        wait_lgkm0();
        step(15);
      } else {
#pragma unroll 1
        for (int t = 0; t < 13; ++t) bar();
        flags_fetch();
#pragma unroll 1
        for (int t = 13; t < 20; ++t) bar();
        wait_vm<0>();
        flags_check();
        if (bid == 255 && lane == 0) __hip_atomic_store(sy, tag, RLX_AGENT);
#pragma unroll 1
        for (int t = 20; t < B1_NT - PF_BT; ++t) bar();
        touch(a.w2, F * S * 2, UA + WA + (PF_WB / 2) * ri, PF_WB / 2); touch(a.w2, F * S * 2, WA + (PF_WB / 2) * ri, PF_WB / 2);
#pragma unroll 1
        for (int t = 0; t < PF_BT; ++t) bar();
      }
    } else if (roleA) {
      const int T0 = chunk * 256, p0 = cb * 64;
      if (pw < 2) {
        const int i3 = lane >> 3;
        const unsigned ve = (unsigned)((T0 + 128 * pw + 32 * (i3 >> 2) + 16 * (i3 & 1) + ((i3 >> 1) & 1)) * D * 2 + ((lane & 7) ^ (lane >> 4)) * 16);
        const unsigned vo = (ve ^ 64u) + 64 * D * 2;
        const rsrc_t rs = mk_rsrc(a.x, S * D * 2);
        unsigned so = 0, lo = pw * 16384;
        auto issue = [&]() {
#pragma unroll
          for (int j = 0; j < 16; ++j) dma16(rs, (j & 1) ? vo : ve, so + (j >> 1) * (2 * D * 2), lds + lo + j * 1024);
          so += 128; lo = lo >= (A1_NS - 1) * A1_STAGE ? lo - (A1_NS - 1) * A1_STAGE : lo + A1_STAGE;
        };
        PSTAMP(4, 21);
        prod_fill(A1_NS - 1, issue);
        PSTAMP(4, 22);
        LOAD_TAG(); HOLD_TAG();
        prod_run<16, A1_NS>(A1_NT - (A1_NS - 1), issue);
        prod_tail<16, A1_NS - 2>();
      } else {
        const unsigned ve = (unsigned)((p0 + (lane >> 3)) * D * 2 + ((lane & 7) ^ (lane >> 4)) * 16), vo = ve ^ 64u;
        const rsrc_t rre = mk_rsrc(a.Bre, P * D * 2), rim = mk_rsrc(a.Bim, P * D * 2);
        unsigned so = 0, lo = 32768;
        auto issue = [&]() {
#pragma unroll
          for (int j = 0; j < 8; ++j) {
            dma16(rre, (j & 1) ? vo : ve, so + j * (8 * D * 2), lds + lo + (j >> 1) * 4096 + (j & 1) * 1024);
            dma16(rim, (j & 1) ? vo : ve, so + j * (8 * D * 2), lds + lo + (j >> 1) * 4096 + (j & 1) * 1024 + 2048);
          }
          so += 128; lo = lo >= (A1_NS - 1) * A1_STAGE ? lo - (A1_NS - 1) * A1_STAGE : lo + A1_STAGE;
        };
        prod_fill(A1_NS - 1, issue);
        LOAD_TAG(); HOLD_TAG();
        prod_run<16, A1_NS>(A1_NT - (A1_NS - 1), issue);
        prod_tail<16, A1_NS - 2>();
      }
    } else {
      const int dd0 = dt1 * 128;
      if (pw < 2) {
        unsigned voff[7];
#pragma unroll
        for (int j = 0; j < 7; ++j) voff[j] = w1_voff(pw * 7 + j);
        const rsrc_t rs = mk_rsrc(a.w1, S * F * 2);
        unsigned so = (B1_NS - 1) * (32 * F * 2), lo = (B1_NS - 1) * B1_STAGE + pw * 7168;
        auto issue = [&]() {
#pragma unroll
          for (int j = 0; j < 7; ++j) dma16(rs, voff[j], so, lds + lo + j * 1024);
          so += 32 * F * 2; lo = lo >= (B1_NS - 1) * B1_STAGE ? lo - (B1_NS - 1) * B1_STAGE : lo + B1_STAGE;
        };
        prod_run<7, B1_NS>(B1_NT - (B1_NS - 1), issue);
        prod_tail<7, B1_NS - 2>();
      } else {
        unsigned voff[2];
#pragma unroll
        for (int j = 0; j < 2; ++j) {
          const int k = j * 8 + (lane >> 4), pos = lane & 15;
          const int c = pos ^ (((k & 3) << 1) | (((k >> 3) & 1) << 3));
          voff[j] = (unsigned)((lane >> 4) * D + dd0 + c * 8) * 2u;
        }
        const rsrc_t rs = mk_rsrc(a.ws + WS_HL, S * D * 2);
        unsigned so = 0, lo = B1_ABYTES;
        auto issue = [&]() {
#pragma unroll
          for (int j = 0; j < 8; ++j) dma16c(rs, voff[(j >> 1) & 1], so + j * 4 * (D * 2), lds + lo + j * 1024);
          so += 32 * D * 2; lo = lo >= (B1_NS - 1) * B1_STAGE ? lo - (B1_NS - 1) * B1_STAGE : lo + B1_STAGE;
        };
        int done = B1_NS - 1;
        wait_flags<1>(sy + SY_LN + lane, lane < 2 * done, tag);
        PSTAMP(6, 16);
        prod_fill(B1_NS - 1, issue);
        wait_vm<(B1_NS - 2) * 8>();
        bar();
        int pre = flag_prefix<2>(sy + SY_LN + lane, 64, 2 * done + 2, tag, lane);
        issue();
        ++done;
#pragma unroll 1
        while (pre < 128 && done < B1_NT) {
          const int n = pre / 2 - done;
          prod_run<8, B1_NS>(n, issue);
          done += n;
          wait_vm<(B1_NS - 2) * 8>();
          bar();
          pre = flag_prefix<2>(sy + SY_LN + lane, 64, 2 * done + 2, tag, lane);
          issue();
          ++done;
        }
        prod_run<8, B1_NS>(B1_NT - done, issue);
        prod_tail<8, B1_NS - 2>();
      }
    }
    bar();
    const int nU = nt2 - nC;
    if (pw == 1 || pw == 2) {
      const rsrc_t rxx = mk_rsrc(a.x, S * D * 2);
      const int dk = d0 + (roleA ? 0 : 64);
#pragma unroll
      for (int k = 0; k < 8; ++k) dma16(rxx, (unsigned)((lane >> 3) * (D * 2) + ((lane & 7) ^ (lane >> 3)) * 16), (unsigned)((t0 + 64 * (pw - 1) + 8 * k) * D + dk) * 2u, lds + LDS_RES + (8 * (pw - 1) + k) * 1024);
      if (pw == 1) dma16c(rsy, (unsigned)lane * 16u, (unsigned)(SY_ST + t0) * 8u, lds + LDS_RES2);
      else { dma4(mk_rsrc(a.b2, S * 2), (unsigned)lane * 4u, (unsigned)t0 * 2u, lds + LDS_RES2 + 1024); dma4(mk_rsrc(a.Dsk, D * 2), (unsigned)lane * 4u, (unsigned)dk * 2u, lds + LDS_RES2 + 1280); }
      wait_vm<0>();
      if (lane == 0) __hip_atomic_fetch_add((LAS unsigned*)(size_t)(LDS_CNT + 12), 1u, __ATOMIC_RELAXED, __HIP_MEMORY_SCOPE_WORKGROUP);
    }
    if (pw == 0 || pw == 3) {
      const int dh = d0 + (((pw ? 1 : 0) ^ (roleA ? 0 : 1)) << 6), r4 = lane >> 4;
      const unsigned vce = (unsigned)(dh + (lane >> 3)) * (P * 2) + (unsigned)(((lane & 7) ^ (lane >> 4)) * 16), vco = vce ^ 64u;
      const unsigned vu = (unsigned)((dh + (lane >> 2)) * 64 + (((lane & 3) ^ (((r4 & 1) << 1) ^ ((r4 >> 1) * 3))) * 16));
      const rsrc_t ru = mk_rsrc(a.ws + WS_U, D * F * 2);
      rsrc_t rc = mk_rsrc(a.Cre, D * P * 2), rc2 = mk_rsrc(a.Cim, D * P * 2);
      unsigned so = 0, lo = pw ? 8192 : 0, sh = 0;
      auto issueC = [&]() {
        so = ((unsigned)cbs & 15u) * 128;
#pragma unroll
        for (int j = 0; j < 8; ++j) dma16(rc, (j & 1) ? vco : vce, so + j * 8 * (P * 2), lds + lo + j * 1024);
        const rsrc_t t_ = rc; rc = rc2; rc2 = t_;
        cbs >>= sh; sh ^= 4; lo = (lo + P2_STAGE) & (P2_NS * P2_STAGE - 1);
      };
      auto issueU = [&]() {
#pragma unroll
        for (int j = 0; j < 8; ++j) dma16c(ru, vu, so + (j & 1) * (D * 64) + (j >> 1) * 1024, lds + lo + j * 1024);
        so += 2 * D * 64; lo = (lo + P2_STAGE) & (P2_NS * P2_STAGE - 1);
      };
      prod_fill(P2_NS - 1, issueC);
      if (roleA) {
        const int bst = 1 + ((ri * PF_NST2) >> 7);
        prod_run<8, P2_NS>(bst, issueC);
        if (pw == 3) { touch(a.w2, F * S * 2, UA + WA + WB + RB * ri, RB); touch(a.w2, F * S * 2, WA + WB + RA * ri, RA); }
        prod_run<8, P2_NS>(nC - 3 - bst, issueC);
      } else {
        prod_run<8, P2_NS>(nC - 3, issueC);
      }
      so = ku0 * (2 * D * 64);
      wait_vm<16>();
      bar();
      wait_flags<1>(sy + SY_U + dt * 16 + (lane & 15), true, tag);
      PSTAMP(4, 19);
      issueU();
      prod_run<8, P2_NS>(nU - 1, issueU);
    } else {
      unsigned voff[2];
#pragma unroll
      for (int j = 0; j < 2; ++j) {
        const int k = j * 8 + (lane >> 4), pos = lane & 15;
        const int c = pos ^ (((k & 3) << 1) | (((k >> 3) & 1) << 3));
        voff[j] = (unsigned)(((pw - 1) * 32 + (lane >> 4)) * S + t0 + c * 8) * 2u;
      }
      const unsigned vh = (unsigned)(lane * 16 + (pw - 1) * 8192);
      const rsrc_t rh = mk_rsrc(a.ws + WS_HT, 2 * P * S * 2), rw = mk_rsrc(a.w2, F * S * 2);
      unsigned so = 0, lo = 16384 + (pw - 1) * 8192, sh = 0;
      gu64* hf = sy + SY_H + (tt >> 1) * 16 + ((unsigned)(cbs >> (4 * (lane & 15))) & 15u);
      auto issueH = [&]() {
        so = (tt * 32 + sh * 4 + ((unsigned)cbs & 15u)) * 16384;
#pragma unroll
        for (int j = 0; j < 8; ++j) dma16c(rh, vh, so + j * 1024, lds + lo + j * 1024);
        cbs >>= sh; sh ^= 4; lo = (lo + P2_STAGE) & (P2_NS * P2_STAGE - 1);
      };
      auto issueW = [&]() {
#pragma unroll
        for (int j = 0; j < 8; ++j) dma16(rw, voff[(j >> 1) & 1], so + j * 4 * (S * 2), lds + lo + j * 1024);
        so += 64 * S * 2; lo = (lo + P2_STAGE) & (P2_NS * P2_STAGE - 1);
      };
      if (roleA) lds_wait(LDS_CNT, 4u);
      int done = P2_NS - 1, pre = flag_prefix<1>(hf, nC / 2, 2, tag, lane);
      PSTAMP(6, 17);
      prod_fill(P2_NS - 1, issueH);
#pragma unroll 1
      while (pre < 128 && done < nC) {
        const int n = (2 * pre < nC ? 2 * pre : nC) - done;
        prod_run<8, P2_NS>(n, issueH);
        done += n;
        if (done < nC) {
          wait_vm<16>();
          bar();
          pre = flag_prefix<1>(hf, nC / 2, done / 2 + 1, tag, lane);
          issueH();
          ++done;
        }
      }
      prod_run<8, P2_NS>(nC - done, issueH);
      so = ku0 * (64 * S * 2);
      if (!roleA && pw == 1) wait_flags<1>(sy + SY_H + chunk * 16 + cb, true, tag);
      prod_run<8, P2_NS>(nU, issueW);
    }
    prod_tail<8, P2_NS - 2>();
    return;
  }

  const int wm = wid >> 1, wn = wid & 1;
  int cl = lane & 15, g = lane >> 4, q4 = cl >> 2, p4 = lane & 3, ln = lane;
#define RELANE() do { ln = __builtin_amdgcn_mbcnt_hi(~0u, __builtin_amdgcn_mbcnt_lo(~0u, 0u)); asm volatile("" : "+v"(ln)); cl = ln & 15; g = ln >> 4; q4 = cl >> 2; p4 = ln & 3; } while (0)
  const int kh = roleA ? 0 : 1, pa = bid ^ 1;
  const rsrc_t rxb = mk_rsrc(a.ws + WS_XB, 256 * 16384);
  auto xb_clear = [&]() {
#pragma unroll
    for (int n = 0; n < 4; ++n) st16_wt(rxb, (unsigned)(wid * 4096 + ln * 16 + n * 1024), bid * 16384, (u32x4){0u, 0u, 0u, 0u});
  };
  f32x4 res[2][4];
  auto residual = [&]() {
    lds_wait(LDS_CNT + 12, 2u);
#pragma unroll
    for (int n = 0; n < 4; ++n) {
      const int tl = 64 * wn + 16 * n + cl;
      const unsigned ms = *(LAS unsigned*)(lds + LDS_RES2 + tl * 8);
      const float rs_ = __builtin_amdgcn_rcpf(bfhi(ms)), mb = bflo(ms), b2 = bf2f(*(LAS u16*)(lds + LDS_RES2 + 1024 + tl * 2));
#pragma unroll
      for (int mi = 0; mi < 2; ++mi) {
        const int c8 = 4 * wm + 2 * mi + (g >> 1);
        const u32x2 xv = *(LAS u32x2*)(lds + LDS_RES + tl * 128 + ((c8 ^ (tl & 7)) * 16) + (g & 1) * 8);
        const u32x2 dk = *(LAS u32x2*)(lds + LDS_RES2 + 1280 + c8 * 16 + (g & 1) * 8);
#pragma unroll
        for (int h = 0; h < 2; ++h) {
          const float x0 = bflo(xv[h]), x1 = bfhi(xv[h]);
          const unsigned xn = pk_bf16((x0 - mb) * rs_, (x1 - mb) * rs_);
          res[mi][n][2 * h] = fmaf(bflo(xn), bflo(dk[h]), x0 + b2); res[mi][n][2 * h + 1] = fmaf(bfhi(xn), bfhi(dk[h]), x1 + b2);
        }
        asm volatile("" : "+v"(res[mi][n]));
      }
    }
  };

  if (roleA) {
    LOAD_TAG(); HOLD_TAG();
    RELANE();
    const int p0 = cb * 64;
    const unsigned rb = (unsigned)(cl * 128 + ((g ^ (cl >> 1)) * 16));
    unsigned pa0 = rb + wm * 16384, pa1 = pa0 ^ 64u, pb0 = rb + 32768 + wn * 8192, pb1 = pb0 ^ 64u;
    f32x4 acc[8][4];
#pragma unroll
    for (int m = 0; m < 8; ++m)
#pragma unroll
      for (int n = 0; n < 4; ++n) acc[m][n] = (f32x4){0.f, 0.f, 0.f, 0.f};
    bf16x8 xa[8], xb[4], ya[8], yb[4];
    auto rd = [&](bf16x8* fa, bf16x8* fb, unsigned pa_, unsigned pb_, int r) {
      if (r == 0) fa[0] = lds_rd128(pa_); else if (r < 5) fb[r - 1] = lds_rd128(pb_ + (r - 1) * 2048); else fa[r - 4] = lds_rd128(pa_ + (r - 4) * 2048);
    };
    int slot = 0, dl = 0;
    STAMP(23);
    bar();
    STAMP(3);
#pragma unroll
    for (int r = 0; r < 12; ++r) rd(xa, xb, pa0, pb0, r);
#pragma unroll 1
    for (int t = 0; t < A1_NT; ++t) {
      SCHED_FENCE(); wait_lgkm0(); SCHED_FENCE();
      phase<32>(acc, xa, xb, [&](int i) {
        if (i % A1_RS == 0 && i / A1_RS < 12) rd(ya, yb, pa1, pb1, i / A1_RS);
        if (i == 25) { dl = (slot == A1_NS - 1) ? -(A1_NS - 1) * A1_STAGE : A1_STAGE; slot = (slot == A1_NS - 1) ? 0 : slot + 1; }
        if (i == 27) { pa0 += dl; pb0 += dl; asm volatile("" : "+v"(pa0), "+v"(pb0)); }
        if (i == 29) { pa1 += dl; pb1 += dl; asm volatile("" : "+v"(pa1), "+v"(pb1)); }
      });
      wait_lgkm0();
      bar();
      SCHED_FENCE();
      phase<32>(acc, ya, yb, [&](int i) { if (i % A1_RS == 0 && i / A1_RS < 12) rd(xa, xb, pa0, pb0, i / A1_RS); });
    }
    RELANE();
    STAMP(4);
    xb_clear();
    float cf[2][14], sb[4];
#pragma unroll
    for (int q = 0; q < 2; ++q) {
#pragma unroll
      for (int k = 0; k < 14; ++k) cf[q][k] = ((LAS float*)(lds + LDS_COEF))[k * 64 + 32 * wn + 16 * q + cl];
      sb[2 * q] = ((LAS float*)(lds + LDS_SB))[32 * wn + 16 * q + cl]; sb[2 * q + 1] = ((LAS float*)(lds + LDS_SB))[64 + 32 * wn + 16 * q + cl];
    }
    f32x2 er[2], ei[2];
#pragma unroll
    for (int q = 0; q < 2; ++q) { er[q] = bc2(0.f); ei[q] = bc2(0.f); }
#pragma unroll
    for (int m = 0; m < 8; ++m)
#pragma unroll
      for (int jj = 0; jj < 2; ++jj) {
        const f32x4 st = *(LAS f32x4*)(lds + LDS_STAT + (4 * wm + g) * 272 + (2 * m + jj) * 16);
        const f32x2 c2 = {st[0], st[1]}, r2 = {st[2], st[3]};
#pragma unroll
        for (int q = 0; q < 2; ++q) {
          const f32x2 ur = fma2(c2, bc2(sb[2 * q]), (f32x2){acc[m][2 * q][2 * jj], acc[m][2 * q][2 * jj + 1]}) * r2;
          const f32x2 ui = fma2(c2, bc2(sb[2 * q + 1]), (f32x2){acc[m][2 * q + 1][2 * jj], acc[m][2 * q + 1][2 * jj + 1]}) * r2;
          acc[m][2 * q][2 * jj] = ur.x; acc[m][2 * q][2 * jj + 1] = ur.y; acc[m][2 * q + 1][2 * jj] = ui.x; acc[m][2 * q + 1][2 * jj + 1] = ui.y;
          const f32x2 nr = fma2(bc2(cf[q][0]), er[q], fma2(bc2(-cf[q][1]), ei[q], ur));
          const f32x2 ni = fma2(bc2(cf[q][0]), ei[q], fma2(bc2(cf[q][1]), er[q], ui));
          er[q] = nr; ei[q] = ni;
        }
        if (jj == 1 && (m & 1)) asm volatile("" : "+v"(er[0]), "+v"(ei[0]), "+v"(er[1]), "+v"(ei[1]));
      }
    float sr[2], si[2], pr_[2], pi_[2];
    LAS float* tot = (LAS float*)(lds + LDS_TOT);
#pragma unroll
    for (int q = 0; q < 2; ++q) {
      float tr, ti;
      CMUL(tr, ti, cf[q][4], cf[q][5], er[q].x, ei[q].x);
      sr[q] = tr + er[q].y; si[q] = ti + ei[q].y;
      float vr = __shfl_up(sr[q], 16), vi = __shfl_up(si[q], 16);
      CMUL(tr, ti, cf[q][6], cf[q][7], vr, vi);
      if (g >= 1) { sr[q] += tr; si[q] += ti; }
      vr = __shfl_up(sr[q], 32); vi = __shfl_up(si[q], 32);
      CMUL(tr, ti, cf[q][8], cf[q][9], vr, vi);
      if (g >= 2) { sr[q] += tr; si[q] += ti; }
      vr = __shfl_up(sr[q], 16); vi = __shfl_up(si[q], 16);
      pr_[q] = g >= 1 ? vr : 0.f; pi_[q] = g >= 1 ? vi : 0.f;
      if (wm == 0 && g == 3) *(LAS f32x2*)(tot + (32 * wn + 16 * q + cl) * 2) = (f32x2){sr[q], si[q]};
    }
    if (wm == 0) {
      wait_lgkm0();
      if (ln == 0) __hip_atomic_store((LAS unsigned*)(size_t)(LDS_CNT + 4 + 4 * wn), 1u, __ATOMIC_RELAXED, __HIP_MEMORY_SCOPE_WORKGROUP);
    } else {
      lds_wait(LDS_CNT + 4 + 4 * wn, 1u);
    }
    f32x2 tq[2];
    gu64* gran = sy + SY_GR;
#pragma unroll
    for (int q = 0; q < 2; ++q) {
      tq[q] = *(LAS f32x2*)(tot + (32 * wn + 16 * q + cl) * 2);
      if (wm == 1 && g == 3 && chunk < 7) {
        float tr, ti;
        CMUL(tr, ti, cf[q][10], cf[q][11], tq[q].x, tq[q].y);
        gu64* gp = gran + ((size_t)chunk * P + p0 + 32 * wn + 16 * q + cl) * 2;
        __hip_atomic_store(gp, (1ull << 32) | __float_as_uint(tr + sr[q]), RLX_AGENT);
        __hip_atomic_store(gp + 1, (1ull << 32) | __float_as_uint(ti + si[q]), RLX_AGENT);
      }
    }
    STAMP(5);
#pragma unroll
    for (int m = 0; m < 8; ++m)
#pragma unroll
      for (int q = 0; q < 2; ++q) {
        const f32x4 ur = acc[m][2 * q], ui = acc[m][2 * q + 1];
        acc[m][2 * q] = ur * cf[q][2] - ui * cf[q][3];
        acc[m][2 * q + 1] = ui * (-cf[q][2]) - ur * cf[q][3];
        asm volatile("" : "+v"(acc[m][2 * q]), "+v"(acc[m][2 * q + 1]));
      }
    float hr[2] = {0.f, 0.f}, hi[2] = {0.f, 0.f};
    if (chunk > 0) {
      float gr[2][2], gi[2][2];
#pragma unroll 1
      for (unsigned spins = 0;; ++spins) {
        bool ok = true;
#pragma unroll
        for (int q = 0; q < 2; ++q)
#pragma unroll
          for (int k = 0; k < 2; ++k) {
            const int cc = (g + 4 * k < chunk) ? g + 4 * k : 0;
            gu64* gp = gran + ((size_t)cc * P + p0 + 32 * wn + 16 * q + cl) * 2;
            const u64 vr = __hip_atomic_load(gp, RLX_AGENT), vi = __hip_atomic_load(gp + 1, RLX_AGENT);
            ok &= ((vr >> 32) == 1ull) & ((vi >> 32) == 1ull);
            gr[q][k] = __uint_as_float((unsigned)vr); gi[q][k] = __uint_as_float((unsigned)vi);
          }
        if (__all(ok) || spins > SPIN_CAP) break;
        __builtin_amdgcn_s_sleep(1);
      }
#pragma unroll
      for (int q = 0; q < 2; ++q) {
        float ar_ = 0.f, ai_ = 0.f;
#pragma unroll
        for (int k = 0; k < 2; ++k) {
          float vr = gr[q][k], vi = gi[q][k];
          const int e = chunk - 1 - (g + 4 * k);
#pragma unroll
          for (int i = 0; i < 6 - 4 * k; ++i) {
            float tr, ti;
            CMUL(tr, ti, cf[q][12], cf[q][13], vr, vi);
            vr = i < e ? tr : vr; vi = i < e ? ti : vi;
          }
          ar_ += e >= 0 ? vr : 0.f; ai_ += e >= 0 ? vi : 0.f;
        }
        ar_ += __shfl_xor(ar_, 16); ai_ += __shfl_xor(ai_, 16);
        ar_ += __shfl_xor(ar_, 32); ai_ += __shfl_xor(ai_, 32);
        hr[q] = ar_; hi[q] = ai_;
      }
    }
    STAMP(6);
    f32x2 wr[2], wi[2];
#pragma unroll
    for (int q = 0; q < 2; ++q) {
      float tr, ti;
      if (wm == 1) { CMUL(tr, ti, cf[q][10], cf[q][11], hr[q], hi[q]); hr[q] = tr + tq[q].x; hi[q] = ti + tq[q].y; }
      CMUL(tr, ti, cf[q][6], cf[q][7], hr[q], hi[q]);
      if (g & 1) { hr[q] = tr; hi[q] = ti; }
      CMUL(tr, ti, cf[q][8], cf[q][9], hr[q], hi[q]);
      if (g & 2) { hr[q] = tr; hi[q] = ti; }
      hr[q] += pr_[q]; hi[q] += pi_[q];
      CMUL(tr, ti, cf[q][4], cf[q][5], hr[q], hi[q]);
      const f32x2 h0r = {hr[q], tr + er[q].x}, h0i = {hi[q], ti + ei[q].x};
      wr[q] = h0r * cf[q][2] - h0i * cf[q][3];
      wi[q] = h0i * (-cf[q][2]) - h0r * cf[q][3];
    }
    LAS char* scr = lds + wid * P2_STAGE + 16384;
    const unsigned fsw = (unsigned)(((cl & 3) << 1) | (((cl >> 3) & 1) << 3));
    const rsrc_t rh = mk_rsrc(a.ws + WS_HT, 2 * P * S * 2);
#pragma unroll
    for (int q = 0; q < 2; ++q) {
      const f32x2 ar2 = bc2(cf[q][0]), ai2 = bc2(cf[q][1]);
#pragma unroll
      for (int mh = 0; mh < 2; ++mh) {
        u32x4 o[2][2];
#pragma unroll
        for (int mq = 0; mq < 4; ++mq) {
          const int m = 4 * mh + mq;
          const f32x2 u0r = {acc[m][2 * q][0], acc[m][2 * q][1]}, u0i = {acc[m][2 * q + 1][0], acc[m][2 * q + 1][1]};
          const f32x2 u1r = {acc[m][2 * q][2], acc[m][2 * q][3]}, u1i = {acc[m][2 * q + 1][2], acc[m][2 * q + 1][3]};
          const f32x2 n0r = fma2(ar2, wr[q], fma2(ai2, wi[q], u0r)), n0i = fma2(ar2, wi[q], fma2(-ai2, wr[q], u0i));
          const f32x2 n1r = fma2(ar2, n0r, fma2(ai2, n0i, u1r)), n1i = fma2(ar2, n0i, fma2(-ai2, n0r, u1i));
          wr[q] = n1r; wi[q] = n1i;
          o[0][0][mq] = pk_bf16(n0r.x, n1r.x); o[0][1][mq] = pk_bf16(n0r.y, n1r.y);
          o[1][0][mq] = pk_bf16(n0i.x, n1i.x); o[1][1][mq] = pk_bf16(n0i.y, n1i.y);
        }
#pragma unroll
        for (int part = 0; part < 2; ++part)
#pragma unroll
          for (int run = 0; run < 2; ++run)
            *(LAS u32x4*)(scr + (part * 32 + 16 * q + cl) * 256 + (((unsigned)(4 * g + 2 * run + mh) ^ fsw) * 16)) = o[part][run];
      }
#pragma unroll
      for (int i = 0; i < 8; ++i) {
        const int part = i >> 2, pc = 4 * q + (i & 3);
        const u32x4 v = *(LAS u32x4*)(scr + part * 8192 + pc * 1024 + ln * 16);
        st16_wt(rh, (unsigned)(ln * 16 + pc * 1024), (unsigned)(((2 * chunk + wm) * 32 + part * 16 + cb) * 16384 + wn * 8192), v);
      }
      SCHED_FENCE();
    }
    STAMP(7);
    publish(LDS_CNT, sy + SY_H + chunk * 16 + cb, tag, ln);
    STAMP(8);
    residual();
  } else {
    RELANE();
    const int f0 = ft * 224, dd0 = dt1 * 128;
    const int k0 = 8 * g + q4;
    unsigned pa0 = (unsigned)(k0 * 448 + ((12 * wm + p4) ^ (3 * (g & 1))) * 16 + (p4 & 1) * 8);
    unsigned pa1 = (unsigned)(k0 * 448 + ((12 * wm + p4) ^ (3 * (g & 1))) * 16 + ((p4 & 1) ^ 1) * 8);
    unsigned pa2 = (unsigned)(k0 * 448 + ((24 + 2 * wm + (p4 >> 1)) ^ (3 * (g & 1))) * 16 + (p4 & 1) * 8);
    unsigned pb[4];
#pragma unroll
    for (int n = 0; n < 4; ++n) pb[n] = (unsigned)(B1_ABYTES + k0 * 256 + ((8 * wn + 2 * n + (p4 >> 1)) ^ ((q4 << 1) | ((g & 1) << 3))) * 16 + (p4 & 1) * 8);
    f32x4 acc[7][4];
#pragma unroll
    for (int m = 0; m < 7; ++m) {
      const u32x2 bv = *(const u32x2*)(a.b1 + f0 + (m < 6 ? 96 * wm + 32 * (m >> 1) + 8 * g + 4 * ((m & 1) ^ (g & 1)) : 192 + 16 * wm + 4 * g));
#pragma unroll
      for (int n = 0; n < 4; ++n) acc[m][n] = (f32x4){bflo(bv[0]), bfhi(bv[0]), bflo(bv[1]), bfhi(bv[1])};
    }
    TrFrag xa[7], xb[4], ya[7], yb[4];
    int slot = 0, dl = 0;
    auto side = [&](TrFrag* fa, TrFrag* fb, int i) {
      const int f = i >> 1, h = i & 1;
      if (f == 0) fa[0].h[h] = lds_rdtr(pa0 + h * 1792);
      else if (f < 5) fb[f - 1].h[h] = lds_rdtr(pb[f - 1] + h * 1024);
      else if (f < 10) fa[f - 4].h[h] = lds_rdtr((((f - 4) & 1) ? pa1 : pa0) + ((f - 4) >> 1) * 64 + h * 1792);
      else if (f == 10) fa[6].h[h] = lds_rdtr(pa2 + h * 1792);
      else if (i == 22) { dl = (slot == B1_NS - 1) ? -(B1_NS - 1) * B1_STAGE : B1_STAGE; slot = (slot == B1_NS - 1) ? 0 : slot + 1; }
      else if (i == 23) { pa0 += dl; pb[0] += dl; asm volatile("" : "+v"(pa0), "+v"(pb[0])); }
      else if (i == 24) { pb[1] += dl; pb[2] += dl; asm volatile("" : "+v"(pb[1]), "+v"(pb[2])); }
      else if (i == 25) { pb[3] += dl; pa1 += dl; asm volatile("" : "+v"(pb[3]), "+v"(pa1)); }
      else if (i == 26) { pa2 += dl; asm volatile("" : "+v"(pa2)); }
    };
    STAMP(15);
    bar();
    STAMP(3);
#pragma unroll
    for (int i = 0; i < 28; ++i) side(xa, xb, i);
#pragma unroll 1
    for (int t = 0; t < B1_NT; t += 2) {
      SCHED_FENCE(); wait_lgkm0(); bar(); SCHED_FENCE();
      phase<28>(acc, xa, xb, [&](int i) { side(ya, yb, i); });
      wait_lgkm0(); bar(); SCHED_FENCE();
      phase<28>(acc, ya, yb, [&](int i) { side(xa, xb, i); });
    }
    RELANE();
    STAMP(4);
    xb_clear();
    const rsrc_t ru = mk_rsrc(a.ws + WS_U, D * F * 2);
#pragma unroll
    for (int n = 0; n < 4; ++n) {
      const unsigned vo = (unsigned)((dd0 + 64 * wn + 16 * n + cl) * 64);
      u32x2 w[7];
#pragma unroll
      for (int m = 0; m < 7; ++m) {
        const f32x2 v0 = gelu_tanh2((f32x2){acc[m][n][0], acc[m][n][1]}), v1 = gelu_tanh2((f32x2){acc[m][n][2], acc[m][n][3]});
        w[m][0] = pk_bf16(v0.x, v0.y); w[m][1] = pk_bf16(v1.x, v1.y);
      }
#pragma unroll
      for (int mm = 0; mm < 3; ++mm) {
        const bool sw = g & 1;
        const u32x2 lo = sw ? w[2 * mm + 1] : w[2 * mm], hi = sw ? w[2 * mm] : w[2 * mm + 1];
        st16_wt(ru, vo + 16 * g, (unsigned)((ft * 7 + 3 * wm + mm) * D * 64), (u32x4){lo[0], lo[1], hi[0], hi[1]});
      }
      st8_wt(ru, vo + 8 * g, (unsigned)((ft * 7 + 6) * D * 64 + 32 * wm), w[6]);
    }
    STAMP(7);
    residual();
    publish(LDS_CNT, sy + SY_U + dt1 * 16 + ft, tag, ln);
    STAMP(8);
  }

  f32x4 acc[4][4];
#pragma unroll
  for (int n = 0; n < 4; ++n) { acc[0][n] = res[0][n]; acc[1][n] = res[1][n]; acc[2][n] = (f32x4){0.f, 0.f, 0.f, 0.f}; acc[3][n] = (f32x4){0.f, 0.f, 0.f, 0.f}; }
  const unsigned au0 = (unsigned)(wm * 4096 + cl * 64 + ((g ^ (((q4 & 1) << 1) ^ ((q4 >> 1) * 3))) * 16));
  unsigned pa0 = (unsigned)((wm * 32 + cl) * 128 + ((g ^ (cl >> 1)) * 16)), pa1 = pa0 ^ 64u;
  const unsigned sw0 = pa0 ^ au0 ^ 65536u, sw1 = pa1 ^ (au0 + 1024u) ^ 65536u;
  unsigned pb[4];
#pragma unroll
  for (int n = 0; n < 4; ++n) pb[n] = (unsigned)(16384 + (8 * g + q4) * 256 + ((8 * wn + 2 * n + (p4 >> 1)) ^ ((q4 << 1) | ((g & 1) << 3))) * 16 + (p4 & 1) * 8);
  bf16x8 xa[4], ya[4];
  TrFrag xb[4], yb[4];
  static_assert(P2_NS == 4 && NAB % 2 == 0 && NBA % 2 == 0 && NAB >= P2_NS && NAB <= 16 && PF_NST2 < 29 - NAB, "phase-2 loop: two stages per trip, 64-KB windows");
  auto rd = [&](bf16x8* fa, TrFrag* fb, int so, int ks, int r) {
    const unsigned pa_ = (ks ? pa1 : pa0) + so;
    if (r == 0) fa[0] = lds_rd128(pa_);
    else if (r < 9) fb[(r - 1) >> 1].h[(r - 1) & 1] = lds_rdtr(pb[(r - 1) >> 1] + so + ks * 8192 + ((r - 1) & 1) * 1024);
    else if (r < 12) fa[r - 8] = lds_rd128(pa_ + ((r - 8) >> 1) * 8192 + ((r - 8) & 1) * 2048);
  };
  STAMP(18);
  bar();
  STAMP(9);
#pragma unroll
  for (int r = 0; r < 12; ++r) rd(xa, xb, 0, 0, r);
#pragma unroll 1
  for (int t = 0; t < nt2; t += 2) {
    unsigned x0, x1;
    SCHED_FENCE(); wait_lgkm0(); SCHED_FENCE();
    phase<16>(acc, xa, xb, [&](int i) { rd(ya, yb, 0, 1, i); });
    wait_lgkm0(); bar(); SCHED_FENCE();
    phase<16>(acc, ya, yb, [&](int i) {
      rd(xa, xb, P2_STAGE, 0, i);
      if (i == 13) { const bool sw = (t + 2 == nC); x0 = sw ? sw0 : 65536u; x1 = sw ? sw1 : 65536u; asm volatile("" : "+v"(x0), "+v"(x1)); }
      if (i == 14) { pa0 ^= x0; asm volatile("" : "+v"(pa0)); }
    });
    wait_lgkm0(); SCHED_FENCE();
    phase<16>(acc, xa, xb, [&](int i) {
      rd(ya, yb, P2_STAGE, 1, i);
      if (i == 12) { pb[0] ^= 65536u; pb[1] ^= 65536u; asm volatile("" : "+v"(pb[0]), "+v"(pb[1])); }
      if (i == 13) { pb[2] ^= 65536u; pb[3] ^= 65536u; asm volatile("" : "+v"(pb[2]), "+v"(pb[3])); }
      if (i == 14) { pa1 ^= x1; asm volatile("" : "+v"(pa1)); }
    });
    wait_lgkm0(); bar(); SCHED_FENCE();
    phase<16>(acc, ya, yb, [&](int i) { rd(xa, xb, 0, 0, i); });
  }
  STAMP(10);
  const unsigned xo = (unsigned)(wid * 4096 + ln * 16);
#pragma unroll
  for (int n = 0; n < 4; ++n) {
    u32x4 w = {pk_bf16(acc[2][n][0], acc[2][n][1]), pk_bf16(acc[2][n][2], acc[2][n][3]), pk_bf16(acc[3][n][0], acc[3][n][1]), pk_bf16(acc[3][n][2], acc[3][n][3])};
#pragma unroll
    for (int k = 0; k < 4; ++k) w[k] = w[k] ? w[k] : 0x80000000u;
    st16_wt(rxb, xo + n * 1024, bid * 16384, w);
  }
  STAMP(11);
  u32x4 rv[4];
#pragma unroll 1
  for (unsigned spins = 0; spins < SPIN_CAP; ++spins) {
    bool ok = true;
#pragma unroll
    for (int n = 0; n < 4; ++n) rv[n] = __builtin_amdgcn_raw_buffer_load_b128(rxb, xo + n * 1024 + pa * 16384, 0, 16);
#pragma unroll
    for (int n = 0; n < 4; ++n) ok &= (rv[n][0] != 0u) & (rv[n][1] != 0u) & (rv[n][2] != 0u) & (rv[n][3] != 0u);
    if (__all(ok)) break;
#pragma unroll 1
    for (; spins < SPIN_CAP; ++spins) {
      __builtin_amdgcn_s_sleep(XB_SLEEP);
      asm volatile("" ::: "memory");
      if (__builtin_amdgcn_readfirstlane(__builtin_amdgcn_raw_buffer_load_b32(rxb, wid * 4096 + 4092 + pa * 16384, 0, 16)) != 0u) break;
    }
    asm volatile("" ::: "memory");
  }
  STAMP(12);
#pragma unroll
  for (int n = 0; n < 4; ++n) {
    acc[0][n] += (f32x4){bflo(rv[n][0]), bfhi(rv[n][0]), bflo(rv[n][1]), bfhi(rv[n][1])};
    acc[1][n] += (f32x4){bflo(rv[n][2]), bfhi(rv[n][2]), bflo(rv[n][3]), bfhi(rv[n][3])};
  }
#pragma unroll
  for (int n = 0; n < 4; ++n)
#pragma unroll
    for (int mi = 0; mi < 2; ++mi) {
      const int d = d0 + 64 * kh + 32 * wm + 16 * mi + 4 * g, tok = t0 + 64 * wn + 16 * n + cl;
      __builtin_amdgcn_raw_buffer_store_b128(__builtin_bit_cast(u32x4, acc[mi][n]), mk_rsrc(a.out, S * D * 4), (unsigned)(tok * D + d) * 4u, 0, 16);
    }
  STAMP(13);
}

extern "C" void kernel_launch(void* const* d_in, const int* in_sizes, int n_in, void* d_out, int out_size, void* d_ws, size_t ws_size,
                              hipStream_t stream) {
  (void)hipFuncSetAttribute((const void*)fused, hipFuncAttributeMaxDynamicSharedMemorySize, LDS_BYTES);
  Args a;
  a.x = (const u16*)d_in[0]; a.Lre = (const u16*)d_in[1]; a.Lim = (const u16*)d_in[2]; a.Bre = (const u16*)d_in[3]; a.Bim = (const u16*)d_in[4];
  a.Cre = (const u16*)d_in[5]; a.Cim = (const u16*)d_in[6]; a.logdt = (const u16*)d_in[7]; a.Dsk = (const u16*)d_in[8];
  a.w1 = (const u16*)d_in[10]; a.b1 = (const u16*)d_in[11]; a.w2 = (const u16*)d_in[12]; a.b2 = (const u16*)d_in[13];
  a.out = (float*)d_out; a.ws = (char*)d_ws;
  fused<<<256, 512, LDS_BYTES, stream>>>(a);
}
```

```cpp
#define PROBE_I 3
#define PROBE_ROLE 0
#include <hip/hip_runtime.h>
#include <stdint.h>

typedef unsigned short u16;
typedef unsigned long long u64;
typedef __bf16 bf16x8 __attribute__((ext_vector_type(8)));
typedef short s16x4 __attribute__((ext_vector_type(4)));
typedef short s16x8 __attribute__((ext_vector_type(8)));
typedef float f32x2 __attribute__((ext_vector_type(2)));
typedef float f32x4 __attribute__((ext_vector_type(4)));
typedef unsigned u32x4 __attribute__((ext_vector_type(4)));
typedef unsigned u32x2 __attribute__((ext_vector_type(2)));
#define LAS __attribute__((address_space(3)))
#define GAS __attribute__((address_space(1)))
typedef GAS unsigned long long gu64;
typedef __amdgpu_buffer_rsrc_t rsrc_t;
#define RLX_AGENT __ATOMIC_RELAXED, __HIP_MEMORY_SCOPE_AGENT

constexpr int S = 2048, D = 1024, P = 1024, F = 3584;
constexpr size_t MB = 1024 * 1024;
constexpr size_t WS_HL = 4 * MB, WS_HT = 8 * MB, WS_U = 16 * MB, WS_XB = 24 * MB, WS_SYNC = 32 * MB;
constexpr int SY_LN = 64, SY_H = 320, SY_U = 448, SY_ST = 1024, SY_SB = 3072, SY_GR = 5120;
constexpr unsigned SYNC_BYTES = (SY_GR + 8 * P * 2) * 8;
constexpr unsigned SPIN_CAP = 1u << 17;
#define NBA 24
#define NAB 6
constexpr unsigned CODE_PF_BYTES = 36 * 1024;
constexpr unsigned long long CB_ORDER = 0x80F7E6D5C491B3A2ull;
#define OUT_LDS 1
#define XB_SLEEP 2
#define PF_NST 11
#define PF_ST0 1
#define PF_N1 13
#define PF_NST2 20
#define PF_WA 0
#define PF_BT 5
#define PF_WB 4
#define A1_RS 2
#define PSTAMP(w, i)
#define STAMP_INIT()
#define STAMP_DUMP()
#define TRACE(e)
#define TRACE_DUMP()
#define STAMP(i) do { if ((i) == 0) probe_t0 = __builtin_amdgcn_s_memrealtime(); if ((i) == PROBE_I) probe_d = __builtin_amdgcn_s_memrealtime() - probe_t0; } while (0)

__device__ __forceinline__ float bf2f(u16 v) { return __uint_as_float(((unsigned)v) << 16); }
__device__ __forceinline__ float bflo(unsigned v) { return __uint_as_float(v << 16); }
__device__ __forceinline__ float bfhi(unsigned v) { return __uint_as_float(v & 0xffff0000u); }
__device__ __forceinline__ unsigned pk_bf16(float lo, float hi) {
  typedef __bf16 bf16x2 __attribute__((ext_vector_type(2)));
  f32x2 v = {lo, hi};
  bf16x2 r = __builtin_convertvector(v, bf16x2);
  return __builtin_bit_cast(unsigned, r);
}
__device__ __forceinline__ float rbf(float f) { return bflo(pk_bf16(f, f)); }
__device__ __forceinline__ float dot2(unsigned a, unsigned b, float c) {
  typedef __bf16 bf16x2 __attribute__((ext_vector_type(2)));
  return __builtin_amdgcn_fdot2_f32_bf16(__builtin_bit_cast(bf16x2, a), __builtin_bit_cast(bf16x2, b), c, false);
}
__device__ __forceinline__ f32x2 fma2(f32x2 a, f32x2 b, f32x2 c) { return __builtin_elementwise_fma(a, b, c); }
__device__ __forceinline__ f32x2 bc2(float v) { return (f32x2){v, v}; }

__device__ __forceinline__ rsrc_t mk_rsrc(const void* p, unsigned bytes) { return __builtin_amdgcn_make_buffer_rsrc((void*)p, 0, bytes, 0x00020000); }
__device__ __forceinline__ void dma16(rsrc_t rs, unsigned voff, unsigned soff, LAS char* l) { __builtin_amdgcn_raw_ptr_buffer_load_lds(rs, (LAS void*)l, 16, voff, soff, 0, 0); }
__device__ __forceinline__ void dma16c(rsrc_t rs, unsigned voff, unsigned soff, LAS char* l) { __builtin_amdgcn_raw_ptr_buffer_load_lds(rs, (LAS void*)l, 16, voff, soff, 0, 16); }
__device__ __forceinline__ void dma4(rsrc_t rs, unsigned voff, unsigned soff, LAS char* l) { __builtin_amdgcn_raw_ptr_buffer_load_lds(rs, (LAS void*)l, 4, voff, soff, 0, 0); }
__device__ __forceinline__ void st16_wt(rsrc_t rs, unsigned voff, unsigned soff, u32x4 v) { __builtin_amdgcn_raw_buffer_store_b128(v, rs, voff + soff, 0, 16); }
__device__ __forceinline__ void st8_wt(rsrc_t rs, unsigned voff, unsigned soff, u32x2 v) { __builtin_amdgcn_raw_buffer_store_b64(v, rs, voff + soff, 0, 16); }
template <int N> __device__ __forceinline__ void wait_vm() { asm volatile("s_waitcnt vmcnt(%0)" ::"n"(N) : "memory"); }
__device__ __forceinline__ void wait_lgkm0() { asm volatile("" ::: "memory"); __builtin_amdgcn_s_waitcnt(0xC07F); asm volatile("" ::: "memory"); }
__device__ __forceinline__ void bar() {
  asm volatile("" ::: "memory");
  __builtin_amdgcn_s_barrier();
  asm volatile("" ::: "memory");
}
#define SCHED_FENCE() __builtin_amdgcn_sched_barrier(0)
__device__ __forceinline__ bf16x8 lds_rd128(unsigned p) { return *(LAS bf16x8*)(size_t)p; }
__device__ __forceinline__ s16x4 lds_rdtr(unsigned p) { return __builtin_amdgcn_ds_read_tr16_b64_v4i16((LAS s16x4*)(size_t)p); }
struct TrFrag { s16x4 h[2]; };
__device__ __forceinline__ bf16x8 frag(const bf16x8& f) { return f; }
__device__ __forceinline__ bf16x8 frag(const TrFrag& f) { s16x8 ab = __builtin_shufflevector(f.h[0], f.h[1], 0, 1, 2, 3, 4, 5, 6, 7); return __builtin_bit_cast(bf16x8, ab); }
#define MFMA(a, b, c) __builtin_amdgcn_mfma_f32_16x16x32_bf16(frag(a), frag(b), (c), 0, 0, 0)
template <int NM, class FA, class FB, class Side>
__device__ __forceinline__ void phase(f32x4 (*acc)[4], const FA* fa, const FB* fb, Side side) {
#pragma unroll
  for (int i = 0; i < NM; ++i) {
    acc[i >> 2][i & 3] = MFMA(fa[i >> 2], fb[i & 3], acc[i >> 2][i & 3]);
    side(i);
    SCHED_FENCE();
  }
}

template <int NL> __device__ __forceinline__ void wait_flags(gu64* p, bool active, u64 tag) {
#pragma unroll 1
  for (unsigned spins = 0; spins < SPIN_CAP; ++spins) {
    bool ok = true;
    if (active) {
#pragma unroll
      for (int k = 0; k < NL; ++k) ok &= __hip_atomic_load(p + 64 * k, RLX_AGENT) == tag;
    }
    if (__all(ok)) break;
    __builtin_amdgcn_s_sleep(2);
  }
  __builtin_amdgcn_fence(__ATOMIC_ACQUIRE, "wavefront");
}

template <int NL> __device__ __forceinline__ int flag_prefix(gu64* p, int n, int need, u64 tag, int lane) {
  int pre = 0;
#pragma unroll 1
  for (unsigned spins = 0; spins < SPIN_CAP; ++spins) {
    const u64 m0 = ~__ballot(lane >= n || __hip_atomic_load(p, RLX_AGENT) == tag);
    const u64 m1 = NL == 2 ? ~__ballot(__hip_atomic_load(p + 64, RLX_AGENT) == tag) : 0ull;
    pre = m0 ? __builtin_ctzll(m0) : m1 ? 64 + __builtin_ctzll(m1) : 128;
    if (pre >= need) break;
    __builtin_amdgcn_s_sleep(1);
  }
  __builtin_amdgcn_fence(__ATOMIC_ACQUIRE, "wavefront");
  return pre < need ? 128 : pre;
}
__device__ __forceinline__ void lds_wait(unsigned addr, unsigned want) {
#pragma unroll 1
  for (unsigned spins = 0; spins < (SPIN_CAP << 4); ++spins) {
    if (__builtin_amdgcn_readfirstlane(__hip_atomic_load((LAS unsigned*)(size_t)addr, __ATOMIC_RELAXED, __HIP_MEMORY_SCOPE_WORKGROUP)) >= want) break;
    __builtin_amdgcn_s_sleep(1);
  }
  asm volatile("" ::: "memory");
}
__device__ __forceinline__ void publish(unsigned cnt, gu64* flag, u64 tag, int lane) {
  wait_vm<0>();
  wait_lgkm0();
  if (lane == 0 && __hip_atomic_fetch_add((LAS unsigned*)(size_t)cnt, 1u, __ATOMIC_RELAXED, __HIP_MEMORY_SCOPE_WORKGROUP) == 3u) __hip_atomic_store(flag, tag, RLX_AGENT);
}

template <class Issue> __device__ __forceinline__ void prod_fill(int n, Issue issue) {
#pragma unroll 1
  for (int t = 0; t < n; ++t) issue();
}
template <int L, int NS, class Issue> __device__ __forceinline__ void prod_run(int n, Issue issue) {
#pragma unroll 1
  for (int t = 0; t < n; ++t) { wait_vm<(NS - 2) * L>(); bar(); issue(); }
}
template <int L, int K, int X = 0> __device__ __forceinline__ void prod_tail() {
  wait_vm<K * L + X>();
  bar();
  if constexpr (K > 0) prod_tail<L, K - 1, X>();
}

__device__ __forceinline__ float wave_sum(float v) {
  v += __builtin_bit_cast(float, __builtin_amdgcn_update_dpp(0, __builtin_bit_cast(int, v), 0xB1, 0xf, 0xf, true));
  v += __builtin_bit_cast(float, __builtin_amdgcn_update_dpp(0, __builtin_bit_cast(int, v), 0x4E, 0xf, 0xf, true));
  v += __builtin_bit_cast(float, __builtin_amdgcn_update_dpp(0, __builtin_bit_cast(int, v), 0x141, 0xf, 0xf, true));
  v += __builtin_bit_cast(float, __builtin_amdgcn_update_dpp(0, __builtin_bit_cast(int, v), 0x140, 0xf, 0xf, true));
  const int iv = __builtin_bit_cast(int, v);
  const float a = __builtin_bit_cast(float, __builtin_amdgcn_readlane(iv, 0)), b = __builtin_bit_cast(float, __builtin_amdgcn_readlane(iv, 16));
  const float c = __builtin_bit_cast(float, __builtin_amdgcn_readlane(iv, 32)), d = __builtin_bit_cast(float, __builtin_amdgcn_readlane(iv, 48));
  return (a + b) + (c + d);
}

struct Args {
  const u16 *x, *Lre, *Lim, *Bre, *Bim, *Cre, *Cim, *logdt, *Dsk, *w1, *b1, *w2, *b2;
  float* out;
  char* ws;
};

constexpr int A1_NS = 3, A1_STAGE = 49152, A1_NT = D / 64;
constexpr int B1_NS = 7, B1_ABYTES = 32 * 448, B1_STAGE = B1_ABYTES + 32 * 256, B1_NT = S / 32;
constexpr int P2_NS = 4, P2_STAGE = 32768;
constexpr int LDS_BYTES = 160 * 1024;
constexpr int LDS_COEF = A1_NS * A1_STAGE;
constexpr int LDS_TOT = LDS_COEF + 14 * 256;
constexpr int LDS_SB = LDS_TOT + 512;
constexpr int LDS_STAT = LDS_SB + 512;
constexpr int LDS_RAW = LDS_STAT + 8 * 272;
constexpr int LDS_RES = P2_NS * P2_STAGE;
constexpr int LDS_U6 = LDS_COEF;
constexpr int LDS_RES2 = 158208;
constexpr int LDS_CNT = LDS_BYTES - 256 - 2048 - 64;
constexpr int LDS_FLG = LDS_BYTES - 256 - 2048;
constexpr int LDS_DUMMY = LDS_BYTES - 256;
static_assert(LDS_RAW + 3072 + 768 <= LDS_CNT && B1_NS * B1_STAGE <= LDS_CNT && LDS_RES + 16384 <= LDS_COEF && LDS_RAW + 3840 <= LDS_RES2 && LDS_RES2 + 1536 <= LDS_CNT && P2_NS * P2_STAGE <= LDS_COEF && LDS_RAW % 16 == 0, "lds");

constexpr int UA = NBA * 32, WA = PF_WA * 128 / 2, WB = PF_WB * 128 / 2;
constexpr int RA = (UA - WA - WB + 127) / 128, RB = (1792 - UA - WA - WB + 127) / 128;

__device__ __forceinline__ f32x2 gelu_tanh2(f32x2 x) {
  const f32x2 z = x * fma2(x * x, bc2(-0.10294323967f), bc2(-2.30220819813f));
  const f32x2 d = (f32x2){__builtin_amdgcn_exp2f(z.x), __builtin_amdgcn_exp2f(z.y)} + bc2(1.0f);
  return x * (f32x2){__builtin_amdgcn_rcpf(d.x), __builtin_amdgcn_rcpf(d.y)};
}
#define CMUL(or_, oi_, ar_, ai_, br_, bi_) do { const float t_ = (ar_) * (br_) - (ai_) * (bi_); oi_ = (ar_) * (bi_) + (ai_) * (br_); or_ = t_; } while (0)

__global__ __launch_bounds__(512, 2) void fused(Args a) {
  extern __shared__ __attribute__((aligned(16))) char smem[];
  LAS char* lds = (LAS char*)smem;
  u64 pc0;
  asm volatile("s_getpc_b64 %0" : "=s"(pc0));
  const int wid = __builtin_amdgcn_readfirstlane(threadIdx.x >> 6), lane = threadIdx.x & 63;
  const int bid = blockIdx.x, xc = bid & 7, idx = bid >> 3, xg = xc >> 1;
  const bool roleA = !(xc & 1);
  const int ri = bid >> 1;
  gu64* sy = (gu64*)(a.ws + WS_SYNC);
  u64 tag;
#define LOAD_TAG() do { const __attribute__((address_space(4))) u64* ep_ = (const __attribute__((address_space(4))) u64*)(a.ws + WS_SYNC); \
    asm volatile("" : "+s"(ep_)::"memory"); tag = *ep_ + 1ull; } while (0)
#define HOLD_TAG() asm volatile("" ::"s"(tag))
#define tag32 ((unsigned)tag)
  const int chunk = 4 * (xg & 1) + (idx & 3), cb = 8 * (xg >> 1) + (idx >> 2);
  const int ft = 4 * xg + (idx & 3), dt1 = idx >> 2;
  const int tt = 4 * xg + (idx & 3), dt = idx >> 2;
  const int t0 = tt * 128, d0 = dt * 128;
  u64 cbs = roleA ? CB_ORDER : CB_ORDER >> (2 * (32 - NAB));
  const int nC = roleA ? 32 - NAB : NAB, ku0 = roleA ? 0 : NBA, nt2 = nC + (roleA ? NBA : 56 - NBA);
  const rsrc_t rsy = mk_rsrc(a.ws + WS_SYNC, SYNC_BYTES);
  u64 probe_t0 = 0, probe_d = 0; (void)probe_t0; (void)probe_d;
  STAMP_INIT();
  STAMP(0);

  auto touch = [&](const u16* base, unsigned bytes, int u0, int n) {
    const rsrc_t rs = mk_rsrc(base, bytes);
#pragma unroll 1
    for (int k = 0; k < n; ++k) dma4(rs, (unsigned)(lane * 128), (unsigned)(u0 + k) * 8192u, lds + LDS_DUMMY);
  };
  auto w1_voff = [&](int piece) {
    const int ch = piece * 64 + lane, k = ch / 28, pos = ch - k * 28;
    return (unsigned)(k * F + ft * 224 + (pos ^ (3 * ((k >> 3) & 1))) * 8) * 2u;
  };
  if (!roleA) {
    const int row = ri * 16 + wid * 2;
    u32x4 r[2][2], brow[2][2];
    {
      const u16* xp = a.x + (size_t)row * D;
      const u16* bp = (ri < 64 ? a.Bre : a.Bim) + (size_t)(row & 1023) * D;
      const unsigned lo = (unsigned)lane * 16u;
      asm volatile("global_load_dwordx4 %0, %4, %5\n\tglobal_load_dwordx4 %1, %4, %5 offset:1024\n\tglobal_load_dwordx4 %2, %4, %5 offset:2048\n\tglobal_load_dwordx4 %3, %4, %5 offset:3072"
                   : "=&v"(r[0][0]), "=&v"(r[0][1]), "=&v"(r[1][0]), "=&v"(r[1][1]) : "v"(lo), "s"(xp) : "memory");
      asm volatile("global_load_dwordx4 %0, %4, %5\n\tglobal_load_dwordx4 %1, %4, %5 offset:1024\n\tglobal_load_dwordx4 %2, %4, %5 offset:2048\n\tglobal_load_dwordx4 %3, %4, %5 offset:3072"
                   : "=&v"(brow[0][0]), "=&v"(brow[0][1]), "=&v"(brow[1][0]), "=&v"(brow[1][1]) : "v"(lo), "s"(bp) : "memory");
    }
    LOAD_TAG();
    bar();
    {
      const rsrc_t rs = mk_rsrc(a.w1, S * F * 2);
#pragma unroll
      for (int j = 0; j < 2; ++j) {
        const int piece = j == 0 ? wid : (wid < 6 ? wid + 8 : wid);
        const unsigned vo = w1_voff(piece);
#pragma unroll
        for (int t = 0; t < B1_NS - 1; ++t) dma16(rs, vo, t * (32 * F * 2), lds + t * B1_STAGE + piece * 1024);
      }
    }
    wait_vm<16>();
    STAMP(20);
    asm volatile("" : "+v"(r[0][0]), "+v"(r[0][1]), "+v"(r[1][0]), "+v"(r[1][1])::"memory");
    const rsrc_t rx = mk_rsrc(a.ws + WS_HL, S * D * 2);
#pragma unroll
    for (int pass = 0; pass < 2; ++pass) {
      float s1[2], s2[2];
#pragma unroll
      for (int i = 0; i < 2; ++i) {
        float a1 = 0.f, a2 = 0.f;
#pragma unroll
        for (int k = 0; k < 8; ++k) { const unsigned w = r[i][k >> 2][k & 3]; a1 = dot2(w, 0x3f803f80u, a1); a2 = dot2(w, w, a2); }
        s1[i] = a1; s2[i] = a2;
      }
#pragma unroll
      for (int i = 0; i < 2; ++i) { s1[i] = wave_sum(s1[i]); s2[i] = wave_sum(s2[i]); }
#pragma unroll
      for (int i = 0; i < 2; ++i) {
        const float mean = s1[i] * (1.0f / D), var = fmaxf(fmaf(-mean, mean, s2[i] * (1.0f / D)), 0.f);
        const unsigned ms = pk_bf16(mean, __builtin_amdgcn_sqrtf(rbf(rbf(var) + 1e-5f)));
        const float rs_ = __builtin_amdgcn_rcpf(bfhi(ms)), mb = bflo(ms);
#pragma unroll
        for (int k = 0; k < 8; ++k) {
          const unsigned w = r[i][k >> 2][k & 3];
          r[i][k >> 2][k & 3] = pk_bf16((bflo(w) - mb) * rs_, (bfhi(w) - mb) * rs_);
        }
        if (pass == 0) {
          if (lane == 0) __hip_atomic_store(sy + SY_ST + row + i, ((u64)tag32 << 32) | ms, RLX_AGENT);
        } else {
          st16_wt(rx, (unsigned)lane * 16u, (unsigned)((row + i) * D) * 2u, r[i][0]);
          st16_wt(rx, (unsigned)lane * 16u + 1024u, (unsigned)((row + i) * D) * 2u, r[i][1]);
        }
      }
    }
    STAMP(1);
    asm volatile("s_waitcnt vmcnt(0)" : "+v"(brow[0][0]), "+v"(brow[0][1]), "+v"(brow[1][0]), "+v"(brow[1][1])::"memory");
    bar();
    if (wid == 0 && lane == 0) __hip_atomic_store(sy + SY_LN + ri, tag, RLX_AGENT);
    STAMP(2);
    {
      float sums[2];
#pragma unroll
      for (int i = 0; i < 2; ++i) {
        float s = 0.f;
#pragma unroll
        for (int k = 0; k < 8; ++k) { const unsigned w = brow[i][k >> 2][k & 3]; s = dot2(w, 0x3f803f80u, s); }
        sums[i] = wave_sum(s);
      }
      if (lane < 2) __hip_atomic_store(sy + SY_SB + row + lane, ((u64)tag32 << 32) | __float_as_uint(lane ? sums[1] : sums[0]), RLX_AGENT);
    }
  }

  if (wid >= 4) {
    const int pw = wid - 4;
    if (pw == 3) {
      auto flags_fetch = [&]() {
        dma16c(rsy, (unsigned)lane * 16u, SY_LN * 8, lds + LDS_FLG);
        dma16c(rsy, (unsigned)lane * 16u, SY_LN * 8 + 1024, lds + LDS_FLG + 1024);
      };
      auto flags_check = [&]() {
        bool ok = true;
#pragma unroll
        for (int k = 0; k < 4; ++k) ok &= *(LAS u64*)(lds + LDS_FLG + k * 512 + lane * 8) == tag;
        if (!__all(ok)) wait_flags<4>(sy + SY_LN + lane, true, tag);
      };
      auto xb_clear = [&](int k0) {
        const rsrc_t rxb = mk_rsrc(a.ws + WS_XB, 256 * 16384);
#pragma unroll
        for (int k = 0; k < 4; ++k) st16_wt(rxb, (unsigned)(lane * 16 + k * 1024), (unsigned)(bid * 16384 + k0 * 4096), (u32x4){0u, 0u, 0u, 0u});
      };
      if (lane < 4) *(LAS unsigned*)(size_t)(LDS_CNT + lane * 4) = 0u;
      dma4(mk_rsrc((const void*)(pc0 & ~255ull), CODE_PF_BYTES), (unsigned)(lane * 128), (unsigned)((idx % 5) * 8192), lds + LDS_DUMMY);
      if (roleA) {
        st16_wt(rsy, (unsigned)lane * 16u, (unsigned)(SY_GR * 8 + (chunk * P + cb * 64) * 16), (u32x4){0u, 0u, 0u, 0u});
        dma4(mk_rsrc(a.Lre, P * 2), (unsigned)(lane & 31) * 4u, cb * 128, lds + LDS_RAW + 3072);
        dma4(mk_rsrc(a.Lim, P * 2), (unsigned)(lane & 31) * 4u, cb * 128, lds + LDS_RAW + 3072 + 256);
        dma4(mk_rsrc(a.logdt, P * 2), (unsigned)(lane & 31) * 4u, cb * 128, lds + LDS_RAW + 3072 + 512);
        LOAD_TAG(); HOLD_TAG();
        const int bst = PF_ST0 + ((ri * PF_NST) >> 7);
        auto step = [&](int t) {
          bar();
          if (t == bst) touch(a.w1, S * F * 2, 128 + PF_N1 * ri, PF_N1);
          if (t == bst + PF_NST) touch(a.w1, S * F * 2, 128 + 128 * PF_N1 + (13 - PF_N1) * ri, 13 - PF_N1);
        };
        PSTAMP(7, 24);
        step(0); step(1); step(2);
        if (bst <= 2) wait_vm<PF_N1>(); else wait_vm<0>();
        if (lane == 0) __hip_atomic_store(sy + SY_LN + 128 + ri, tag, RLX_AGENT);
        LAS float* cf = (LAS float*)(lds + LDS_COEF) + lane;
        const float lr = bf2f(*(LAS u16*)(lds + LDS_RAW + 3072 + lane * 2)), li = bf2f(*(LAS u16*)(lds + LDS_RAW + 3072 + 256 + lane * 2));
        const float dtt = rbf(expf(bf2f(*(LAS u16*)(lds + LDS_RAW + 3072 + 512 + lane * 2)))), er = expf(dtt * lr);
        float arg = dtt * li;
        asm volatile("" : "+v"(arg));
        step(3);
        float sn, cs;
        sincosf(arg, &sn, &cs);
        float pr = er * cs, pi = er * sn;
        asm volatile("" : "+v"(pr), "+v"(pi));
        step(4);
        const float nr = pr - 1.0f, den = lr * lr + li * li;
        cf[0] = pr; cf[64] = pi;
        cf[128] = 2.0f * (nr * lr + pi * li) / den; cf[192] = 2.0f * (pi * lr - nr * li) / den;
        step(5);
#pragma unroll
        for (int k = 0; k < 8; ++k) {
          const float t = pr * pr - pi * pi; pi = 2.0f * pr * pi; pr = t;
          if (k >= 3) { cf[(4 + 2 * (k - 3)) * 64] = pr; cf[(5 + 2 * (k - 3)) * 64] = pi; }
        }
#pragma unroll 1
        for (int t = 6; t < 12; ++t) { step(t); if (t < 10) xb_clear(t - 6); }
        const unsigned sbo = (unsigned)(SY_SB * 8 + cb * 512 + (lane >> 5) * (P * 8) + (lane & 31) * 16);
#pragma unroll 1
        for (unsigned spins = 0;; ++spins) {
          dma16c(rsy, (unsigned)lane * 16u, (unsigned)(SY_ST * 8 + chunk * 2048), lds + LDS_RAW);
          dma16c(rsy, (unsigned)lane * 16u, (unsigned)(SY_ST * 8 + chunk * 2048 + 1024), lds + LDS_RAW + 1024);
          dma16c(rsy, sbo, 0, lds + LDS_RAW + 2048);
          if (spins == 0) { flags_fetch(); step(12); step(13); }
          wait_vm<0>();
          u32x2 gs[4], gb[2];
          bool ok = true;
#pragma unroll
          for (int k = 0; k < 4; ++k) { gs[k] = *(LAS u32x2*)(lds + LDS_RAW + k * 512 + lane * 8); ok &= gs[k][1] == tag32; }
#pragma unroll
          for (int k = 0; k < 2; ++k) { gb[k] = *(LAS u32x2*)(lds + LDS_RAW + 2048 + k * 512 + lane * 8); ok &= gb[k][1] == tag32; }
          if (__all(ok) || spins > SPIN_CAP) {
#pragma unroll
            for (int k = 0; k < 4; ++k) {
              LAS float* e = (LAS float*)(lds + LDS_STAT + (2 * k + (lane >> 5)) * 272 + (lane & 15) * 16 + ((lane >> 4) & 1) * 4);
              e[0] = -bflo(gs[k][0]); e[2] = __builtin_amdgcn_rcpf(bfhi(gs[k][0]));
            }
#pragma unroll
            for (int k = 0; k < 2; ++k) *(LAS unsigned*)(lds + LDS_SB + k * 256 + lane * 4) = gb[k][0];
            break;
          }
          __builtin_amdgcn_s_sleep(2);
        }
        touch(a.Cre, D * P * 2, 2 * ri, 2); touch(a.Cim, D * P * 2, 2 * ri, 2);
        touch(a.w2, F * S * 2, UA + (PF_WA / 2) * ri, PF_WA / 2); touch(a.w2, F * S * 2, (PF_WA / 2) * ri, PF_WA / 2);
        bar();
        flags_check();
        wait_lgkm0();
        step(15);
      } else {
#pragma unroll 1
        for (int t = 0; t < 13; ++t) { bar(); if (t >= 4 && !(t & 1) && t < 12) xb_clear((t - 4) >> 1); }
        flags_fetch();
#pragma unroll 1
        for (int t = 13; t < 20; ++t) bar();
        wait_vm<0>();
        flags_check();
        if (bid == 255 && lane == 0) __hip_atomic_store(sy, tag, RLX_AGENT);
#pragma unroll 1
        for (int t = 20; t < B1_NT - PF_BT; ++t) bar();
        touch(a.w2, F * S * 2, UA + WA + (PF_WB / 2) * ri, PF_WB / 2); touch(a.w2, F * S * 2, WA + (PF_WB / 2) * ri, PF_WB / 2);
#pragma unroll 1
        for (int t = 0; t < PF_BT; ++t) bar();
      }
    } else if (roleA) {
      const int T0 = chunk * 256, p0 = cb * 64;
      if (pw < 2) {
        const int i3 = lane >> 3;
        const unsigned ve = (unsigned)((T0 + 128 * pw + 32 * (i3 >> 2) + 16 * (i3 & 1) + ((i3 >> 1) & 1)) * D * 2 + ((lane & 7) ^ (lane >> 4)) * 16);
        const unsigned vo = (ve ^ 64u) + 64 * D * 2;
        const rsrc_t rs = mk_rsrc(a.x, S * D * 2);
        unsigned so = 0, lo = pw * 16384;
        auto issue = [&]() {
#pragma unroll
          for (int j = 0; j < 16; ++j) dma16(rs, (j & 1) ? vo : ve, so + (j >> 1) * (2 * D * 2), lds + lo + j * 1024);
          so += 128; lo = lo >= (A1_NS - 1) * A1_STAGE ? lo - (A1_NS - 1) * A1_STAGE : lo + A1_STAGE;
        };
        PSTAMP(4, 21);
        prod_fill(A1_NS - 1, issue);
        PSTAMP(4, 22);
        LOAD_TAG(); HOLD_TAG();
        prod_run<16, A1_NS>(A1_NT - (A1_NS - 1), issue);
        prod_tail<16, A1_NS - 2>();
      } else {
        const unsigned ve = (unsigned)((p0 + (lane >> 3)) * D * 2 + ((lane & 7) ^ (lane >> 4)) * 16), vo = ve ^ 64u;
        const rsrc_t rre = mk_rsrc(a.Bre, P * D * 2), rim = mk_rsrc(a.Bim, P * D * 2);
        unsigned so = 0, lo = 32768;
        auto issue = [&]() {
#pragma unroll
          for (int j = 0; j < 8; ++j) {
            dma16(rre, (j & 1) ? vo : ve, so + j * (8 * D * 2), lds + lo + (j >> 1) * 4096 + (j & 1) * 1024);
            dma16(rim, (j & 1) ? vo : ve, so + j * (8 * D * 2), lds + lo + (j >> 1) * 4096 + (j & 1) * 1024 + 2048);
          }
          so += 128; lo = lo >= (A1_NS - 1) * A1_STAGE ? lo - (A1_NS - 1) * A1_STAGE : lo + A1_STAGE;
        };
        prod_fill(A1_NS - 1, issue);
        LOAD_TAG(); HOLD_TAG();
        prod_run<16, A1_NS>(A1_NT - (A1_NS - 1), issue);
        prod_tail<16, A1_NS - 2>();
      }
    } else {
      const int dd0 = dt1 * 128;
      if (pw < 2) {
        unsigned voff[7];
#pragma unroll
        for (int j = 0; j < 7; ++j) voff[j] = w1_voff(pw * 7 + j);
        const rsrc_t rs = mk_rsrc(a.w1, S * F * 2);
        unsigned so = (B1_NS - 1) * (32 * F * 2), lo = (B1_NS - 1) * B1_STAGE + pw * 7168;
        auto issue = [&]() {
#pragma unroll
          for (int j = 0; j < 7; ++j) dma16(rs, voff[j], so, lds + lo + j * 1024);
          so += 32 * F * 2; lo = lo >= (B1_NS - 1) * B1_STAGE ? lo - (B1_NS - 1) * B1_STAGE : lo + B1_STAGE;
        };
        prod_run<7, B1_NS>(B1_NT - (B1_NS - 1), issue);
        prod_tail<7, B1_NS - 2>();
      } else {
        unsigned voff[2];
#pragma unroll
        for (int j = 0; j < 2; ++j) {
          const int k = j * 8 + (lane >> 4), pos = lane & 15;
          const int c = pos ^ (((k & 3) << 1) | (((k >> 3) & 1) << 3));
          voff[j] = (unsigned)((lane >> 4) * D + dd0 + c * 8) * 2u;
        }
        const rsrc_t rs = mk_rsrc(a.ws + WS_HL, S * D * 2);
        unsigned so = 0, lo = B1_ABYTES;
        auto issue = [&]() {
#pragma unroll
          for (int j = 0; j < 8; ++j) dma16c(rs, voff[(j >> 1) & 1], so + j * 4 * (D * 2), lds + lo + j * 1024);
          so += 32 * D * 2; lo = lo >= (B1_NS - 1) * B1_STAGE ? lo - (B1_NS - 1) * B1_STAGE : lo + B1_STAGE;
        };
        int done = B1_NS - 1;
        wait_flags<1>(sy + SY_LN + lane, lane < 2 * done, tag);
        PSTAMP(6, 16);
        prod_fill(B1_NS - 1, issue);
        wait_vm<(B1_NS - 2) * 8>();
        bar();
        int pre = flag_prefix<2>(sy + SY_LN + lane, 64, 2 * done + 2, tag, lane);
        issue();
        ++done;
#pragma unroll 1
        while (pre < 128 && done < B1_NT) {
          const int n = pre / 2 - done;
          prod_run<8, B1_NS>(n, issue);
          done += n;
          wait_vm<(B1_NS - 2) * 8>();
          bar();
          pre = flag_prefix<2>(sy + SY_LN + lane, 64, 2 * done + 2, tag, lane);
          issue();
          ++done;
        }
        prod_run<8, B1_NS>(B1_NT - done, issue);
        prod_tail<8, B1_NS - 2>();
      }
    }
    bar();
    const int nU = nt2 - nC;
    if (pw == 1 || pw == 2) {
      const rsrc_t rxx = mk_rsrc(a.x, S * D * 2);
      const int dk = d0 + (roleA ? 0 : 64);
#pragma unroll
      for (int k = 0; k < 8; ++k) dma16(rxx, (unsigned)((lane >> 3) * (D * 2) + ((lane & 7) ^ (lane >> 3)) * 16), (unsigned)((t0 + 64 * (pw - 1) + 8 * k) * D + dk) * 2u, lds + LDS_RES + (8 * (pw - 1) + k) * 1024);
      if (pw == 1) dma16c(rsy, (unsigned)lane * 16u, (unsigned)(SY_ST + t0) * 8u, lds + LDS_RES2);
      else { dma4(mk_rsrc(a.b2, S * 2), (unsigned)lane * 4u, (unsigned)t0 * 2u, lds + LDS_RES2 + 1024); dma4(mk_rsrc(a.Dsk, D * 2), (unsigned)lane * 4u, (unsigned)dk * 2u, lds + LDS_RES2 + 1280); }
      wait_vm<0>();
      if (lane == 0) __hip_atomic_fetch_add((LAS unsigned*)(size_t)(LDS_CNT + 12), 1u, __ATOMIC_RELAXED, __HIP_MEMORY_SCOPE_WORKGROUP);
    }
    if (pw == 0 || pw == 3) {
      const int dh = d0 + (((pw ? 1 : 0) ^ (roleA ? 0 : 1)) << 6), r4 = lane >> 4;
      const unsigned vce = (unsigned)(dh + (lane >> 3)) * (P * 2) + (unsigned)(((lane & 7) ^ (lane >> 4)) * 16), vco = vce ^ 64u;
      const unsigned vu = (unsigned)((dh + (lane >> 2)) * 64 + (((lane & 3) ^ (((r4 & 1) << 1) ^ ((r4 >> 1) * 3))) * 16));
      const rsrc_t ru = mk_rsrc(a.ws + WS_U, D * F * 2);
      rsrc_t rc = mk_rsrc(a.Cre, D * P * 2), rc2 = mk_rsrc(a.Cim, D * P * 2);
      unsigned so = 0, lo = pw ? 8192 : 0, sh = 0;
      auto issueC = [&]() {
        so = ((unsigned)cbs & 15u) * 128;
#pragma unroll
        for (int j = 0; j < 8; ++j) dma16(rc, (j & 1) ? vco : vce, so + j * 8 * (P * 2), lds + lo + j * 1024);
        const rsrc_t t_ = rc; rc = rc2; rc2 = t_;
        cbs >>= sh; sh ^= 4; lo = (lo + P2_STAGE) & (P2_NS * P2_STAGE - 1);
      };
      auto issueU = [&]() {
#pragma unroll
        for (int j = 0; j < 8; ++j) dma16c(ru, vu, so + (j & 1) * (D * 64) + (j >> 1) * 1024, lds + lo + j * 1024);
        so += 2 * D * 64; lo = (lo + P2_STAGE) & (P2_NS * P2_STAGE - 1);
      };
      prod_fill(P2_NS - 1, issueC);
      if (roleA) {
        const int bst = 1 + ((ri * PF_NST2) >> 7);
        prod_run<8, P2_NS>(bst, issueC);
        if (pw == 3) { touch(a.w2, F * S * 2, UA + WA + WB + RB * ri, RB); touch(a.w2, F * S * 2, WA + WB + RA * ri, RA); }
        prod_run<8, P2_NS>(nC - 3 - bst, issueC);
      } else {
        prod_run<8, P2_NS>(nC - 3, issueC);
      }
      so = ku0 * (2 * D * 64);
      wait_vm<16>();
      bar();
      wait_flags<1>(sy + SY_U + dt * 16 + (lane & 15), true, tag);
      PSTAMP(4, 19);
      issueU();
      prod_run<8, P2_NS>(nU - 1, issueU);
    } else {
      unsigned voff[2];
#pragma unroll
      for (int j = 0; j < 2; ++j) {
        const int k = j * 8 + (lane >> 4), pos = lane & 15;
        const int c = pos ^ (((k & 3) << 1) | (((k >> 3) & 1) << 3));
        voff[j] = (unsigned)(((pw - 1) * 32 + (lane >> 4)) * S + t0 + c * 8) * 2u;
      }
      const unsigned vh = (unsigned)(lane * 16 + (pw - 1) * 8192);
      const rsrc_t rh = mk_rsrc(a.ws + WS_HT, 2 * P * S * 2), rw = mk_rsrc(a.w2, F * S * 2);
      unsigned so = 0, lo = 16384 + (pw - 1) * 8192, sh = 0;
      gu64* hf = sy + SY_H + (tt >> 1) * 16 + ((unsigned)(cbs >> (4 * (lane & 15))) & 15u);
      auto issueH = [&]() {
        so = (tt * 32 + sh * 4 + ((unsigned)cbs & 15u)) * 16384;
#pragma unroll
        for (int j = 0; j < 8; ++j) dma16c(rh, vh, so + j * 1024, lds + lo + j * 1024);
        cbs >>= sh; sh ^= 4; lo = (lo + P2_STAGE) & (P2_NS * P2_STAGE - 1);
      };
      auto issueW = [&]() {
#pragma unroll
        for (int j = 0; j < 8; ++j) dma16(rw, voff[(j >> 1) & 1], so + j * 4 * (S * 2), lds + lo + j * 1024);
        so += 64 * S * 2; lo = (lo + P2_STAGE) & (P2_NS * P2_STAGE - 1);
      };
      if (roleA) lds_wait(LDS_CNT, 4u);
      int done = P2_NS - 1, pre = flag_prefix<1>(hf, nC / 2, 2, tag, lane);
      PSTAMP(6, 17);
      prod_fill(P2_NS - 1, issueH);
#pragma unroll 1
      while (pre < 128 && done < nC) {
        const int n = (2 * pre < nC ? 2 * pre : nC) - done;
        prod_run<8, P2_NS>(n, issueH);
        done += n;
        if (done < nC) {
          wait_vm<16>();
          bar();
          pre = flag_prefix<1>(hf, nC / 2, done / 2 + 1, tag, lane);
          issueH();
          ++done;
        }
      }
      prod_run<8, P2_NS>(nC - done, issueH);
      so = ku0 * (64 * S * 2);
      if (!roleA && pw == 1) wait_flags<1>(sy + SY_H + chunk * 16 + cb, true, tag);
      prod_run<8, P2_NS>(nU, issueW);
    }
    prod_tail<8, P2_NS - 2>();
    return;
  }

  const int wm = wid >> 1, wn = wid & 1;
  int cl = lane & 15, g = lane >> 4, q4 = cl >> 2, p4 = lane & 3, ln = lane;
#define RELANE() do { ln = __builtin_amdgcn_mbcnt_hi(~0u, __builtin_amdgcn_mbcnt_lo(~0u, 0u)); asm volatile("" : "+v"(ln)); cl = ln & 15; g = ln >> 4; q4 = cl >> 2; p4 = ln & 3; } while (0)
  const int kh = roleA ? 0 : 1, pa = bid ^ 1;
  const rsrc_t rxb = mk_rsrc(a.ws + WS_XB, 256 * 16384);
  f32x4 res[2][4];
  auto residual = [&]() {
    lds_wait(LDS_CNT + 12, 2u);
#pragma unroll
    for (int n = 0; n < 4; ++n) {
      const int tl = 64 * wn + 16 * n + cl;
      const unsigned ms = *(LAS unsigned*)(lds + LDS_RES2 + tl * 8);
      const float rs_ = __builtin_amdgcn_rcpf(bfhi(ms)), mb = bflo(ms), b2 = bf2f(*(LAS u16*)(lds + LDS_RES2 + 1024 + tl * 2));
#pragma unroll
      for (int mi = 0; mi < 2; ++mi) {
        const int c8 = 4 * wm + 2 * mi + (g >> 1);
        const u32x2 xv = *(LAS u32x2*)(lds + LDS_RES + tl * 128 + ((c8 ^ (tl & 7)) * 16) + (g & 1) * 8);
        const u32x2 dk = *(LAS u32x2*)(lds + LDS_RES2 + 1280 + c8 * 16 + (g & 1) * 8);
#pragma unroll
        for (int h = 0; h < 2; ++h) {
          const float x0 = bflo(xv[h]), x1 = bfhi(xv[h]);
          const unsigned xn = pk_bf16((x0 - mb) * rs_, (x1 - mb) * rs_);
          res[mi][n][2 * h] = fmaf(bflo(xn), bflo(dk[h]), x0 + b2); res[mi][n][2 * h + 1] = fmaf(bfhi(xn), bfhi(dk[h]), x1 + b2);
        }
        asm volatile("" : "+v"(res[mi][n]));
      }
    }
  };

  if (roleA) {
    LOAD_TAG(); HOLD_TAG();
    RELANE();
    const int p0 = cb * 64;
    const unsigned rb = (unsigned)(cl * 128 + ((g ^ (cl >> 1)) * 16));
    unsigned pa0 = rb + wm * 16384, pa1 = pa0 ^ 64u, pb0 = rb + 32768 + wn * 8192, pb1 = pb0 ^ 64u;
    f32x4 acc[8][4];
#pragma unroll
    for (int m = 0; m < 8; ++m)
#pragma unroll
      for (int n = 0; n < 4; ++n) acc[m][n] = (f32x4){0.f, 0.f, 0.f, 0.f};
    bf16x8 xa[8], xb[4], ya[8], yb[4];
    auto rd = [&](bf16x8* fa, bf16x8* fb, unsigned pa_, unsigned pb_, int r) {
      if (r == 0) fa[0] = lds_rd128(pa_); else if (r < 5) fb[r - 1] = lds_rd128(pb_ + (r - 1) * 2048); else fa[r - 4] = lds_rd128(pa_ + (r - 4) * 2048);
    };
    int slot = 0, dl = 0;
    STAMP(23);
    bar();
    STAMP(3);
#pragma unroll
    for (int r = 0; r < 12; ++r) rd(xa, xb, pa0, pb0, r);
#pragma unroll 1
    for (int t = 0; t < A1_NT; ++t) {
      SCHED_FENCE(); wait_lgkm0(); SCHED_FENCE();
      phase<32>(acc, xa, xb, [&](int i) {
        if (i % A1_RS == 0 && i / A1_RS < 12) rd(ya, yb, pa1, pb1, i / A1_RS);
        if (i == 25) { dl = (slot == A1_NS - 1) ? -(A1_NS - 1) * A1_STAGE : A1_STAGE; slot = (slot == A1_NS - 1) ? 0 : slot + 1; }
        if (i == 27) { pa0 += dl; pb0 += dl; asm volatile("" : "+v"(pa0), "+v"(pb0)); }
        if (i == 29) { pa1 += dl; pb1 += dl; asm volatile("" : "+v"(pa1), "+v"(pb1)); }
      });
      wait_lgkm0();
      bar();
      SCHED_FENCE();
      phase<32>(acc, ya, yb, [&](int i) { if (i % A1_RS == 0 && i / A1_RS < 12) rd(xa, xb, pa0, pb0, i / A1_RS); });
    }
    RELANE();
    STAMP(4);
    float cf[2][14], sb[4];
#pragma unroll
    for (int q = 0; q < 2; ++q) {
#pragma unroll
      for (int k = 0; k < 14; ++k) cf[q][k] = ((LAS float*)(lds + LDS_COEF))[k * 64 + 32 * wn + 16 * q + cl];
      sb[2 * q] = ((LAS float*)(lds + LDS_SB))[32 * wn + 16 * q + cl]; sb[2 * q + 1] = ((LAS float*)(lds + LDS_SB))[64 + 32 * wn + 16 * q + cl];
    }
    f32x2 er[2], ei[2];
#pragma unroll
    for (int q = 0; q < 2; ++q) { er[q] = bc2(0.f); ei[q] = bc2(0.f); }
#pragma unroll
    for (int m = 0; m < 8; ++m)
#pragma unroll
      for (int jj = 0; jj < 2; ++jj) {
        const f32x4 st = *(LAS f32x4*)(lds + LDS_STAT + (4 * wm + g) * 272 + (2 * m + jj) * 16);
        const f32x2 c2 = {st[0], st[1]}, r2 = {st[2], st[3]};
#pragma unroll
        for (int q = 0; q < 2; ++q) {
          const f32x2 ur = fma2(c2, bc2(sb[2 * q]), (f32x2){acc[m][2 * q][2 * jj], acc[m][2 * q][2 * jj + 1]}) * r2;
          const f32x2 ui = fma2(c2, bc2(sb[2 * q + 1]), (f32x2){acc[m][2 * q + 1][2 * jj], acc[m][2 * q + 1][2 * jj + 1]}) * r2;
          acc[m][2 * q][2 * jj] = ur.x; acc[m][2 * q][2 * jj + 1] = ur.y; acc[m][2 * q + 1][2 * jj] = ui.x; acc[m][2 * q + 1][2 * jj + 1] = ui.y;
          const f32x2 nr = fma2(bc2(cf[q][0]), er[q], fma2(bc2(-cf[q][1]), ei[q], ur));
          const f32x2 ni = fma2(bc2(cf[q][0]), ei[q], fma2(bc2(cf[q][1]), er[q], ui));
          er[q] = nr; ei[q] = ni;
        }
        if (jj == 1 && (m & 1)) asm volatile("" : "+v"(er[0]), "+v"(ei[0]), "+v"(er[1]), "+v"(ei[1]));
      }
    float sr[2], si[2], pr_[2], pi_[2];
    LAS float* tot = (LAS float*)(lds + LDS_TOT);
#pragma unroll
    for (int q = 0; q < 2; ++q) {
      float tr, ti;
      CMUL(tr, ti, cf[q][4], cf[q][5], er[q].x, ei[q].x);
      sr[q] = tr + er[q].y; si[q] = ti + ei[q].y;
      float vr = __shfl_up(sr[q], 16), vi = __shfl_up(si[q], 16);
      CMUL(tr, ti, cf[q][6], cf[q][7], vr, vi);
      if (g >= 1) { sr[q] += tr; si[q] += ti; }
      vr = __shfl_up(sr[q], 32); vi = __shfl_up(si[q], 32);
      CMUL(tr, ti, cf[q][8], cf[q][9], vr, vi);
      if (g >= 2) { sr[q] += tr; si[q] += ti; }
      vr = __shfl_up(sr[q], 16); vi = __shfl_up(si[q], 16);
      pr_[q] = g >= 1 ? vr : 0.f; pi_[q] = g >= 1 ? vi : 0.f;
      if (wm == 0 && g == 3) *(LAS f32x2*)(tot + (32 * wn + 16 * q + cl) * 2) = (f32x2){sr[q], si[q]};
    }
    if (wm == 0) {
      wait_lgkm0();
      if (ln == 0) __hip_atomic_store((LAS unsigned*)(size_t)(LDS_CNT + 4 + 4 * wn), 1u, __ATOMIC_RELAXED, __HIP_MEMORY_SCOPE_WORKGROUP);
    } else {
      lds_wait(LDS_CNT + 4 + 4 * wn, 1u);
    }
    f32x2 tq[2];
#pragma unroll
    for (int q = 0; q < 2; ++q) {
      tq[q] = *(LAS f32x2*)(tot + (32 * wn + 16 * q + cl) * 2);
      if (wm == 1 && g == 3 && chunk < 7) {
        float tr, ti;
        CMUL(tr, ti, cf[q][10], cf[q][11], tq[q].x, tq[q].y);
        st16_wt(rsy, (unsigned)(SY_GR * 8 + (chunk * P + p0 + 32 * wn + 16 * q + cl) * 16), 0, (u32x4){__float_as_uint(tr + sr[q]), 1u, __float_as_uint(ti + si[q]), 1u});
      }
    }
    STAMP(5);
#pragma unroll
    for (int m = 0; m < 8; ++m)
#pragma unroll
      for (int q = 0; q < 2; ++q) {
        const f32x4 ur = acc[m][2 * q], ui = acc[m][2 * q + 1];
        acc[m][2 * q] = ur * cf[q][2] - ui * cf[q][3];
        acc[m][2 * q + 1] = ui * (-cf[q][2]) - ur * cf[q][3];
        asm volatile("" : "+v"(acc[m][2 * q]), "+v"(acc[m][2 * q + 1]));
      }
    float hr[2] = {0.f, 0.f}, hi[2] = {0.f, 0.f};
    if (chunk > 0) {
      float gr[2][2], gi[2][2];
#pragma unroll 1
      for (unsigned spins = 0;; ++spins) {
        bool ok = true;
#pragma unroll
        for (int q = 0; q < 2; ++q)
#pragma unroll
          for (int k = 0; k < 2; ++k) {
            const int cc = (g + 4 * k < chunk) ? g + 4 * k : 0;
            const u32x4 v = __builtin_amdgcn_raw_buffer_load_b128(rsy, (unsigned)(SY_GR * 8 + (cc * P + p0 + 32 * wn + 16 * q + cl) * 16), 0, 16);
            ok &= (v[1] == 1u) & (v[3] == 1u);
            gr[q][k] = __uint_as_float(v[0]); gi[q][k] = __uint_as_float(v[2]);
          }
        if (__all(ok) || spins > SPIN_CAP) break;
        __builtin_amdgcn_s_sleep(1);
        asm volatile("" ::: "memory");
      }
#pragma unroll
      for (int q = 0; q < 2; ++q) {
        float ar_ = 0.f, ai_ = 0.f;
#pragma unroll
        for (int k = 0; k < 2; ++k) {
          float vr = gr[q][k], vi = gi[q][k];
          const int e = chunk - 1 - (g + 4 * k);
#pragma unroll
          for (int i = 0; i < 6 - 4 * k; ++i) {
            float tr, ti;
            CMUL(tr, ti, cf[q][12], cf[q][13], vr, vi);
            vr = i < e ? tr : vr; vi = i < e ? ti : vi;
          }
          ar_ += e >= 0 ? vr : 0.f; ai_ += e >= 0 ? vi : 0.f;
        }
        ar_ += __shfl_xor(ar_, 16); ai_ += __shfl_xor(ai_, 16);
        ar_ += __shfl_xor(ar_, 32); ai_ += __shfl_xor(ai_, 32);
        hr[q] = ar_; hi[q] = ai_;
      }
    }
    STAMP(6);
    f32x2 wr[2], wi[2];
#pragma unroll
    for (int q = 0; q < 2; ++q) {
      float tr, ti;
      if (wm == 1) { CMUL(tr, ti, cf[q][10], cf[q][11], hr[q], hi[q]); hr[q] = tr + tq[q].x; hi[q] = ti + tq[q].y; }
      CMUL(tr, ti, cf[q][6], cf[q][7], hr[q], hi[q]);
      if (g & 1) { hr[q] = tr; hi[q] = ti; }
      CMUL(tr, ti, cf[q][8], cf[q][9], hr[q], hi[q]);
      if (g & 2) { hr[q] = tr; hi[q] = ti; }
      hr[q] += pr_[q]; hi[q] += pi_[q];
      CMUL(tr, ti, cf[q][4], cf[q][5], hr[q], hi[q]);
      const f32x2 h0r = {hr[q], tr + er[q].x}, h0i = {hi[q], ti + ei[q].x};
      wr[q] = h0r * cf[q][2] - h0i * cf[q][3];
      wi[q] = h0i * (-cf[q][2]) - h0r * cf[q][3];
    }
    LAS char* scr = lds + wid * P2_STAGE + 16384;
    const unsigned fsw = (unsigned)(((cl & 3) << 1) | (((cl >> 3) & 1) << 3));
    const rsrc_t rh = mk_rsrc(a.ws + WS_HT, 2 * P * S * 2);
#pragma unroll
    for (int q = 0; q < 2; ++q) {
      const f32x2 ar2 = bc2(cf[q][0]), ai2 = bc2(cf[q][1]);
#pragma unroll
      for (int mh = 0; mh < 2; ++mh) {
        u32x4 o[2][2];
#pragma unroll
        for (int mq = 0; mq < 4; ++mq) {
          const int m = 4 * mh + mq;
          const f32x2 u0r = {acc[m][2 * q][0], acc[m][2 * q][1]}, u0i = {acc[m][2 * q + 1][0], acc[m][2 * q + 1][1]};
          const f32x2 u1r = {acc[m][2 * q][2], acc[m][2 * q][3]}, u1i = {acc[m][2 * q + 1][2], acc[m][2 * q + 1][3]};
          const f32x2 n0r = fma2(ar2, wr[q], fma2(ai2, wi[q], u0r)), n0i = fma2(ar2, wi[q], fma2(-ai2, wr[q], u0i));
          const f32x2 n1r = fma2(ar2, n0r, fma2(ai2, n0i, u1r)), n1i = fma2(ar2, n0i, fma2(-ai2, n0r, u1i));
          wr[q] = n1r; wi[q] = n1i;
          o[0][0][mq] = pk_bf16(n0r.x, n1r.x); o[0][1][mq] = pk_bf16(n0r.y, n1r.y);
          o[1][0][mq] = pk_bf16(n0i.x, n1i.x); o[1][1][mq] = pk_bf16(n0i.y, n1i.y);
        }
#pragma unroll
        for (int part = 0; part < 2; ++part)
#pragma unroll
          for (int run = 0; run < 2; ++run)
            *(LAS u32x4*)(scr + (part * 32 + 16 * q + cl) * 256 + (((unsigned)(4 * g + 2 * run + mh) ^ fsw) * 16)) = o[part][run];
      }
#pragma unroll
      for (int i = 0; i < 8; ++i) {
        const int part = i >> 2, pc = 4 * q + (i & 3);
        const u32x4 v = *(LAS u32x4*)(scr + part * 8192 + pc * 1024 + ln * 16);
        st16_wt(rh, (unsigned)(ln * 16 + pc * 1024), (unsigned)(((2 * chunk + wm) * 32 + part * 16 + cb) * 16384 + wn * 8192), v);
      }
      SCHED_FENCE();
    }
    STAMP(7);
    publish(LDS_CNT, sy + SY_H + chunk * 16 + cb, tag, ln);
    STAMP(8);
    residual();
  } else {
    RELANE();
    const int f0 = ft * 224, dd0 = dt1 * 128;
    const int k0 = 8 * g + q4;
    unsigned pa0 = (unsigned)(k0 * 448 + ((12 * wm + p4) ^ (3 * (g & 1))) * 16 + (p4 & 1) * 8);
    unsigned pa1 = (unsigned)(k0 * 448 + ((12 * wm + p4) ^ (3 * (g & 1))) * 16 + ((p4 & 1) ^ 1) * 8);
    unsigned pa2 = (unsigned)(k0 * 448 + ((24 + 2 * wm + (p4 >> 1)) ^ (3 * (g & 1))) * 16 + (p4 & 1) * 8);
    unsigned pb[4];
#pragma unroll
    for (int n = 0; n < 4; ++n) pb[n] = (unsigned)(B1_ABYTES + k0 * 256 + ((8 * wn + 2 * n + (p4 >> 1)) ^ ((q4 << 1) | ((g & 1) << 3))) * 16 + (p4 & 1) * 8);
    f32x4 acc[7][4];
#pragma unroll
    for (int m = 0; m < 7; ++m) {
      const u32x2 bv = *(const u32x2*)(a.b1 + f0 + (m < 6 ? 96 * wm + 32 * (m >> 1) + 8 * g + 4 * ((m & 1) ^ (g & 1)) : 192 + 16 * wm + 4 * g));
#pragma unroll
      for (int n = 0; n < 4; ++n) acc[m][n] = (f32x4){bflo(bv[0]), bfhi(bv[0]), bflo(bv[1]), bfhi(bv[1])};
    }
    TrFrag xa[7], xb[4], ya[7], yb[4];
    int slot = 0, dl = 0;
    auto side = [&](TrFrag* fa, TrFrag* fb, int i) {
      const int f = i >> 1, h = i & 1;
      if (f == 0) fa[0].h[h] = lds_rdtr(pa0 + h * 1792);
      else if (f < 5) fb[f - 1].h[h] = lds_rdtr(pb[f - 1] + h * 1024);
      else if (f < 10) fa[f - 4].h[h] = lds_rdtr((((f - 4) & 1) ? pa1 : pa0) + ((f - 4) >> 1) * 64 + h * 1792);
      else if (f == 10) fa[6].h[h] = lds_rdtr(pa2 + h * 1792);
      else if (i == 22) { dl = (slot == B1_NS - 1) ? -(B1_NS - 1) * B1_STAGE : B1_STAGE; slot = (slot == B1_NS - 1) ? 0 : slot + 1; }
      else if (i == 23) { pa0 += dl; pb[0] += dl; asm volatile("" : "+v"(pa0), "+v"(pb[0])); }
      else if (i == 24) { pb[1] += dl; pb[2] += dl; asm volatile("" : "+v"(pb[1]), "+v"(pb[2])); }
      else if (i == 25) { pb[3] += dl; pa1 += dl; asm volatile("" : "+v"(pb[3]), "+v"(pa1)); }
      else if (i == 26) { pa2 += dl; asm volatile("" : "+v"(pa2)); }
    };
    STAMP(15);
    bar();
    STAMP(3);
#pragma unroll
    for (int i = 0; i < 28; ++i) side(xa, xb, i);
#pragma unroll 1
    for (int t = 0; t < B1_NT; t += 2) {
      SCHED_FENCE(); wait_lgkm0(); bar(); SCHED_FENCE();
      phase<28>(acc, xa, xb, [&](int i) { side(ya, yb, i); });
      wait_lgkm0(); bar(); SCHED_FENCE();
      phase<28>(acc, ya, yb, [&](int i) { side(xa, xb, i); });
    }
    RELANE();
    STAMP(4);
    const rsrc_t ru = mk_rsrc(a.ws + WS_U, D * F * 2);
    auto act4 = [&](const f32x4& v) {
      const f32x2 v0 = gelu_tanh2((f32x2){v[0], v[1]}), v1 = gelu_tanh2((f32x2){v[2], v[3]});
      return (u32x2){pk_bf16(v0.x, v0.y), pk_bf16(v1.x, v1.y)};
    };
#pragma unroll
    for (int n = 0; n < 4; ++n) *(LAS u32x2*)(lds + LDS_U6 + (64 * wn + 16 * n + cl) * 64 + 32 * wm + 8 * g) = act4(acc[6][n]);
    wait_lgkm0();
    if (ln == 0) __hip_atomic_fetch_add((LAS unsigned*)(size_t)(LDS_CNT + 4 + 4 * wn), 1u, __ATOMIC_RELAXED, __HIP_MEMORY_SCOPE_WORKGROUP);
#pragma unroll
    for (int n = 0; n < 4; ++n) {
      const unsigned vo = (unsigned)((dd0 + 64 * wn + 16 * n + cl) * 64);
#pragma unroll
      for (int mm = 0; mm < 3; ++mm) {
        const u32x2 w0 = act4(acc[2 * mm][n]), w1 = act4(acc[2 * mm + 1][n]);
        const bool sw = g & 1;
        const u32x2 lo = sw ? w1 : w0, hi = sw ? w0 : w1;
        st16_wt(ru, vo + 16 * g, (unsigned)((ft * 7 + 3 * wm + mm) * D * 64), (u32x4){lo[0], lo[1], hi[0], hi[1]});
      }
      if (n == 1) {
        lds_wait(LDS_CNT + 4 + 4 * wn, 2u);
#pragma unroll
        for (int k = 0; k < 2; ++k) {
          const unsigned o = (unsigned)(wn * 4096 + wm * 2048 + k * 1024 + ln * 16);
          st16_wt(ru, o, (unsigned)(((ft * 7 + 6) * D + dd0) * 64), *(LAS u32x4*)(lds + LDS_U6 + o));
        }
      }
    }
    STAMP(7);
    residual();
    publish(LDS_CNT, sy + SY_U + dt1 * 16 + ft, tag, ln);
    STAMP(8);
  }

  f32x4 acc[4][4];
#pragma unroll
  for (int n = 0; n < 4; ++n) { acc[0][n] = res[0][n]; acc[1][n] = res[1][n]; acc[2][n] = (f32x4){0.f, 0.f, 0.f, 0.f}; acc[3][n] = (f32x4){0.f, 0.f, 0.f, 0.f}; }
  const unsigned au0 = (unsigned)(wm * 4096 + cl * 64 + ((g ^ (((q4 & 1) << 1) ^ ((q4 >> 1) * 3))) * 16));
  unsigned pa0 = (unsigned)((wm * 32 + cl) * 128 + ((g ^ (cl >> 1)) * 16)), pa1 = pa0 ^ 64u;
  const unsigned sw0 = pa0 ^ au0 ^ 65536u, sw1 = pa1 ^ (au0 + 1024u) ^ 65536u;
  unsigned pb[4];
#pragma unroll
  for (int n = 0; n < 4; ++n) pb[n] = (unsigned)(16384 + (8 * g + q4) * 256 + ((8 * wn + 2 * n + (p4 >> 1)) ^ ((q4 << 1) | ((g & 1) << 3))) * 16 + (p4 & 1) * 8);
  bf16x8 xa[4], ya[4];
  TrFrag xb[4], yb[4];
  static_assert(P2_NS == 4 && NAB % 2 == 0 && NBA % 2 == 0 && NAB >= P2_NS && NAB <= 16 && PF_NST2 < 29 - NAB, "phase-2 loop: two stages per trip, 64-KB windows");
  auto rd = [&](bf16x8* fa, TrFrag* fb, int so, int ks, int r) {
    const unsigned pa_ = (ks ? pa1 : pa0) + so;
    if (r == 0) fa[0] = lds_rd128(pa_);
    else if (r < 9) fb[(r - 1) >> 1].h[(r - 1) & 1] = lds_rdtr(pb[(r - 1) >> 1] + so + ks * 8192 + ((r - 1) & 1) * 1024);
    else if (r < 12) fa[r - 8] = lds_rd128(pa_ + ((r - 8) >> 1) * 8192 + ((r - 8) & 1) * 2048);
  };
  STAMP(18);
  bar();
  STAMP(9);
#pragma unroll
  for (int r = 0; r < 12; ++r) rd(xa, xb, 0, 0, r);
#pragma unroll 1
  for (int t = 0; t < nt2; t += 2) {
    unsigned x0, x1;
    SCHED_FENCE(); wait_lgkm0(); SCHED_FENCE();
    phase<16>(acc, xa, xb, [&](int i) { rd(ya, yb, 0, 1, i); });
    wait_lgkm0(); bar(); SCHED_FENCE();
    phase<16>(acc, ya, yb, [&](int i) {
      rd(xa, xb, P2_STAGE, 0, i);
      if (i == 13) { const bool sw = (t + 2 == nC); x0 = sw ? sw0 : 65536u; x1 = sw ? sw1 : 65536u; asm volatile("" : "+v"(x0), "+v"(x1)); }
      if (i == 14) { pa0 ^= x0; asm volatile("" : "+v"(pa0)); }
    });
    wait_lgkm0(); SCHED_FENCE();
    phase<16>(acc, xa, xb, [&](int i) {
      rd(ya, yb, P2_STAGE, 1, i);
      if (i == 12) { pb[0] ^= 65536u; pb[1] ^= 65536u; asm volatile("" : "+v"(pb[0]), "+v"(pb[1])); }
      if (i == 13) { pb[2] ^= 65536u; pb[3] ^= 65536u; asm volatile("" : "+v"(pb[2]), "+v"(pb[3])); }
      if (i == 14) { pa1 ^= x1; asm volatile("" : "+v"(pa1)); }
    });
    wait_lgkm0(); bar(); SCHED_FENCE();
    phase<16>(acc, ya, yb, [&](int i) { rd(xa, xb, 0, 0, i); });
  }
  STAMP(10);
  const unsigned xo = (unsigned)(wid * 4096 + ln * 16);
#pragma unroll
  for (int n = 0; n < 4; ++n) {
    u32x4 w = {pk_bf16(acc[2][n][0], acc[2][n][1]), pk_bf16(acc[2][n][2], acc[2][n][3]), pk_bf16(acc[3][n][0], acc[3][n][1]), pk_bf16(acc[3][n][2], acc[3][n][3])};
#pragma unroll
    for (int k = 0; k < 4; ++k) w[k] = w[k] ? w[k] : 0x80000000u;
    st16_wt(rxb, xo + n * 1024, bid * 16384, w);
  }
  STAMP(11);
  u32x4 rv[4];
#pragma unroll 1
  for (unsigned spins = 0; spins < SPIN_CAP; ++spins) {
    bool ok = true;
#pragma unroll
    for (int n = 0; n < 4; ++n) rv[n] = __builtin_amdgcn_raw_buffer_load_b128(rxb, xo + n * 1024 + pa * 16384, 0, 16);
#pragma unroll
    for (int n = 0; n < 4; ++n) ok &= (rv[n][0] != 0u) & (rv[n][1] != 0u) & (rv[n][2] != 0u) & (rv[n][3] != 0u);
    if (__all(ok)) break;
#pragma unroll 1
    for (; spins < SPIN_CAP; ++spins) {
      __builtin_amdgcn_s_sleep(XB_SLEEP);
      asm volatile("" ::: "memory");
      if (__builtin_amdgcn_readfirstlane(__builtin_amdgcn_raw_buffer_load_b32(rxb, wid * 4096 + 4092 + pa * 16384, 0, 16)) != 0u) break;
    }
    asm volatile("" ::: "memory");
  }
  STAMP(12);
#pragma unroll
  for (int n = 0; n < 4; ++n) {
    acc[0][n] += (f32x4){bflo(rv[n][0]), bfhi(rv[n][0]), bflo(rv[n][1]), bfhi(rv[n][1])};
    acc[1][n] += (f32x4){bflo(rv[n][2]), bfhi(rv[n][2]), bflo(rv[n][3]), bfhi(rv[n][3])};
  }
  {
    LAS char* tb = lds + wid * 8192;
#pragma unroll
    for (int n = 0; n < 4; ++n)
#pragma unroll
      for (int mi = 0; mi < 2; ++mi) *(LAS f32x4*)(tb + (16 * n + cl) * 128 + (((4 * mi + g) ^ (cl & 7)) * 16)) = acc[mi][n];
    const rsrc_t rout = mk_rsrc(a.out, S * D * 4);
#pragma unroll
    for (int k = 0; k < 8; ++k) {
      const u32x4 v = *(LAS u32x4*)(tb + k * 1024 + ln * 16);
      __builtin_amdgcn_raw_buffer_store_b128(v, rout, (unsigned)((t0 + 64 * wn + 8 * k + (ln >> 3)) * D + d0 + 64 * kh + 32 * wm) * 4u + (unsigned)(((ln & 7) ^ (ln >> 3)) * 16), 0, 16);
    }
  }
  STAMP(13);
  if ((bid & 1) == PROBE_ROLE && wid == 0) { const u64 te = __builtin_amdgcn_s_memrealtime(); while (__builtin_amdgcn_s_memrealtime() - te < probe_d) __builtin_amdgcn_s_sleep(2); }
}

extern "C" void kernel_launch(void* const* d_in, const int* in_sizes, int n_in, void* d_out, int out_size, void* d_ws, size_t ws_size,
                              hipStream_t stream) {
  (void)hipFuncSetAttribute((const void*)fused, hipFuncAttributeMaxDynamicSharedMemorySize, LDS_BYTES);
  Args a;
  a.x = (const u16*)d_in[0]; a.Lre = (const u16*)d_in[1]; a.Lim = (const u16*)d_in[2]; a.Bre = (const u16*)d_in[3]; a.Bim = (const u16*)d_in[4];
  a.Cre = (const u16*)d_in[5]; a.Cim = (const u16*)d_in[6]; a.logdt = (const u16*)d_in[7]; a.Dsk = (const u16*)d_in[8];
  a.w1 = (const u16*)d_in[10]; a.b1 = (const u16*)d_in[11]; a.w2 = (const u16*)d_in[12]; a.b2 = (const u16*)d_in[13];
  a.out = (float*)d_out; a.ws = (char*)d_ws;
  fused<<<256, 512, LDS_BYTES, stream>>>(a);
}
```

```cpp
#include <hip/hip_runtime.h>
#include <stdint.h>

typedef unsigned short u16;
typedef unsigned long long u64;
typedef __bf16 bf16x8 __attribute__((ext_vector_type(8)));
typedef short s16x4 __attribute__((ext_vector_type(4)));
typedef short s16x8 __attribute__((ext_vector_type(8)));
typedef float f32x2 __attribute__((ext_vector_type(2)));
typedef float f32x4 __attribute__((ext_vector_type(4)));
typedef unsigned u32x4 __attribute__((ext_vector_type(4)));
typedef unsigned u32x2 __attribute__((ext_vector_type(2)));
#define LAS __attribute__((address_space(3)))
#define GAS __attribute__((address_space(1)))
typedef GAS unsigned long long gu64;
typedef __amdgpu_buffer_rsrc_t rsrc_t;
#define RLX_AGENT __ATOMIC_RELAXED, __HIP_MEMORY_SCOPE_AGENT

constexpr int S = 2048, D = 1024, P = 1024, F = 3584;
constexpr size_t MB = 1024 * 1024;
constexpr size_t WS_HL = 4 * MB, WS_HT = 8 * MB, WS_U = 16 * MB, WS_XB = 24 * MB, WS_SYNC = 32 * MB;
constexpr int SY_LN = 64, SY_H = 320, SY_U = 448, SY_ST = 1024, SY_SB = 3072, SY_GR = 5120;
constexpr unsigned SYNC_BYTES = (SY_GR + 8 * P * 2) * 8;
constexpr unsigned SPIN_CAP = 1u << 17;
#define NBA 22
#define NAB 6
constexpr unsigned CODE_PF_BYTES = 40 * 1024;
constexpr unsigned long long CB_ORDER = 0x80F7E6D5C491B3A2ull;
#define OUT_LDS 1
#define XB_SLEEP 2
#define PF_NST 11
#define PF_ST0 0
#define PF_DELAY 24
#define PF_N0 2
#define PF_NST2 20
#define PF_WA 0
#define PF_BT 5
#define PF_WB 4
#define A1_RS 2
#define PSTAMP(w, i)
#define STAMP_INIT()
#define STAMP_DUMP()
#define TRACE(e)
#define TRACE_DUMP()
#define STAMP(i) PSTAMP(0, i)

__device__ __forceinline__ float bf2f(u16 v) { return __uint_as_float(((unsigned)v) << 16); }
__device__ __forceinline__ float bflo(unsigned v) { return __uint_as_float(v << 16); }
__device__ __forceinline__ float bfhi(unsigned v) { return __uint_as_float(v & 0xffff0000u); }
__device__ __forceinline__ unsigned pk_bf16(float lo, float hi) {
  typedef __bf16 bf16x2 __attribute__((ext_vector_type(2)));
  f32x2 v = {lo, hi};
  bf16x2 r = __builtin_convertvector(v, bf16x2);
  return __builtin_bit_cast(unsigned, r);
}
__device__ __forceinline__ float rbf(float f) { return bflo(pk_bf16(f, f)); }
__device__ __forceinline__ float dot2(unsigned a, unsigned b, float c) {
  typedef __bf16 bf16x2 __attribute__((ext_vector_type(2)));
  return __builtin_amdgcn_fdot2_f32_bf16(__builtin_bit_cast(bf16x2, a), __builtin_bit_cast(bf16x2, b), c, false);
}
__device__ __forceinline__ f32x2 fma2(f32x2 a, f32x2 b, f32x2 c) { return __builtin_elementwise_fma(a, b, c); }
__device__ __forceinline__ f32x2 bc2(float v) { return (f32x2){v, v}; }

__device__ __forceinline__ rsrc_t mk_rsrc(const void* p, unsigned bytes) { return __builtin_amdgcn_make_buffer_rsrc((void*)p, 0, bytes, 0x00020000); }
__device__ __forceinline__ void dma16(rsrc_t rs, unsigned voff, unsigned soff, LAS char* l) { __builtin_amdgcn_raw_ptr_buffer_load_lds(rs, (LAS void*)l, 16, voff, soff, 0, 0); }
__device__ __forceinline__ void dma16c(rsrc_t rs, unsigned voff, unsigned soff, LAS char* l) { __builtin_amdgcn_raw_ptr_buffer_load_lds(rs, (LAS void*)l, 16, voff, soff, 0, 16); }
__device__ __forceinline__ void dma4(rsrc_t rs, unsigned voff, unsigned soff, LAS char* l) { __builtin_amdgcn_raw_ptr_buffer_load_lds(rs, (LAS void*)l, 4, voff, soff, 0, 0); }
__device__ __forceinline__ void st16_wt(rsrc_t rs, unsigned voff, unsigned soff, u32x4 v) { __builtin_amdgcn_raw_buffer_store_b128(v, rs, voff + soff, 0, 16); }
__device__ __forceinline__ void st8_wt(rsrc_t rs, unsigned voff, unsigned soff, u32x2 v) { __builtin_amdgcn_raw_buffer_store_b64(v, rs, voff + soff, 0, 16); }
template <int N> __device__ __forceinline__ void wait_vm() { asm volatile("s_waitcnt vmcnt(%0)" ::"n"(N) : "memory"); }
__device__ __forceinline__ void wait_lgkm0() { asm volatile("" ::: "memory"); __builtin_amdgcn_s_waitcnt(0xC07F); asm volatile("" ::: "memory"); }
__device__ __forceinline__ void bar() {
  asm volatile("" ::: "memory");
  __builtin_amdgcn_s_barrier();
  asm volatile("" ::: "memory");
}
#define SCHED_FENCE() __builtin_amdgcn_sched_barrier(0)
__device__ __forceinline__ bf16x8 lds_rd128(unsigned p) { return *(LAS bf16x8*)(size_t)p; }
__device__ __forceinline__ s16x4 lds_rdtr(unsigned p) { return __builtin_amdgcn_ds_read_tr16_b64_v4i16((LAS s16x4*)(size_t)p); }
struct TrFrag { s16x4 h[2]; };
__device__ __forceinline__ bf16x8 frag(const bf16x8& f) { return f; }
__device__ __forceinline__ bf16x8 frag(const TrFrag& f) { s16x8 ab = __builtin_shufflevector(f.h[0], f.h[1], 0, 1, 2, 3, 4, 5, 6, 7); return __builtin_bit_cast(bf16x8, ab); }
#define MFMA(a, b, c) __builtin_amdgcn_mfma_f32_16x16x32_bf16(frag(a), frag(b), (c), 0, 0, 0)
template <int NM, class FA, class FB, class Side>
__device__ __forceinline__ void phase(f32x4 (*acc)[4], const FA* fa, const FB* fb, Side side) {
#pragma unroll
  for (int i = 0; i < NM; ++i) {
    acc[i >> 2][i & 3] = MFMA(fa[i >> 2], fb[i & 3], acc[i >> 2][i & 3]);
    side(i);
    SCHED_FENCE();
  }
}

template <int NL> __device__ __forceinline__ void wait_flags(gu64* p, bool active, u64 tag) {
#pragma unroll 1
  for (unsigned spins = 0; spins < SPIN_CAP; ++spins) {
    bool ok = true;
    if (active) {
#pragma unroll
      for (int k = 0; k < NL; ++k) ok &= __hip_atomic_load(p + 64 * k, RLX_AGENT) == tag;
    }
    if (__all(ok)) break;
    __builtin_amdgcn_s_sleep(2);
  }
  __builtin_amdgcn_fence(__ATOMIC_ACQUIRE, "wavefront");
}

template <int NL> __device__ __forceinline__ int flag_prefix(gu64* p, int n, int need, u64 tag, int lane) {
  int pre = 0;
#pragma unroll 1
  for (unsigned spins = 0; spins < SPIN_CAP; ++spins) {
    const u64 m0 = ~__ballot(lane >= n || __hip_atomic_load(p, RLX_AGENT) == tag);
    const u64 m1 = NL == 2 ? ~__ballot(__hip_atomic_load(p + 64, RLX_AGENT) == tag) : 0ull;
    pre = m0 ? __builtin_ctzll(m0) : m1 ? 64 + __builtin_ctzll(m1) : 128;
    if (pre >= need) break;
    __builtin_amdgcn_s_sleep(1);
  }
  __builtin_amdgcn_fence(__ATOMIC_ACQUIRE, "wavefront");
  return pre < need ? 128 : pre;
}
__device__ __forceinline__ void lds_wait(unsigned addr, unsigned want) {
#pragma unroll 1
  for (unsigned spins = 0; spins < (SPIN_CAP << 4); ++spins) {
    if (__builtin_amdgcn_readfirstlane(__hip_atomic_load((LAS unsigned*)(size_t)addr, __ATOMIC_RELAXED, __HIP_MEMORY_SCOPE_WORKGROUP)) >= want) break;
    __builtin_amdgcn_s_sleep(1);
  }
  asm volatile("" ::: "memory");
}
__device__ __forceinline__ void publish(unsigned cnt, gu64* flag, u64 tag, int lane) {
  wait_vm<0>();
  wait_lgkm0();
  if (lane == 0 && __hip_atomic_fetch_add((LAS unsigned*)(size_t)cnt, 1u, __ATOMIC_RELAXED, __HIP_MEMORY_SCOPE_WORKGROUP) == 3u) __hip_atomic_store(flag, tag, RLX_AGENT);
}

template <class Issue> __device__ __forceinline__ void prod_fill(int n, Issue issue) {
#pragma unroll 1
  for (int t = 0; t < n; ++t) issue();
}
template <int L, int NS, class Issue> __device__ __forceinline__ void prod_run(int n, Issue issue) {
#pragma unroll 1
  for (int t = 0; t < n; ++t) { wait_vm<(NS - 2) * L>(); bar(); issue(); }
}
template <int L, int K, int X = 0> __device__ __forceinline__ void prod_tail() {
  wait_vm<K * L + X>();
  bar();
  if constexpr (K > 0) prod_tail<L, K - 1, X>();
}

__device__ __forceinline__ float wave_sum(float v) {
  v += __builtin_bit_cast(float, __builtin_amdgcn_update_dpp(0, __builtin_bit_cast(int, v), 0xB1, 0xf, 0xf, true));
  v += __builtin_bit_cast(float, __builtin_amdgcn_update_dpp(0, __builtin_bit_cast(int, v), 0x4E, 0xf, 0xf, true));
  v += __builtin_bit_cast(float, __builtin_amdgcn_update_dpp(0, __builtin_bit_cast(int, v), 0x141, 0xf, 0xf, true));
  v += __builtin_bit_cast(float, __builtin_amdgcn_update_dpp(0, __builtin_bit_cast(int, v), 0x140, 0xf, 0xf, true));
  const int iv = __builtin_bit_cast(int, v);
  const float a = __builtin_bit_cast(float, __builtin_amdgcn_readlane(iv, 0)), b = __builtin_bit_cast(float, __builtin_amdgcn_readlane(iv, 16));
  const float c = __builtin_bit_cast(float, __builtin_amdgcn_readlane(iv, 32)), d = __builtin_bit_cast(float, __builtin_amdgcn_readlane(iv, 48));
  return (a + b) + (c + d);
}

struct Args {
  const u16 *x, *Lre, *Lim, *Bre, *Bim, *Cre, *Cim, *logdt, *Dsk, *w1, *b1, *w2, *b2;
  float* out;
  char* ws;
};

constexpr int A1_NS = 3, A1_STAGE = 49152, A1_NT = D / 64;
constexpr int B1_NS = 7, B1_ABYTES = 32 * 448, B1_STAGE = B1_ABYTES + 32 * 256, B1_NT = S / 32;
constexpr int P2_NS = 4, P2_STAGE = 32768;
constexpr int LDS_BYTES = 160 * 1024;
constexpr int LDS_COEF = A1_NS * A1_STAGE;
constexpr int LDS_TOT = LDS_COEF + 14 * 256;
constexpr int LDS_SB = LDS_TOT + 512;
constexpr int LDS_STAT = LDS_SB + 512;
constexpr int LDS_RAW = LDS_STAT + 8 * 272;
constexpr int LDS_RES = P2_NS * P2_STAGE;
constexpr int LDS_U6 = LDS_COEF;
constexpr int LDS_RES2 = 158208;
constexpr int LDS_CNT = LDS_BYTES - 256 - 2048 - 64;
constexpr int LDS_FLG = LDS_BYTES - 256 - 2048;
constexpr int LDS_DUMMY = LDS_BYTES - 256;
static_assert(LDS_RAW + 3072 + 768 <= LDS_CNT && B1_NS * B1_STAGE <= LDS_CNT && LDS_RES + 16384 <= LDS_COEF && LDS_RAW + 3840 <= LDS_RES2 && LDS_RES2 + 1536 <= LDS_CNT && P2_NS * P2_STAGE <= LDS_COEF && LDS_RAW % 16 == 0, "lds");

constexpr int UA = NBA * 32, WA = PF_WA * 128 / 2, WB = PF_WB * 128 / 2;
constexpr int RA = (UA - WA - WB + 127) / 128, RB = (1792 - UA - WA - WB + 127) / 128;

__device__ __forceinline__ f32x2 gelu_tanh2(f32x2 x) {
  const f32x2 z = x * fma2(x * x, bc2(-0.10294323967f), bc2(-2.30220819813f));
  const f32x2 d = (f32x2){__builtin_amdgcn_exp2f(z.x), __builtin_amdgcn_exp2f(z.y)} + bc2(1.0f);
  return x * (f32x2){__builtin_amdgcn_rcpf(d.x), __builtin_amdgcn_rcpf(d.y)};
}
#define CMUL(or_, oi_, ar_, ai_, br_, bi_) do { const float t_ = (ar_) * (br_) - (ai_) * (bi_); oi_ = (ar_) * (bi_) + (ai_) * (br_); or_ = t_; } while (0)

__global__ __launch_bounds__(512, 2) void fused(Args a) {
  extern __shared__ __attribute__((aligned(16))) char smem[];
  LAS char* lds = (LAS char*)smem;
  u64 pc0;
  asm volatile("s_getpc_b64 %0" : "=s"(pc0));
  const int wid = __builtin_amdgcn_readfirstlane(threadIdx.x >> 6), lane = threadIdx.x & 63;
  const int bid = blockIdx.x, xc = bid & 7, idx = bid >> 3, xg = xc >> 1;
  const bool roleA = !(xc & 1);
  const int ri = bid >> 1;
  gu64* sy = (gu64*)(a.ws + WS_SYNC);
  u64 tag = 0;
#define LOAD_TAG() do { const __attribute__((address_space(4))) u64* ep_ = (const __attribute__((address_space(4))) u64*)(a.ws + WS_SYNC); \
    asm volatile("" : "+s"(ep_)::"memory"); tag = *ep_ + 1ull; } while (0)
#define HOLD_TAG() asm volatile("" ::"s"(tag))
#define tag32 ((unsigned)tag)
  const int chunk = 4 * (xg & 1) + (idx & 3), cb = 8 * (xg >> 1) + (idx >> 2);
  const int ft = 4 * xg + (idx & 3), dt1 = idx >> 2;
  const int tt = 4 * xg + (idx & 3), dt = idx >> 2;
  const int t0 = tt * 128, d0 = dt * 128;
  u64 cbs = roleA ? CB_ORDER : CB_ORDER >> (2 * (32 - NAB));
  const int nC = roleA ? 32 - NAB : NAB, ku0 = roleA ? 0 : NBA, nt2 = nC + (roleA ? NBA : 56 - NBA);
  const rsrc_t rsy = mk_rsrc(a.ws + WS_SYNC, SYNC_BYTES);
  STAMP_INIT();
  STAMP(0);

  auto touch = [&](const u16* base, unsigned bytes, int u0, int n) {
    const rsrc_t rs = mk_rsrc(base, bytes);
#pragma unroll 1
    for (int k = 0; k < n; ++k) dma4(rs, (unsigned)(lane * 128), (unsigned)(u0 + k) * 8192u, lds + LDS_DUMMY);
  };
  auto w1_voff = [&](int piece) {
    const int ch = piece * 64 + lane, k = ch / 28, pos = ch - k * 28;
    return (unsigned)(k * F + ft * 224 + (pos ^ (3 * ((k >> 3) & 1))) * 8) * 2u;
  };
  auto a1_vx = [&](int l) { return (unsigned)((chunk * 256 + 32 * (l >> 5) + 16 * ((l >> 3) & 1) + ((l >> 4) & 1)) * D * 2 + ((l & 7) ^ (l >> 4)) * 16); };
  auto a1_vb = [&](int l) { return (unsigned)((cb * 64 + (l >> 3)) * D * 2 + ((l & 7) ^ (l >> 4)) * 16); };
  if (roleA) {
    const rsrc_t rs = mk_rsrc(a.x, S * D * 2), rre = mk_rsrc(a.Bre, P * D * 2), rim = mk_rsrc(a.Bim, P * D * 2);
    const unsigned vxe = a1_vx(lane), vxo = (vxe ^ 64u) + 64 * D * 2, vbe = a1_vb(lane);
    const unsigned sx = (unsigned)((wid >> 2) * 128 + (wid & 3) * 4) * (D * 2), sbb = (unsigned)wid * (8 * D * 2), vb = (wid & 1) ? vbe ^ 64u : vbe;
#pragma unroll
    for (int t = 0; t < A1_NS - 1; ++t) {
#pragma unroll
      for (int j = 0; j < 4; ++j) dma16(rs, (j & 1) ? vxo : vxe, sx + (j >> 1) * (2 * D * 2) + t * 128, lds + t * A1_STAGE + wid * 4096 + j * 1024);
      dma16(rre, vb, sbb + t * 128, lds + t * A1_STAGE + 32768 + (wid >> 1) * 4096 + (wid & 1) * 1024);
      dma16(rim, vb, sbb + t * 128, lds + t * A1_STAGE + 32768 + (wid >> 1) * 4096 + (wid & 1) * 1024 + 2048);
    }
    if (wid == 7) {
      dma4(mk_rsrc((const void*)(pc0 & ~255ull), CODE_PF_BYTES), (unsigned)(lane * 128), (unsigned)((idx % 5) * 8192), lds + LDS_DUMMY);
      __builtin_amdgcn_s_sleep(PF_DELAY);
      touch(a.w1, S * F * 2, PF_N0 * ri, PF_N0);
    }
    LOAD_TAG();
  }
  int isB = xc & 1;
  asm volatile("" : "+s"(isB));
  if (isB) {
    const int row = ri * 16 + wid * 2;
    u32x4 r[2][2], brow[2][2];
    {
      const u16* xp = a.x + (size_t)row * D;
      const u16* bp = (ri < 64 ? a.Bre : a.Bim) + (size_t)(row & 1023) * D;
      const unsigned lo = (unsigned)lane * 16u;
      asm volatile("global_load_dwordx4 %0, %4, %5\n\tglobal_load_dwordx4 %1, %4, %5 offset:1024\n\tglobal_load_dwordx4 %2, %4, %5 offset:2048\n\tglobal_load_dwordx4 %3, %4, %5 offset:3072"
                   : "=&v"(r[0][0]), "=&v"(r[0][1]), "=&v"(r[1][0]), "=&v"(r[1][1]) : "v"(lo), "s"(xp) : "memory");
      asm volatile("global_load_dwordx4 %0, %4, %5\n\tglobal_load_dwordx4 %1, %4, %5 offset:1024\n\tglobal_load_dwordx4 %2, %4, %5 offset:2048\n\tglobal_load_dwordx4 %3, %4, %5 offset:3072"
                   : "=&v"(brow[0][0]), "=&v"(brow[0][1]), "=&v"(brow[1][0]), "=&v"(brow[1][1]) : "v"(lo), "s"(bp) : "memory");
    }
    LOAD_TAG();
    wait_vm<4>();
    STAMP(20);
    asm volatile("" : "+v"(r[0][0]), "+v"(r[0][1]), "+v"(r[1][0]), "+v"(r[1][1])::"memory");
    const rsrc_t rx = mk_rsrc(a.ws + WS_HL, S * D * 2);
#pragma unroll
    for (int pass = 0; pass < 2; ++pass) {
      float s1[2], s2[2];
#pragma unroll
      for (int i = 0; i < 2; ++i) {
        float a1 = 0.f, a2 = 0.f;
#pragma unroll
        for (int k = 0; k < 8; ++k) { const unsigned w = r[i][k >> 2][k & 3]; a1 = dot2(w, 0x3f803f80u, a1); a2 = dot2(w, w, a2); }
        s1[i] = a1; s2[i] = a2;
      }
#pragma unroll
      for (int i = 0; i < 2; ++i) { s1[i] = wave_sum(s1[i]); s2[i] = wave_sum(s2[i]); }
#pragma unroll
      for (int i = 0; i < 2; ++i) {
        const float mean = s1[i] * (1.0f / D), var = fmaxf(fmaf(-mean, mean, s2[i] * (1.0f / D)), 0.f);
        const unsigned ms = pk_bf16(mean, __builtin_amdgcn_sqrtf(rbf(rbf(var) + 1e-5f)));
        const float rs_ = __builtin_amdgcn_rcpf(bfhi(ms)), mb = bflo(ms);
#pragma unroll
        for (int k = 0; k < 8; ++k) {
          const unsigned w = r[i][k >> 2][k & 3];
          r[i][k >> 2][k & 3] = pk_bf16((bflo(w) - mb) * rs_, (bfhi(w) - mb) * rs_);
        }
        if (pass == 0) {
          if (lane == 0) __hip_atomic_store(sy + SY_ST + row + i, ((u64)tag32 << 32) | ms, RLX_AGENT);
        } else {
          st16_wt(rx, (unsigned)lane * 16u, (unsigned)((row + i) * D) * 2u, r[i][0]);
          st16_wt(rx, (unsigned)lane * 16u + 1024u, (unsigned)((row + i) * D) * 2u, r[i][1]);
        }
      }
    }
    STAMP(1);
    asm volatile("s_waitcnt vmcnt(0)" : "+v"(brow[0][0]), "+v"(brow[0][1]), "+v"(brow[1][0]), "+v"(brow[1][1])::"memory");
    bar();
    if (wid == 0 && lane == 0) __hip_atomic_store(sy + SY_LN + ri, tag, RLX_AGENT);
    STAMP(2);
    if (wid != 6) {
      const rsrc_t rs = mk_rsrc(a.w1, S * F * 2);
      const int p0_ = 2 * (wid == 7 ? 6 : wid);
      const unsigned v0 = w1_voff(p0_), v1 = w1_voff(p0_ + 1);
#pragma unroll
      for (int t = 0; t < B1_NS - 1; ++t) {
        dma16(rs, v0, t * (32 * F * 2), lds + t * B1_STAGE + p0_ * 1024);
        dma16(rs, v1, t * (32 * F * 2), lds + t * B1_STAGE + p0_ * 1024 + 1024);
      }
    }
    {
      float sums[2];
#pragma unroll
      for (int i = 0; i < 2; ++i) {
        float s = 0.f;
#pragma unroll
        for (int k = 0; k < 8; ++k) { const unsigned w = brow[i][k >> 2][k & 3]; s = dot2(w, 0x3f803f80u, s); }
        sums[i] = wave_sum(s);
      }
      if (lane < 2) __hip_atomic_store(sy + SY_SB + row + lane, ((u64)tag32 << 32) | __float_as_uint(lane ? sums[1] : sums[0]), RLX_AGENT);
    }
  }

  if (wid >= 4) {
    const int pw = wid - 4;
    int lane = __builtin_amdgcn_mbcnt_hi(~0u, __builtin_amdgcn_mbcnt_lo(~0u, 0u));
    asm volatile("" : "+v"(lane));
    if (pw == 3) {
      auto flags_fetch = [&]() {
        dma16c(rsy, (unsigned)lane * 16u, SY_LN * 8, lds + LDS_FLG);
        dma16c(rsy, (unsigned)lane * 16u, SY_LN * 8 + 1024, lds + LDS_FLG + 1024);
      };
      auto flags_check = [&]() {
        bool ok = true;
#pragma unroll
        for (int k = 0; k < 4; ++k) ok &= *(LAS u64*)(lds + LDS_FLG + k * 512 + lane * 8) == tag;
        if (!__all(ok)) wait_flags<4>(sy + SY_LN + lane, true, tag);
      };
      auto xb_clear = [&](int k0) {
        const rsrc_t rxb = mk_rsrc(a.ws + WS_XB, 256 * 16384);
#pragma unroll
        for (int k = 0; k < 4; ++k) st16_wt(rxb, (unsigned)(lane * 16 + k * 1024), (unsigned)(bid * 16384 + k0 * 4096), (u32x4){0u, 0u, 0u, 0u});
      };
      if (lane < 4) *(LAS unsigned*)(size_t)(LDS_CNT + lane * 4) = 0u;
      if (roleA) {
        st16_wt(rsy, (unsigned)lane * 16u, (unsigned)(SY_GR * 8 + (chunk * P + cb * 64) * 16), (u32x4){0u, 0u, 0u, 0u});
        dma4(mk_rsrc(a.Lre, P * 2), (unsigned)(lane & 31) * 4u, cb * 128, lds + LDS_RAW + 3072);
        dma4(mk_rsrc(a.Lim, P * 2), (unsigned)(lane & 31) * 4u, cb * 128, lds + LDS_RAW + 3072 + 256);
        dma4(mk_rsrc(a.logdt, P * 2), (unsigned)(lane & 31) * 4u, cb * 128, lds + LDS_RAW + 3072 + 512);
        HOLD_TAG();
        wait_vm<5 + PF_N0>();
        const int bst = PF_ST0 + ((ri * PF_NST) >> 7);
        auto step = [&](int t) { bar(); if (t == bst) touch(a.w1, S * F * 2, 128 * PF_N0 + (14 - PF_N0) * ri, 14 - PF_N0); };
        PSTAMP(7, 24);
        step(0); step(1); step(2);
        if (bst <= 2) wait_vm<14 - PF_N0>(); else wait_vm<0>();
        if (lane == 0) __hip_atomic_store(sy + SY_LN + 128 + ri, tag, RLX_AGENT);
        LAS float* cf = (LAS float*)(lds + LDS_COEF) + lane;
        const float lr = bf2f(*(LAS u16*)(lds + LDS_RAW + 3072 + lane * 2)), li = bf2f(*(LAS u16*)(lds + LDS_RAW + 3072 + 256 + lane * 2));
        const float dtt = rbf(expf(bf2f(*(LAS u16*)(lds + LDS_RAW + 3072 + 512 + lane * 2)))), er = expf(dtt * lr);
        float arg = dtt * li;
        asm volatile("" : "+v"(arg));
        step(3);
        float sn, cs;
        sincosf(arg, &sn, &cs);
        float pr = er * cs, pi = er * sn;
        asm volatile("" : "+v"(pr), "+v"(pi));
        step(4);
        const float nr = pr - 1.0f, den = lr * lr + li * li;
        cf[0] = pr; cf[64] = pi;
        cf[128] = 2.0f * (nr * lr + pi * li) / den; cf[192] = 2.0f * (pi * lr - nr * li) / den;
        step(5);
#pragma unroll
        for (int k = 0; k < 8; ++k) {
          const float t = pr * pr - pi * pi; pi = 2.0f * pr * pi; pr = t;
          if (k >= 3) { cf[(4 + 2 * (k - 3)) * 64] = pr; cf[(5 + 2 * (k - 3)) * 64] = pi; }
        }
#pragma unroll 1
        for (int t = 6; t < 12; ++t) { step(t); if (t < 10) xb_clear(t - 6); }
        const unsigned sbo = (unsigned)(SY_SB * 8 + cb * 512 + (lane >> 5) * (P * 8) + (lane & 31) * 16);
#pragma unroll 1
        for (unsigned spins = 0;; ++spins) {
          dma16c(rsy, (unsigned)lane * 16u, (unsigned)(SY_ST * 8 + chunk * 2048), lds + LDS_RAW);
          dma16c(rsy, (unsigned)lane * 16u, (unsigned)(SY_ST * 8 + chunk * 2048 + 1024), lds + LDS_RAW + 1024);
          dma16c(rsy, sbo, 0, lds + LDS_RAW + 2048);
          if (spins == 0) { flags_fetch(); step(12); step(13); }
          wait_vm<0>();
          u32x2 gs[4], gb[2];
          bool ok = true;
#pragma unroll
          for (int k = 0; k < 4; ++k) { gs[k] = *(LAS u32x2*)(lds + LDS_RAW + k * 512 + lane * 8); ok &= gs[k][1] == tag32; }
#pragma unroll
          for (int k = 0; k < 2; ++k) { gb[k] = *(LAS u32x2*)(lds + LDS_RAW + 2048 + k * 512 + lane * 8); ok &= gb[k][1] == tag32; }
          if (__all(ok) || spins > SPIN_CAP) {
#pragma unroll
            for (int k = 0; k < 4; ++k) {
              LAS float* e = (LAS float*)(lds + LDS_STAT + (2 * k + (lane >> 5)) * 272 + (lane & 15) * 16 + ((lane >> 4) & 1) * 4);
              e[0] = -bflo(gs[k][0]); e[2] = __builtin_amdgcn_rcpf(bfhi(gs[k][0]));
            }
#pragma unroll
            for (int k = 0; k < 2; ++k) *(LAS unsigned*)(lds + LDS_SB + k * 256 + lane * 4) = gb[k][0];
            break;
          }
          __builtin_amdgcn_s_sleep(2);
        }
        touch(a.Cre, D * P * 2, 2 * ri, 2); touch(a.Cim, D * P * 2, 2 * ri, 2);
        touch(a.w2, F * S * 2, UA + (PF_WA / 2) * ri, PF_WA / 2); touch(a.w2, F * S * 2, (PF_WA / 2) * ri, PF_WA / 2);
        bar();
        flags_check();
        wait_lgkm0();
        step(15);
      } else {
        wait_vm<0>();
        dma4(mk_rsrc((const void*)(pc0 & ~255ull), CODE_PF_BYTES), (unsigned)(lane * 128), (unsigned)((idx % 5) * 8192), lds + LDS_DUMMY);
#pragma unroll 1
        for (int t = 0; t < 13; ++t) { bar(); if (t >= 4 && !(t & 1) && t < 12) xb_clear((t - 4) >> 1); }
        flags_fetch();
#pragma unroll 1
        for (int t = 13; t < 20; ++t) bar();
        wait_vm<0>();
        flags_check();
        if (bid == 255 && lane == 0) __hip_atomic_store(sy, tag, RLX_AGENT);
#pragma unroll 1
        for (int t = 20; t < B1_NT - PF_BT; ++t) bar();
        touch(a.w2, F * S * 2, UA + WA + (PF_WB / 2) * ri, PF_WB / 2); touch(a.w2, F * S * 2, WA + (PF_WB / 2) * ri, PF_WB / 2);
#pragma unroll 1
        for (int t = 0; t < PF_BT; ++t) bar();
      }
    } else if (roleA) {
      if (pw < 2) {
        const rsrc_t rs = mk_rsrc(a.x, S * D * 2);
        const unsigned vxe = a1_vx(lane), vxo = (vxe ^ 64u) + 64 * D * 2;
        unsigned so = (A1_NS - 1) * 128 + pw * (128 * D * 2), lo = (A1_NS - 1) * A1_STAGE + pw * 16384;
        auto issue = [&]() {
#pragma unroll
          for (int j = 0; j < 16; ++j) dma16(rs, (j & 1) ? vxo : vxe, so + (j >> 1) * (2 * D * 2), lds + lo + j * 1024);
          so += 128; lo = lo >= (A1_NS - 1) * A1_STAGE ? lo - (A1_NS - 1) * A1_STAGE : lo + A1_STAGE;
        };
        PSTAMP(4, 21);
        HOLD_TAG();
        wait_vm<0>();
        prod_run<16, A1_NS>(A1_NT - (A1_NS - 1), issue);
        prod_tail<16, A1_NS - 2>();
      } else {
        const unsigned ve = a1_vb(lane), vo = ve ^ 64u;
        const rsrc_t rre = mk_rsrc(a.Bre, P * D * 2), rim = mk_rsrc(a.Bim, P * D * 2);
        unsigned so = (A1_NS - 1) * 128, lo = (A1_NS - 1) * A1_STAGE + 32768;
        auto issue = [&]() {
#pragma unroll
          for (int j = 0; j < 8; ++j) {
            dma16(rre, (j & 1) ? vo : ve, so + j * (8 * D * 2), lds + lo + (j >> 1) * 4096 + (j & 1) * 1024);
            dma16(rim, (j & 1) ? vo : ve, so + j * (8 * D * 2), lds + lo + (j >> 1) * 4096 + (j & 1) * 1024 + 2048);
          }
          so += 128; lo = lo >= (A1_NS - 1) * A1_STAGE ? lo - (A1_NS - 1) * A1_STAGE : lo + A1_STAGE;
        };
        HOLD_TAG();
        wait_vm<0>();
        prod_run<16, A1_NS>(A1_NT - (A1_NS - 1), issue);
        prod_tail<16, A1_NS - 2>();
      }
    } else {
      const int dd0 = dt1 * 128;
      if (pw < 2) {
        unsigned voff[7];
#pragma unroll
        for (int j = 0; j < 7; ++j) voff[j] = w1_voff(pw * 7 + j);
        const rsrc_t rs = mk_rsrc(a.w1, S * F * 2);
        unsigned so = (B1_NS - 1) * (32 * F * 2), lo = (B1_NS - 1) * B1_STAGE + pw * 7168;
        auto issue = [&]() {
#pragma unroll
          for (int j = 0; j < 7; ++j) dma16(rs, voff[j], so, lds + lo + j * 1024);
          so += 32 * F * 2; lo = lo >= (B1_NS - 1) * B1_STAGE ? lo - (B1_NS - 1) * B1_STAGE : lo + B1_STAGE;
        };
        wait_vm<0>();
        prod_run<7, B1_NS>(B1_NT - (B1_NS - 1), issue);
        prod_tail<7, B1_NS - 2>();
      } else {
        unsigned voff[2];
#pragma unroll
        for (int j = 0; j < 2; ++j) {
          const int k = j * 8 + (lane >> 4), pos = lane & 15;
          const int c = pos ^ (((k & 3) << 1) | (((k >> 3) & 1) << 3));
          voff[j] = (unsigned)((lane >> 4) * D + dd0 + c * 8) * 2u;
        }
        const rsrc_t rs = mk_rsrc(a.ws + WS_HL, S * D * 2);
        unsigned so = 0, lo = B1_ABYTES;
        auto issue = [&]() {
#pragma unroll
          for (int j = 0; j < 8; ++j) dma16c(rs, voff[(j >> 1) & 1], so + j * 4 * (D * 2), lds + lo + j * 1024);
          so += 32 * D * 2; lo = lo >= (B1_NS - 1) * B1_STAGE ? lo - (B1_NS - 1) * B1_STAGE : lo + B1_STAGE;
        };
        int done = B1_NS - 1;
        wait_flags<1>(sy + SY_LN + lane, lane < 2 * done, tag);
        PSTAMP(6, 16);
        prod_fill(B1_NS - 1, issue);
        wait_vm<(B1_NS - 2) * 8>();
        bar();
        int pre = flag_prefix<2>(sy + SY_LN + lane, 64, 2 * done + 2, tag, lane);
        issue();
        ++done;
#pragma unroll 1
        while (pre < 128 && done < B1_NT) {
          const int n = pre / 2 - done;
          prod_run<8, B1_NS>(n, issue);
          done += n;
          wait_vm<(B1_NS - 2) * 8>();
          bar();
          pre = flag_prefix<2>(sy + SY_LN + lane, 64, 2 * done + 2, tag, lane);
          issue();
          ++done;
        }
        prod_run<8, B1_NS>(B1_NT - done, issue);
        prod_tail<8, B1_NS - 2>();
      }
    }
    bar();
    const int nU = nt2 - nC;
    if (pw == 1 || pw == 2) {
      const rsrc_t rxx = mk_rsrc(a.x, S * D * 2);
      const int dk = d0 + (roleA ? 0 : 64);
#pragma unroll
      for (int k = 0; k < 8; ++k) dma16(rxx, (unsigned)((lane >> 3) * (D * 2) + ((lane & 7) ^ (lane >> 3)) * 16), (unsigned)((t0 + 64 * (pw - 1) + 8 * k) * D + dk) * 2u, lds + LDS_RES + (8 * (pw - 1) + k) * 1024);
      if (pw == 1) dma16c(rsy, (unsigned)lane * 16u, (unsigned)(SY_ST + t0) * 8u, lds + LDS_RES2);
      else { dma4(mk_rsrc(a.b2, S * 2), (unsigned)lane * 4u, (unsigned)t0 * 2u, lds + LDS_RES2 + 1024); dma4(mk_rsrc(a.Dsk, D * 2), (unsigned)lane * 4u, (unsigned)dk * 2u, lds + LDS_RES2 + 1280); }
      wait_vm<0>();
      if (lane == 0) __hip_atomic_fetch_add((LAS unsigned*)(size_t)(LDS_CNT + 12), 1u, __ATOMIC_RELAXED, __HIP_MEMORY_SCOPE_WORKGROUP);
    }
    if (pw == 0 || pw == 3) {
      const int dh = d0 + (((pw ? 1 : 0) ^ (roleA ? 0 : 1)) << 6), r4 = lane >> 4;
      const unsigned vce = (unsigned)(dh + (lane >> 3)) * (P * 2) + (unsigned)(((lane & 7) ^ (lane >> 4)) * 16), vco = vce ^ 64u;
      const unsigned vu = (unsigned)((dh + (lane >> 2)) * 64 + (((lane & 3) ^ (((r4 & 1) << 1) ^ ((r4 >> 1) * 3))) * 16));
      const rsrc_t ru = mk_rsrc(a.ws + WS_U, D * F * 2);
      rsrc_t rc = mk_rsrc(a.Cre, D * P * 2), rc2 = mk_rsrc(a.Cim, D * P * 2);
      unsigned so = 0, lo = pw ? 8192 : 0, sh = 0;
      auto issueC = [&]() {
        so = ((unsigned)cbs & 15u) * 128;
#pragma unroll
        for (int j = 0; j < 8; ++j) dma16(rc, (j & 1) ? vco : vce, so + j * 8 * (P * 2), lds + lo + j * 1024);
        const rsrc_t t_ = rc; rc = rc2; rc2 = t_;
        cbs >>= sh; sh ^= 4; lo = (lo + P2_STAGE) & (P2_NS * P2_STAGE - 1);
      };
      auto issueU = [&]() {
#pragma unroll
        for (int j = 0; j < 8; ++j) dma16c(ru, vu, so + (j & 1) * (D * 64) + (j >> 1) * 1024, lds + lo + j * 1024);
        so += 2 * D * 64; lo = (lo + P2_STAGE) & (P2_NS * P2_STAGE - 1);
      };
      prod_fill(P2_NS - 1, issueC);
      if (roleA) {
        const int bst = 1 + ((ri * PF_NST2) >> 7);
        prod_run<8, P2_NS>(bst, issueC);
        if (pw == 3) { touch(a.w2, F * S * 2, UA + WA + WB + RB * ri, RB); touch(a.w2, F * S * 2, WA + WB + RA * ri, RA); }
        prod_run<8, P2_NS>(nC - 3 - bst, issueC);
      } else {
        prod_run<8, P2_NS>(nC - 3, issueC);
      }
      so = ku0 * (2 * D * 64);
      wait_vm<16>();
      bar();
      wait_flags<1>(sy + SY_U + dt * 16 + (lane & 15), true, tag);
      PSTAMP(4, 19);
      issueU();
      prod_run<8, P2_NS>(nU - 1, issueU);
    } else {
      unsigned voff[2];
#pragma unroll
      for (int j = 0; j < 2; ++j) {
        const int k = j * 8 + (lane >> 4), pos = lane & 15;
        const int c = pos ^ (((k & 3) << 1) | (((k >> 3) & 1) << 3));
        voff[j] = (unsigned)(((pw - 1) * 32 + (lane >> 4)) * S + t0 + c * 8) * 2u;
      }
      const unsigned vh = (unsigned)(lane * 16 + (pw - 1) * 8192);
      const rsrc_t rh = mk_rsrc(a.ws + WS_HT, 2 * P * S * 2), rw = mk_rsrc(a.w2, F * S * 2);
      unsigned so = 0, lo = 16384 + (pw - 1) * 8192, sh = 0;
      gu64* hf = sy + SY_H + (tt >> 1) * 16 + ((unsigned)(cbs >> (4 * (lane & 15))) & 15u);
      auto issueH = [&]() {
        so = (tt * 32 + sh * 4 + ((unsigned)cbs & 15u)) * 16384;
#pragma unroll
        for (int j = 0; j < 8; ++j) dma16c(rh, vh, so + j * 1024, lds + lo + j * 1024);
        cbs >>= sh; sh ^= 4; lo = (lo + P2_STAGE) & (P2_NS * P2_STAGE - 1);
      };
      auto issueW = [&]() {
#pragma unroll
        for (int j = 0; j < 8; ++j) dma16(rw, voff[(j >> 1) & 1], so + j * 4 * (S * 2), lds + lo + j * 1024);
        so += 64 * S * 2; lo = (lo + P2_STAGE) & (P2_NS * P2_STAGE - 1);
      };
      if (roleA) lds_wait(LDS_CNT, 4u);
      int done = P2_NS - 1, pre = flag_prefix<1>(hf, nC / 2, 2, tag, lane);
      PSTAMP(6, 17);
      prod_fill(P2_NS - 1, issueH);
#pragma unroll 1
      while (pre < 128 && done < nC) {
        const int n = (2 * pre < nC ? 2 * pre : nC) - done;
        prod_run<8, P2_NS>(n, issueH);
        done += n;
        if (done < nC) {
          wait_vm<16>();
          bar();
          pre = flag_prefix<1>(hf, nC / 2, done / 2 + 1, tag, lane);
          issueH();
          ++done;
        }
      }
      prod_run<8, P2_NS>(nC - done, issueH);
      so = ku0 * (64 * S * 2);
      if (!roleA && pw == 1) wait_flags<1>(sy + SY_H + chunk * 16 + cb, true, tag);
      prod_run<8, P2_NS>(nU, issueW);
    }
    prod_tail<8, P2_NS - 2>();
    return;
  }

  const int wm = wid >> 1, wn = wid & 1;
  int cl = lane & 15, g = lane >> 4, q4 = cl >> 2, p4 = lane & 3, ln = lane;
#define RELANE() do { ln = __builtin_amdgcn_mbcnt_hi(~0u, __builtin_amdgcn_mbcnt_lo(~0u, 0u)); asm volatile("" : "+v"(ln)); cl = ln & 15; g = ln >> 4; q4 = cl >> 2; p4 = ln & 3; } while (0)
  const int kh = roleA ? 0 : 1, pa = bid ^ 1;
  const rsrc_t rxb = mk_rsrc(a.ws + WS_XB, 256 * 16384);
  f32x4 res[2][4];
  auto residual = [&]() {
    lds_wait(LDS_CNT + 12, 2u);
#pragma unroll
    for (int n = 0; n < 4; ++n) {
      const int tl = 64 * wn + 16 * n + cl;
      const unsigned ms = *(LAS unsigned*)(lds + LDS_RES2 + tl * 8);
      const float rs_ = __builtin_amdgcn_rcpf(bfhi(ms)), mb = bflo(ms), b2 = bf2f(*(LAS u16*)(lds + LDS_RES2 + 1024 + tl * 2));
#pragma unroll
      for (int mi = 0; mi < 2; ++mi) {
        const int c8 = 4 * wm + 2 * mi + (g >> 1);
        const u32x2 xv = *(LAS u32x2*)(lds + LDS_RES + tl * 128 + ((c8 ^ (tl & 7)) * 16) + (g & 1) * 8);
        const u32x2 dk = *(LAS u32x2*)(lds + LDS_RES2 + 1280 + c8 * 16 + (g & 1) * 8);
#pragma unroll
        for (int h = 0; h < 2; ++h) {
          const float x0 = bflo(xv[h]), x1 = bfhi(xv[h]);
          const unsigned xn = pk_bf16((x0 - mb) * rs_, (x1 - mb) * rs_);
          res[mi][n][2 * h] = fmaf(bflo(xn), bflo(dk[h]), x0 + b2); res[mi][n][2 * h + 1] = fmaf(bfhi(xn), bfhi(dk[h]), x1 + b2);
        }
        asm volatile("" : "+v"(res[mi][n]));
      }
    }
  };

  if (roleA) {
    HOLD_TAG();
    RELANE();
    const int p0 = cb * 64;
    const unsigned rb = (unsigned)(cl * 128 + ((g ^ (cl >> 1)) * 16));
    unsigned pa0 = rb + wm * 16384, pa1 = pa0 ^ 64u, pb0 = rb + 32768 + wn * 8192, pb1 = pb0 ^ 64u;
    f32x4 acc[8][4];
#pragma unroll
    for (int m = 0; m < 8; ++m)
#pragma unroll
      for (int n = 0; n < 4; ++n) acc[m][n] = (f32x4){0.f, 0.f, 0.f, 0.f};
    bf16x8 xa[8], xb[4], ya[8], yb[4];
    auto rd = [&](bf16x8* fa, bf16x8* fb, unsigned pa_, unsigned pb_, int r) {
      if (r == 0) fa[0] = lds_rd128(pa_); else if (r < 5) fb[r - 1] = lds_rd128(pb_ + (r - 1) * 2048); else fa[r - 4] = lds_rd128(pa_ + (r - 4) * 2048);
    };
    int slot = 0, dl = 0;
    STAMP(23);
    wait_vm<0>();
    bar();
    STAMP(3);
#pragma unroll
    for (int r = 0; r < 12; ++r) rd(xa, xb, pa0, pb0, r);
#pragma unroll 1
    for (int t = 0; t < A1_NT; ++t) {
      SCHED_FENCE(); wait_lgkm0(); SCHED_FENCE();
      phase<32>(acc, xa, xb, [&](int i) {
        if (i % A1_RS == 0 && i / A1_RS < 12) rd(ya, yb, pa1, pb1, i / A1_RS);
        if (i == 25) { dl = (slot == A1_NS - 1) ? -(A1_NS - 1) * A1_STAGE : A1_STAGE; slot = (slot == A1_NS - 1) ? 0 : slot + 1; }
        if (i == 27) { pa0 += dl; pb0 += dl; asm volatile("" : "+v"(pa0), "+v"(pb0)); }
        if (i == 29) { pa1 += dl; pb1 += dl; asm volatile("" : "+v"(pa1), "+v"(pb1)); }
      });
      wait_lgkm0();
      bar();
      SCHED_FENCE();
      phase<32>(acc, ya, yb, [&](int i) { if (i % A1_RS == 0 && i / A1_RS < 12) rd(xa, xb, pa0, pb0, i / A1_RS); });
    }
    RELANE();
    STAMP(4);
    float cf[2][14], sb[4];
#pragma unroll
    for (int q = 0; q < 2; ++q) {
#pragma unroll
      for (int k = 0; k < 14; ++k) cf[q][k] = ((LAS float*)(lds + LDS_COEF))[k * 64 + 32 * wn + 16 * q + cl];
      sb[2 * q] = ((LAS float*)(lds + LDS_SB))[32 * wn + 16 * q + cl]; sb[2 * q + 1] = ((LAS float*)(lds + LDS_SB))[64 + 32 * wn + 16 * q + cl];
    }
    f32x2 er[2], ei[2];
#pragma unroll
    for (int q = 0; q < 2; ++q) { er[q] = bc2(0.f); ei[q] = bc2(0.f); }
#pragma unroll
    for (int m = 0; m < 8; ++m)
#pragma unroll
      for (int jj = 0; jj < 2; ++jj) {
        const f32x4 st = *(LAS f32x4*)(lds + LDS_STAT + (4 * wm + g) * 272 + (2 * m + jj) * 16);
        const f32x2 c2 = {st[0], st[1]}, r2 = {st[2], st[3]};
#pragma unroll
        for (int q = 0; q < 2; ++q) {
          const f32x2 ur = fma2(c2, bc2(sb[2 * q]), (f32x2){acc[m][2 * q][2 * jj], acc[m][2 * q][2 * jj + 1]}) * r2;
          const f32x2 ui = fma2(c2, bc2(sb[2 * q + 1]), (f32x2){acc[m][2 * q + 1][2 * jj], acc[m][2 * q + 1][2 * jj + 1]}) * r2;
          acc[m][2 * q][2 * jj] = ur.x; acc[m][2 * q][2 * jj + 1] = ur.y; acc[m][2 * q + 1][2 * jj] = ui.x; acc[m][2 * q + 1][2 * jj + 1] = ui.y;
          const f32x2 nr = fma2(bc2(cf[q][0]), er[q], fma2(bc2(-cf[q][1]), ei[q], ur));
          const f32x2 ni = fma2(bc2(cf[q][0]), ei[q], fma2(bc2(cf[q][1]), er[q], ui));
          er[q] = nr; ei[q] = ni;
        }
        if (jj == 1 && (m & 1)) asm volatile("" : "+v"(er[0]), "+v"(ei[0]), "+v"(er[1]), "+v"(ei[1]));
      }
    float sr[2], si[2], pr_[2], pi_[2];
    LAS float* tot = (LAS float*)(lds + LDS_TOT);
#pragma unroll
    for (int q = 0; q < 2; ++q) {
      float tr, ti;
      CMUL(tr, ti, cf[q][4], cf[q][5], er[q].x, ei[q].x);
      sr[q] = tr + er[q].y; si[q] = ti + ei[q].y;
      float vr = __shfl_up(sr[q], 16), vi = __shfl_up(si[q], 16);
      CMUL(tr, ti, cf[q][6], cf[q][7], vr, vi);
      if (g >= 1) { sr[q] += tr; si[q] += ti; }
      vr = __shfl_up(sr[q], 32); vi = __shfl_up(si[q], 32);
      CMUL(tr, ti, cf[q][8], cf[q][9], vr, vi);
      if (g >= 2) { sr[q] += tr; si[q] += ti; }
      vr = __shfl_up(sr[q], 16); vi = __shfl_up(si[q], 16);
      pr_[q] = g >= 1 ? vr : 0.f; pi_[q] = g >= 1 ? vi : 0.f;
      if (wm == 0 && g == 3) *(LAS f32x2*)(tot + (32 * wn + 16 * q + cl) * 2) = (f32x2){sr[q], si[q]};
    }
    if (wm == 0) {
      wait_lgkm0();
      if (ln == 0) __hip_atomic_store((LAS unsigned*)(size_t)(LDS_CNT + 4 + 4 * wn), 1u, __ATOMIC_RELAXED, __HIP_MEMORY_SCOPE_WORKGROUP);
    } else {
      lds_wait(LDS_CNT + 4 + 4 * wn, 1u);
    }
    f32x2 tq[2];
#pragma unroll
    for (int q = 0; q < 2; ++q) {
      tq[q] = *(LAS f32x2*)(tot + (32 * wn + 16 * q + cl) * 2);
      if (wm == 1 && g == 3 && chunk < 7) {
        float tr, ti;
        CMUL(tr, ti, cf[q][10], cf[q][11], tq[q].x, tq[q].y);
        st16_wt(rsy, (unsigned)(SY_GR * 8 + (chunk * P + p0 + 32 * wn + 16 * q + cl) * 16), 0, (u32x4){__float_as_uint(tr + sr[q]), 1u, __float_as_uint(ti + si[q]), 1u});
      }
    }
    STAMP(5);
#pragma unroll
    for (int m = 0; m < 8; ++m)
#pragma unroll
      for (int q = 0; q < 2; ++q) {
        const f32x4 ur = acc[m][2 * q], ui = acc[m][2 * q + 1];
        acc[m][2 * q] = ur * cf[q][2] - ui * cf[q][3];
        acc[m][2 * q + 1] = ui * (-cf[q][2]) - ur * cf[q][3];
        asm volatile("" : "+v"(acc[m][2 * q]), "+v"(acc[m][2 * q + 1]));
      }
    float hr[2] = {0.f, 0.f}, hi[2] = {0.f, 0.f};
    if (chunk > 0) {
      float gr[2][2], gi[2][2];
#pragma unroll 1
      for (unsigned spins = 0;; ++spins) {
        bool ok = true;
#pragma unroll
        for (int q = 0; q < 2; ++q)
#pragma unroll
          for (int k = 0; k < 2; ++k) {
            const int cc = (g + 4 * k < chunk) ? g + 4 * k : 0;
            const u32x4 v = __builtin_amdgcn_raw_buffer_load_b128(rsy, (unsigned)(SY_GR * 8 + (cc * P + p0 + 32 * wn + 16 * q + cl) * 16), 0, 16);
            ok &= (v[1] == 1u) & (v[3] == 1u);
            gr[q][k] = __uint_as_float(v[0]); gi[q][k] = __uint_as_float(v[2]);
          }
        if (__all(ok) || spins > SPIN_CAP) break;
        __builtin_amdgcn_s_sleep(1);
        asm volatile("" ::: "memory");
      }
#pragma unroll
      for (int q = 0; q < 2; ++q) {
        float ar_ = 0.f, ai_ = 0.f;
#pragma unroll
        for (int k = 0; k < 2; ++k) {
          float vr = gr[q][k], vi = gi[q][k];
          const int e = chunk - 1 - (g + 4 * k);
#pragma unroll
          for (int i = 0; i < 6 - 4 * k; ++i) {
            float tr, ti;
            CMUL(tr, ti, cf[q][12], cf[q][13], vr, vi);
            vr = i < e ? tr : vr; vi = i < e ? ti : vi;
          }
          ar_ += e >= 0 ? vr : 0.f; ai_ += e >= 0 ? vi : 0.f;
        }
        ar_ += __shfl_xor(ar_, 16); ai_ += __shfl_xor(ai_, 16);
        ar_ += __shfl_xor(ar_, 32); ai_ += __shfl_xor(ai_, 32);
        hr[q] = ar_; hi[q] = ai_;
      }
    }
    STAMP(6);
    f32x2 wr[2], wi[2];
#pragma unroll
    for (int q = 0; q < 2; ++q) {
      float tr, ti;
      if (wm == 1) { CMUL(tr, ti, cf[q][10], cf[q][11], hr[q], hi[q]); hr[q] = tr + tq[q].x; hi[q] = ti + tq[q].y; }
      CMUL(tr, ti, cf[q][6], cf[q][7], hr[q], hi[q]);
      if (g & 1) { hr[q] = tr; hi[q] = ti; }
      CMUL(tr, ti, cf[q][8], cf[q][9], hr[q], hi[q]);
      if (g & 2) { hr[q] = tr; hi[q] = ti; }
      hr[q] += pr_[q]; hi[q] += pi_[q];
      CMUL(tr, ti, cf[q][4], cf[q][5], hr[q], hi[q]);
      const f32x2 h0r = {hr[q], tr + er[q].x}, h0i = {hi[q], ti + ei[q].x};
      wr[q] = h0r * cf[q][2] - h0i * cf[q][3];
      wi[q] = h0i * (-cf[q][2]) - h0r * cf[q][3];
    }
    LAS char* scr = lds + wid * P2_STAGE + 16384;
    const unsigned fsw = (unsigned)(((cl & 3) << 1) | (((cl >> 3) & 1) << 3));
    const rsrc_t rh = mk_rsrc(a.ws + WS_HT, 2 * P * S * 2);
#pragma unroll
    for (int q = 0; q < 2; ++q) {
      const f32x2 ar2 = bc2(cf[q][0]), ai2 = bc2(cf[q][1]);
#pragma unroll
      for (int mh = 0; mh < 2; ++mh) {
        u32x4 o[2][2];
#pragma unroll
        for (int mq = 0; mq < 4; ++mq) {
          const int m = 4 * mh + mq;
          const f32x2 u0r = {acc[m][2 * q][0], acc[m][2 * q][1]}, u0i = {acc[m][2 * q + 1][0], acc[m][2 * q + 1][1]};
          const f32x2 u1r = {acc[m][2 * q][2], acc[m][2 * q][3]}, u1i = {acc[m][2 * q + 1][2], acc[m][2 * q + 1][3]};
          const f32x2 n0r = fma2(ar2, wr[q], fma2(ai2, wi[q], u0r)), n0i = fma2(ar2, wi[q], fma2(-ai2, wr[q], u0i));
          const f32x2 n1r = fma2(ar2, n0r, fma2(ai2, n0i, u1r)), n1i = fma2(ar2, n0i, fma2(-ai2, n0r, u1i));
          wr[q] = n1r; wi[q] = n1i;
          o[0][0][mq] = pk_bf16(n0r.x, n1r.x); o[0][1][mq] = pk_bf16(n0r.y, n1r.y);
          o[1][0][mq] = pk_bf16(n0i.x, n1i.x); o[1][1][mq] = pk_bf16(n0i.y, n1i.y);
        }
#pragma unroll
        for (int part = 0; part < 2; ++part)
#pragma unroll
          for (int run = 0; run < 2; ++run)
            *(LAS u32x4*)(scr + (part * 32 + 16 * q + cl) * 256 + (((unsigned)(4 * g + 2 * run + mh) ^ fsw) * 16)) = o[part][run];
      }
#pragma unroll
      for (int i = 0; i < 8; ++i) {
        const int part = i >> 2, pc = 4 * q + (i & 3);
        const u32x4 v = *(LAS u32x4*)(scr + part * 8192 + pc * 1024 + ln * 16);
        st16_wt(rh, (unsigned)(ln * 16 + pc * 1024), (unsigned)(((2 * chunk + wm) * 32 + part * 16 + cb) * 16384 + wn * 8192), v);
      }
      SCHED_FENCE();
    }
    STAMP(7);
    publish(LDS_CNT, sy + SY_H + chunk * 16 + cb, tag, ln);
    STAMP(8);
    residual();
  } else {
    RELANE();
    const int f0 = ft * 224, dd0 = dt1 * 128;
    const int k0 = 8 * g + q4;
    unsigned pa0 = (unsigned)(k0 * 448 + ((12 * wm + p4) ^ (3 * (g & 1))) * 16 + (p4 & 1) * 8);
    unsigned pa1 = (unsigned)(k0 * 448 + ((12 * wm + p4) ^ (3 * (g & 1))) * 16 + ((p4 & 1) ^ 1) * 8);
    unsigned pa2 = (unsigned)(k0 * 448 + ((24 + 2 * wm + (p4 >> 1)) ^ (3 * (g & 1))) * 16 + (p4 & 1) * 8);
    unsigned pb[4];
#pragma unroll
    for (int n = 0; n < 4; ++n) pb[n] = (unsigned)(B1_ABYTES + k0 * 256 + ((8 * wn + 2 * n + (p4 >> 1)) ^ ((q4 << 1) | ((g & 1) << 3))) * 16 + (p4 & 1) * 8);
    f32x4 acc[7][4];
#pragma unroll
    for (int m = 0; m < 7; ++m) {
      const u32x2 bv = *(const u32x2*)(a.b1 + f0 + (m < 6 ? 96 * wm + 32 * (m >> 1) + 8 * g + 4 * ((m & 1) ^ (g & 1)) : 192 + 16 * wm + 4 * g));
#pragma unroll
      for (int n = 0; n < 4; ++n) acc[m][n] = (f32x4){bflo(bv[0]), bfhi(bv[0]), bflo(bv[1]), bfhi(bv[1])};
    }
    TrFrag xa[7], xb[4], ya[7], yb[4];
    int slot = 0, dl = 0;
    auto side = [&](TrFrag* fa, TrFrag* fb, int i) {
      const int f = i >> 1, h = i & 1;
      if (f == 0) fa[0].h[h] = lds_rdtr(pa0 + h * 1792);
      else if (f < 5) fb[f - 1].h[h] = lds_rdtr(pb[f - 1] + h * 1024);
      else if (f < 10) fa[f - 4].h[h] = lds_rdtr((((f - 4) & 1) ? pa1 : pa0) + ((f - 4) >> 1) * 64 + h * 1792);
      else if (f == 10) fa[6].h[h] = lds_rdtr(pa2 + h * 1792);
      else if (i == 22) { dl = (slot == B1_NS - 1) ? -(B1_NS - 1) * B1_STAGE : B1_STAGE; slot = (slot == B1_NS - 1) ? 0 : slot + 1; }
      else if (i == 23) { pa0 += dl; pb[0] += dl; asm volatile("" : "+v"(pa0), "+v"(pb[0])); }
      else if (i == 24) { pb[1] += dl; pb[2] += dl; asm volatile("" : "+v"(pb[1]), "+v"(pb[2])); }
      else if (i == 25) { pb[3] += dl; pa1 += dl; asm volatile("" : "+v"(pb[3]), "+v"(pa1)); }
      else if (i == 26) { pa2 += dl; asm volatile("" : "+v"(pa2)); }
    };
    wait_vm<0>();
    STAMP(15);
    bar();
    STAMP(3);
#pragma unroll
    for (int i = 0; i < 28; ++i) side(xa, xb, i);
#pragma unroll 1
    for (int t = 0; t < B1_NT; t += 2) {
      SCHED_FENCE(); wait_lgkm0(); bar(); SCHED_FENCE();
      phase<28>(acc, xa, xb, [&](int i) { side(ya, yb, i); });
      wait_lgkm0(); bar(); SCHED_FENCE();
      phase<28>(acc, ya, yb, [&](int i) { side(xa, xb, i); });
    }
    RELANE();
    STAMP(4);
    const rsrc_t ru = mk_rsrc(a.ws + WS_U, D * F * 2);
    auto act4 = [&](const f32x4& v) {
      const f32x2 v0 = gelu_tanh2((f32x2){v[0], v[1]}), v1 = gelu_tanh2((f32x2){v[2], v[3]});
      return (u32x2){pk_bf16(v0.x, v0.y), pk_bf16(v1.x, v1.y)};
    };
#pragma unroll
    for (int n = 0; n < 4; ++n) *(LAS u32x2*)(lds + LDS_U6 + (64 * wn + 16 * n + cl) * 64 + 32 * wm + 8 * g) = act4(acc[6][n]);
    wait_lgkm0();
    if (ln == 0) __hip_atomic_fetch_add((LAS unsigned*)(size_t)(LDS_CNT + 4 + 4 * wn), 1u, __ATOMIC_RELAXED, __HIP_MEMORY_SCOPE_WORKGROUP);
#pragma unroll
    for (int n = 0; n < 4; ++n) {
      const unsigned vo = (unsigned)((dd0 + 64 * wn + 16 * n + cl) * 64);
#pragma unroll
      for (int mm = 0; mm < 3; ++mm) {
        const u32x2 w0 = act4(acc[2 * mm][n]), w1 = act4(acc[2 * mm + 1][n]);
        const bool sw = g & 1;
        const u32x2 lo = sw ? w1 : w0, hi = sw ? w0 : w1;
        st16_wt(ru, vo + 16 * g, (unsigned)((ft * 7 + 3 * wm + mm) * D * 64), (u32x4){lo[0], lo[1], hi[0], hi[1]});
      }
      if (n == 1) {
        lds_wait(LDS_CNT + 4 + 4 * wn, 2u);
#pragma unroll
        for (int k = 0; k < 2; ++k) {
          const unsigned o = (unsigned)(wn * 4096 + wm * 2048 + k * 1024 + ln * 16);
          st16_wt(ru, o, (unsigned)(((ft * 7 + 6) * D + dd0) * 64), *(LAS u32x4*)(lds + LDS_U6 + o));
        }
      }
    }
    STAMP(7);
    residual();
    publish(LDS_CNT, sy + SY_U + dt1 * 16 + ft, tag, ln);
    STAMP(8);
  }

  f32x4 acc[4][4];
#pragma unroll
  for (int n = 0; n < 4; ++n) { acc[0][n] = res[0][n]; acc[1][n] = res[1][n]; acc[2][n] = (f32x4){0.f, 0.f, 0.f, 0.f}; acc[3][n] = (f32x4){0.f, 0.f, 0.f, 0.f}; }
  const unsigned au0 = (unsigned)(wm * 4096 + cl * 64 + ((g ^ (((q4 & 1) << 1) ^ ((q4 >> 1) * 3))) * 16));
  unsigned pa0 = (unsigned)((wm * 32 + cl) * 128 + ((g ^ (cl >> 1)) * 16)), pa1 = pa0 ^ 64u;
  const unsigned sw0 = pa0 ^ au0 ^ 65536u, sw1 = pa1 ^ (au0 + 1024u) ^ 65536u;
  unsigned pb[4];
#pragma unroll
  for (int n = 0; n < 4; ++n) pb[n] = (unsigned)(16384 + (8 * g + q4) * 256 + ((8 * wn + 2 * n + (p4 >> 1)) ^ ((q4 << 1) | ((g & 1) << 3))) * 16 + (p4 & 1) * 8);
  bf16x8 xa[4], ya[4];
  TrFrag xb[4], yb[4];
  static_assert(P2_NS == 4 && NAB % 2 == 0 && NBA % 2 == 0 && NAB >= P2_NS && NAB <= 16 && PF_NST2 < 29 - NAB, "phase-2 loop: two stages per trip, 64-KB windows");
  auto rd = [&](bf16x8* fa, TrFrag* fb, int so, int ks, int r) {
    const unsigned pa_ = (ks ? pa1 : pa0) + so;
    if (r == 0) fa[0] = lds_rd128(pa_);
    else if (r < 9) fb[(r - 1) >> 1].h[(r - 1) & 1] = lds_rdtr(pb[(r - 1) >> 1] + so + ks * 8192 + ((r - 1) & 1) * 1024);
    else if (r < 12) fa[r - 8] = lds_rd128(pa_ + ((r - 8) >> 1) * 8192 + ((r - 8) & 1) * 2048);
  };
  STAMP(18);
  bar();
  STAMP(9);
#pragma unroll
  for (int r = 0; r < 12; ++r) rd(xa, xb, 0, 0, r);
#pragma unroll 1
  for (int t = 0; t < nt2; t += 2) {
    unsigned x0, x1;
    SCHED_FENCE(); wait_lgkm0(); SCHED_FENCE();
    phase<16>(acc, xa, xb, [&](int i) { rd(ya, yb, 0, 1, i); });
    wait_lgkm0(); bar(); SCHED_FENCE();
    phase<16>(acc, ya, yb, [&](int i) {
      rd(xa, xb, P2_STAGE, 0, i);
      if (i == 13) { const bool sw = (t + 2 == nC); x0 = sw ? sw0 : 65536u; x1 = sw ? sw1 : 65536u; asm volatile("" : "+v"(x0), "+v"(x1)); }
      if (i == 14) { pa0 ^= x0; asm volatile("" : "+v"(pa0)); }
    });
    wait_lgkm0(); SCHED_FENCE();
    phase<16>(acc, xa, xb, [&](int i) {
      rd(ya, yb, P2_STAGE, 1, i);
      if (i == 12) { pb[0] ^= 65536u; pb[1] ^= 65536u; asm volatile("" : "+v"(pb[0]), "+v"(pb[1])); }
      if (i == 13) { pb[2] ^= 65536u; pb[3] ^= 65536u; asm volatile("" : "+v"(pb[2]), "+v"(pb[3])); }
      if (i == 14) { pa1 ^= x1; asm volatile("" : "+v"(pa1)); }
    });
    wait_lgkm0(); bar(); SCHED_FENCE();
    phase<16>(acc, ya, yb, [&](int i) { rd(xa, xb, 0, 0, i); });
  }
  STAMP(10);
  const unsigned xo = (unsigned)(wid * 4096 + ln * 16);
#pragma unroll
  for (int n = 0; n < 4; ++n) {
    u32x4 w = {pk_bf16(acc[2][n][0], acc[2][n][1]), pk_bf16(acc[2][n][2], acc[2][n][3]), pk_bf16(acc[3][n][0], acc[3][n][1]), pk_bf16(acc[3][n][2], acc[3][n][3])};
#pragma unroll
    for (int k = 0; k < 4; ++k) w[k] = w[k] ? w[k] : 0x80000000u;
    st16_wt(rxb, xo + n * 1024, bid * 16384, w);
  }
  STAMP(11);
  u32x4 rv[4];
#pragma unroll 1
  for (unsigned spins = 0; spins < SPIN_CAP; ++spins) {
    bool ok = true;
#pragma unroll
    for (int n = 0; n < 4; ++n) rv[n] = __builtin_amdgcn_raw_buffer_load_b128(rxb, xo + n * 1024 + pa * 16384, 0, 16);
#pragma unroll
    for (int n = 0; n < 4; ++n) ok &= (rv[n][0] != 0u) & (rv[n][1] != 0u) & (rv[n][2] != 0u) & (rv[n][3] != 0u);
    if (__all(ok)) break;
#pragma unroll 1
    for (; spins < SPIN_CAP; ++spins) {
      __builtin_amdgcn_s_sleep(XB_SLEEP);
      asm volatile("" ::: "memory");
      if (__builtin_amdgcn_readfirstlane(__builtin_amdgcn_raw_buffer_load_b32(rxb, wid * 4096 + 4092 + pa * 16384, 0, 16)) != 0u) break;
    }
    asm volatile("" ::: "memory");
  }
  STAMP(12);
#pragma unroll
  for (int n = 0; n < 4; ++n) {
    acc[0][n] += (f32x4){bflo(rv[n][0]), bfhi(rv[n][0]), bflo(rv[n][1]), bfhi(rv[n][1])};
    acc[1][n] += (f32x4){bflo(rv[n][2]), bfhi(rv[n][2]), bflo(rv[n][3]), bfhi(rv[n][3])};
  }
  {
    LAS char* tb = lds + wid * 8192;
#pragma unroll
    for (int n = 0; n < 4; ++n)
#pragma unroll
      for (int mi = 0; mi < 2; ++mi) *(LAS f32x4*)(tb + (16 * n + cl) * 128 + (((4 * mi + g) ^ (cl & 7)) * 16)) = acc[mi][n];
    const rsrc_t rout = mk_rsrc(a.out, S * D * 4);
#pragma unroll
    for (int k = 0; k < 8; ++k) {
      const u32x4 v = *(LAS u32x4*)(tb + k * 1024 + ln * 16);
      __builtin_amdgcn_raw_buffer_store_b128(v, rout, (unsigned)((t0 + 64 * wn + 8 * k + (ln >> 3)) * D + d0 + 64 * kh + 32 * wm) * 4u + (unsigned)(((ln & 7) ^ (ln >> 3)) * 16), 0, 16);
    }
  }
  STAMP(13);
}

extern "C" void kernel_launch(void* const* d_in, const int* in_sizes, int n_in, void* d_out, int out_size, void* d_ws, size_t ws_size,
                              hipStream_t stream) {
  (void)hipFuncSetAttribute((const void*)fused, hipFuncAttributeMaxDynamicSharedMemorySize, LDS_BYTES);
  Args a;
  a.x = (const u16*)d_in[0]; a.Lre = (const u16*)d_in[1]; a.Lim = (const u16*)d_in[2]; a.Bre = (const u16*)d_in[3]; a.Bim = (const u16*)d_in[4];
  a.Cre = (const u16*)d_in[5]; a.Cim = (const u16*)d_in[6]; a.logdt = (const u16*)d_in[7]; a.Dsk = (const u16*)d_in[8];
  a.w1 = (const u16*)d_in[10]; a.b1 = (const u16*)d_in[11]; a.w2 = (const u16*)d_in[12]; a.b2 = (const u16*)d_in[13];
  a.out = (float*)d_out; a.ws = (char*)d_ws;
  fused<<<256, 512, LDS_BYTES, stream>>>(a);
}
```

```cpp
#include <hip/hip_runtime.h>
#include <stdint.h>

typedef unsigned short u16;
typedef unsigned long long u64;
typedef __bf16 bf16x8 __attribute__((ext_vector_type(8)));
typedef short s16x4 __attribute__((ext_vector_type(4)));
typedef short s16x8 __attribute__((ext_vector_type(8)));
typedef float f32x2 __attribute__((ext_vector_type(2)));
typedef float f32x4 __attribute__((ext_vector_type(4)));
typedef unsigned u32x4 __attribute__((ext_vector_type(4)));
typedef unsigned u32x2 __attribute__((ext_vector_type(2)));
#define LAS __attribute__((address_space(3)))
#define GAS __attribute__((address_space(1)))
typedef GAS unsigned long long gu64;
typedef __amdgpu_buffer_rsrc_t rsrc_t;
#define RLX_AGENT __ATOMIC_RELAXED, __HIP_MEMORY_SCOPE_AGENT

constexpr int S = 2048, D = 1024, P = 1024, F = 3584;
constexpr size_t MB = 1024 * 1024;
constexpr size_t WS_HL = 4 * MB, WS_HT = 8 * MB, WS_U = 16 * MB, WS_XB = 24 * MB, WS_SYNC = 32 * MB;
constexpr int SY_LN = 64, SY_H = 320, SY_U = 448, SY_ST = 1024, SY_SB = 3072, SY_GR = 5120;
constexpr unsigned SYNC_BYTES = (SY_GR + 8 * P * 2) * 8;
constexpr unsigned SPIN_CAP = 1u << 17;
#define NBA 22
#define NAB 6
constexpr unsigned CODE_PF_BYTES = 40 * 1024;
constexpr unsigned long long CB_ORDER = 0x80F7E6D5C491B3A2ull;
#define XB_SLEEP 2
#define PF_NST 11
#define PF_ST0 0
#define PF_DELAY 24
#define PF_N0 2
#define PF_NST2 20
#define PF_WA 0
#define PF_BT 5
#define PF_WB 4
#define A1_RS 2

__device__ __forceinline__ float bf2f(u16 v) { return __uint_as_float(((unsigned)v) << 16); }
__device__ __forceinline__ float bflo(unsigned v) { return __uint_as_float(v << 16); }
__device__ __forceinline__ float bfhi(unsigned v) { return __uint_as_float(v & 0xffff0000u); }
__device__ __forceinline__ unsigned pk_bf16(float lo, float hi) {
  typedef __bf16 bf16x2 __attribute__((ext_vector_type(2)));
  f32x2 v = {lo, hi};
  bf16x2 r = __builtin_convertvector(v, bf16x2);
  return __builtin_bit_cast(unsigned, r);
}
__device__ __forceinline__ float rbf(float f) { return bflo(pk_bf16(f, f)); }
__device__ __forceinline__ float dot2(unsigned a, unsigned b, float c) {
  typedef __bf16 bf16x2 __attribute__((ext_vector_type(2)));
  return __builtin_amdgcn_fdot2_f32_bf16(__builtin_bit_cast(bf16x2, a), __builtin_bit_cast(bf16x2, b), c, false);
}
__device__ __forceinline__ f32x2 fma2(f32x2 a, f32x2 b, f32x2 c) { return __builtin_elementwise_fma(a, b, c); }
__device__ __forceinline__ f32x2 bc2(float v) { return (f32x2){v, v}; }

__device__ __forceinline__ rsrc_t mk_rsrc(const void* p, unsigned bytes) { return __builtin_amdgcn_make_buffer_rsrc((void*)p, 0, bytes, 0x00020000); }
__device__ __forceinline__ void dma16(rsrc_t rs, unsigned voff, unsigned soff, LAS char* l) { __builtin_amdgcn_raw_ptr_buffer_load_lds(rs, (LAS void*)l, 16, voff, soff, 0, 0); }
__device__ __forceinline__ void dma16c(rsrc_t rs, unsigned voff, unsigned soff, LAS char* l) { __builtin_amdgcn_raw_ptr_buffer_load_lds(rs, (LAS void*)l, 16, voff, soff, 0, 16); }
__device__ __forceinline__ void dma4(rsrc_t rs, unsigned voff, unsigned soff, LAS char* l) { __builtin_amdgcn_raw_ptr_buffer_load_lds(rs, (LAS void*)l, 4, voff, soff, 0, 0); }
__device__ __forceinline__ void st16_wt(rsrc_t rs, unsigned voff, unsigned soff, u32x4 v) { __builtin_amdgcn_raw_buffer_store_b128(v, rs, voff + soff, 0, 16); }
template <int N> __device__ __forceinline__ void wait_vm() { asm volatile("s_waitcnt vmcnt(%0)" ::"n"(N) : "memory"); }
__device__ __forceinline__ void wait_vm0_known() { asm volatile("" ::: "memory"); __builtin_amdgcn_s_waitcnt(0x0F70); asm volatile("" ::: "memory"); }
__device__ __forceinline__ void wait_lgkm0() { asm volatile("" ::: "memory"); __builtin_amdgcn_s_waitcnt(0xC07F); asm volatile("" ::: "memory"); }
__device__ __forceinline__ void bar() {
  asm volatile("" ::: "memory");
  __builtin_amdgcn_s_barrier();
  asm volatile("" ::: "memory");
}
#define SCHED_FENCE() __builtin_amdgcn_sched_barrier(0)
__device__ __forceinline__ bf16x8 lds_rd128(unsigned p) { return *(LAS bf16x8*)(size_t)p; }
__device__ __forceinline__ s16x4 lds_rdtr(unsigned p) { return __builtin_amdgcn_ds_read_tr16_b64_v4i16((LAS s16x4*)(size_t)p); }
struct TrFrag { s16x4 h[2]; };
__device__ __forceinline__ bf16x8 frag(const bf16x8& f) { return f; }
__device__ __forceinline__ bf16x8 frag(const TrFrag& f) { s16x8 ab = __builtin_shufflevector(f.h[0], f.h[1], 0, 1, 2, 3, 4, 5, 6, 7); return __builtin_bit_cast(bf16x8, ab); }
#define MFMA(a, b, c) __builtin_amdgcn_mfma_f32_16x16x32_bf16(frag(a), frag(b), (c), 0, 0, 0)
template <int NM, class FA, class FB, class Side>
__device__ __forceinline__ void phase(f32x4 (*acc)[4], const FA* fa, const FB* fb, Side side) {
#pragma unroll
  for (int i = 0; i < NM; ++i) {
    constexpr int MT = NM / 4;
    const int n_ = i / MT, r_ = i % MT, m_ = (n_ & 1) ? MT - 1 - r_ : r_;
    acc[m_][n_] = MFMA(fa[m_], fb[n_], acc[m_][n_]);
    side(i);
    SCHED_FENCE();
  }
}

template <int NL> __device__ __forceinline__ void wait_flags(gu64* p, bool active, u64 tag) {
#pragma unroll 1
  for (unsigned spins = 0; spins < SPIN_CAP; ++spins) {
    bool ok = true;
    if (active) {
#pragma unroll
      for (int k = 0; k < NL; ++k) ok &= __hip_atomic_load(p + 64 * k, RLX_AGENT) == tag;
    }
    if (__all(ok)) break;
    __builtin_amdgcn_s_sleep(2);
  }
  __builtin_amdgcn_fence(__ATOMIC_ACQUIRE, "wavefront");
}

template <int NL> __device__ __forceinline__ int flag_prefix(gu64* p, int n, int need, u64 tag, int lane) {
  int pre = 0;
#pragma unroll 1
  for (unsigned spins = 0; spins < SPIN_CAP; ++spins) {
    const u64 m0 = ~__ballot(lane >= n || __hip_atomic_load(p, RLX_AGENT) == tag);
    const u64 m1 = NL == 2 ? ~__ballot(__hip_atomic_load(p + 64, RLX_AGENT) == tag) : 0ull;
    pre = m0 ? __builtin_ctzll(m0) : m1 ? 64 + __builtin_ctzll(m1) : 128;
    if (pre >= need) break;
    __builtin_amdgcn_s_sleep(1);
  }
  __builtin_amdgcn_fence(__ATOMIC_ACQUIRE, "wavefront");
  return pre < need ? 128 : pre;
}
__device__ __forceinline__ void lds_wait(unsigned addr, unsigned want) {
#pragma unroll 1
  for (unsigned spins = 0; spins < (SPIN_CAP << 4); ++spins) {
    if (__builtin_amdgcn_readfirstlane(__hip_atomic_load((LAS unsigned*)(size_t)addr, __ATOMIC_RELAXED, __HIP_MEMORY_SCOPE_WORKGROUP)) >= want) break;
    __builtin_amdgcn_s_sleep(1);
  }
  asm volatile("" ::: "memory");
}
__device__ __forceinline__ void publish(unsigned cnt, gu64* flag, u64 tag, int lane) {
  wait_vm<0>();
  wait_vm0_known();
  wait_lgkm0();
  if (lane == 0 && __hip_atomic_fetch_add((LAS unsigned*)(size_t)cnt, 1u, __ATOMIC_RELAXED, __HIP_MEMORY_SCOPE_WORKGROUP) == 3u) __hip_atomic_store(flag, tag, RLX_AGENT);
}

template <class Issue> __device__ __forceinline__ void prod_fill(int n, Issue issue) {
#pragma unroll 1
  for (int t = 0; t < n; ++t) issue();
}
template <int L, int NS, class Issue> __device__ __forceinline__ void prod_run(int n, Issue issue) {
#pragma unroll 1
  for (int t = 0; t < n; ++t) { wait_vm<(NS - 2) * L>(); bar(); issue(); }
}
template <int L, int K, int X = 0> __device__ __forceinline__ void prod_tail() {
  wait_vm<K * L + X>();
  bar();
  if constexpr (K > 0) prod_tail<L, K - 1, X>();
}

__device__ __forceinline__ float wave_sum(float v) {
  v += __builtin_bit_cast(float, __builtin_amdgcn_update_dpp(0, __builtin_bit_cast(int, v), 0xB1, 0xf, 0xf, true));
  v += __builtin_bit_cast(float, __builtin_amdgcn_update_dpp(0, __builtin_bit_cast(int, v), 0x4E, 0xf, 0xf, true));
  v += __builtin_bit_cast(float, __builtin_amdgcn_update_dpp(0, __builtin_bit_cast(int, v), 0x141, 0xf, 0xf, true));
  v += __builtin_bit_cast(float, __builtin_amdgcn_update_dpp(0, __builtin_bit_cast(int, v), 0x140, 0xf, 0xf, true));
  const int iv = __builtin_bit_cast(int, v);
  const float a = __builtin_bit_cast(float, __builtin_amdgcn_readlane(iv, 0)), b = __builtin_bit_cast(float, __builtin_amdgcn_readlane(iv, 16));
  const float c = __builtin_bit_cast(float, __builtin_amdgcn_readlane(iv, 32)), d = __builtin_bit_cast(float, __builtin_amdgcn_readlane(iv, 48));
  return (a + b) + (c + d);
}

struct Args {
  const u16 *x, *Lre, *Lim, *Bre, *Bim, *Cre, *Cim, *logdt, *Dsk, *w1, *b1, *w2, *b2;
  float* out;
  char* ws;
};

constexpr int A1_NS = 3, A1_STAGE = 49152, A1_NT = D / 64;
constexpr int B1_NS = 7, B1_ABYTES = 32 * 448, B1_STAGE = B1_ABYTES + 32 * 256, B1_NT = S / 32;
constexpr int P2_NS = 4, P2_STAGE = 32768;
constexpr int LDS_BYTES = 160 * 1024;
constexpr int LDS_COEF = A1_NS * A1_STAGE;
constexpr int LDS_TOT = LDS_COEF + 14 * 256;
constexpr int LDS_SB = LDS_TOT + 512;
constexpr int LDS_STAT = LDS_SB + 512;
constexpr int LDS_RAW = LDS_STAT + 8 * 272;
constexpr int LDS_RES = P2_NS * P2_STAGE;
constexpr int LDS_U6 = LDS_COEF;
constexpr int LDS_HFL = LDS_COEF + 8192;
constexpr int LDS_RES2 = 158208;
constexpr int LDS_CNT = LDS_BYTES - 256 - 2048 - 64;
constexpr int LDS_FLG = LDS_BYTES - 256 - 2048;
constexpr int LDS_DUMMY = LDS_BYTES - 256;
static_assert(LDS_RAW + 3072 + 768 <= LDS_CNT && B1_NS * B1_STAGE <= LDS_CNT && LDS_RES + 16384 <= LDS_COEF && LDS_RAW + 3840 <= LDS_RES2 && LDS_RES2 + 1536 <= LDS_CNT && P2_NS * P2_STAGE <= LDS_COEF && LDS_RAW % 16 == 0, "lds");

constexpr int UA = NBA * 32, WA = PF_WA * 128 / 2, WB = PF_WB * 128 / 2;
constexpr int RA = (UA - WA - WB + 127) / 128, RB = (1792 - UA - WA - WB + 127) / 128;

__device__ __forceinline__ f32x2 gelu_tanh2(f32x2 x) {
  const f32x2 z = x * fma2(x * x, bc2(-0.10294323967f), bc2(-2.30220819813f));
  const f32x2 d = (f32x2){__builtin_amdgcn_exp2f(z.x), __builtin_amdgcn_exp2f(z.y)} + bc2(1.0f);
  return x * (f32x2){__builtin_amdgcn_rcpf(d.x), __builtin_amdgcn_rcpf(d.y)};
}
#define CMUL(or_, oi_, ar_, ai_, br_, bi_) do { const float t_ = (ar_) * (br_) - (ai_) * (bi_); oi_ = (ar_) * (bi_) + (ai_) * (br_); or_ = t_; } while (0)

__global__ __launch_bounds__(512, 2) void fused(Args a) {
  extern __shared__ __attribute__((aligned(16))) char smem[];
  LAS char* lds = (LAS char*)smem;
  u64 pc0;
  asm volatile("s_getpc_b64 %0" : "=s"(pc0));
  const int wid = __builtin_amdgcn_readfirstlane(threadIdx.x >> 6), lane = threadIdx.x & 63;
  const int bid = blockIdx.x, xc = bid & 7, idx = bid >> 3, xg = xc >> 1;
  const bool roleA = !(xc & 1);
  const int ri = bid >> 1;
  gu64* sy = (gu64*)(a.ws + WS_SYNC);
  u64 tag = 0;
#define LOAD_TAG() do { const __attribute__((address_space(4))) u64* ep_ = (const __attribute__((address_space(4))) u64*)(a.ws + WS_SYNC); \
    asm volatile("" : "+s"(ep_)::"memory"); tag = *ep_ + 1ull; } while (0)
#define HOLD_TAG() asm volatile("" ::"s"(tag))
#define tag32 ((unsigned)tag)
  const int chunk = 4 * (xg & 1) + (idx & 3), cb = 8 * (xg >> 1) + (idx >> 2);
  const int ft = 4 * xg + (idx & 3), dt1 = idx >> 2;
  const int tt = 4 * xg + (idx & 3), dt = idx >> 2;
  const int t0 = tt * 128, d0 = dt * 128;
  u64 cbs = roleA ? CB_ORDER : CB_ORDER >> (2 * (32 - NAB));
  const int nC = roleA ? 32 - NAB : NAB, ku0 = roleA ? 0 : NBA, nt2 = nC + (roleA ? NBA : 56 - NBA);
  const rsrc_t rsy = mk_rsrc(a.ws + WS_SYNC, SYNC_BYTES);

  auto touch = [&](const u16* base, unsigned bytes, int u0, int n) {
    const rsrc_t rs = mk_rsrc(base, bytes);
#pragma unroll 1
    for (int k = 0; k < n; ++k) dma4(rs, (unsigned)(lane * 128), (unsigned)(u0 + k) * 8192u, lds + LDS_DUMMY);
  };
  auto w1_voff = [&](int piece) {
    const int ch = piece * 64 + lane, k = ch / 28, pos = ch - k * 28;
    return (unsigned)(k * F + ft * 224 + (pos ^ (3 * ((k >> 3) & 1))) * 8) * 2u;
  };
  auto a1_vx = [&](int l) { return (unsigned)((chunk * 256 + 32 * (l >> 5) + 16 * ((l >> 3) & 1) + ((l >> 4) & 1)) * D * 2 + ((l & 7) ^ (l >> 4)) * 16); };
  auto a1_vb = [&](int l) { return (unsigned)((cb * 64 + (l >> 3)) * D * 2 + ((l & 7) ^ (l >> 4)) * 16); };
  if (roleA) {
    const rsrc_t rs = mk_rsrc(a.x, S * D * 2), rre = mk_rsrc(a.Bre, P * D * 2), rim = mk_rsrc(a.Bim, P * D * 2);
    const unsigned vxe = a1_vx(lane), vxo = (vxe ^ 64u) + 64 * D * 2, vbe = a1_vb(lane);
    const unsigned sx = (unsigned)((wid >> 2) * 128 + (wid & 3) * 4) * (D * 2), sbb = (unsigned)wid * (8 * D * 2), vb = (wid & 1) ? vbe ^ 64u : vbe;
#pragma unroll
    for (int t = 0; t < A1_NS - 1; ++t) {
#pragma unroll
      for (int j = 0; j < 4; ++j) dma16(rs, (j & 1) ? vxo : vxe, sx + (j >> 1) * (2 * D * 2) + t * 128, lds + t * A1_STAGE + wid * 4096 + j * 1024);
      dma16(rre, vb, sbb + t * 128, lds + t * A1_STAGE + 32768 + (wid >> 1) * 4096 + (wid & 1) * 1024);
      dma16(rim, vb, sbb + t * 128, lds + t * A1_STAGE + 32768 + (wid >> 1) * 4096 + (wid & 1) * 1024 + 2048);
    }
    if (wid == 7) {
      dma4(mk_rsrc((const void*)(pc0 & ~255ull), CODE_PF_BYTES), (unsigned)(lane * 128), (unsigned)((idx % 5) * 8192), lds + LDS_DUMMY);
      __builtin_amdgcn_s_sleep(PF_DELAY);
      touch(a.w1, S * F * 2, PF_N0 * ri, PF_N0);
    }
    LOAD_TAG();
  }
  int isB = xc & 1;
  asm volatile("" : "+s"(isB));
  if (isB) {
    const int row = ri * 16 + wid * 2;
    u32x4 r[2][2], brow[2][2];
    {
      const u16* xp = a.x + (size_t)row * D + lane * 8;
      const u16* bp = (ri < 64 ? a.Bre : a.Bim) + (size_t)(row & 1023) * D + lane * 8;
#pragma unroll
      for (int i = 0; i < 4; ++i) r[i >> 1][i & 1] = *(const u32x4*)(xp + i * 512);
#pragma unroll
      for (int i = 0; i < 4; ++i) brow[i >> 1][i & 1] = *(const u32x4*)(bp + i * 512);
    }
    LOAD_TAG();
    const rsrc_t rx = mk_rsrc(a.ws + WS_HL, S * D * 2);
#pragma unroll
    for (int pass = 0; pass < 2; ++pass) {
      float s1[2], s2[2];
#pragma unroll
      for (int i = 0; i < 2; ++i) {
        float a1 = 0.f, a2 = 0.f;
#pragma unroll
        for (int k = 0; k < 8; ++k) { const unsigned w = r[i][k >> 2][k & 3]; a1 = dot2(w, 0x3f803f80u, a1); a2 = dot2(w, w, a2); }
        s1[i] = a1; s2[i] = a2;
      }
#pragma unroll
      for (int i = 0; i < 2; ++i) { s1[i] = wave_sum(s1[i]); s2[i] = wave_sum(s2[i]); }
#pragma unroll
      for (int i = 0; i < 2; ++i) {
        const float mean = s1[i] * (1.0f / D), var = fmaxf(fmaf(-mean, mean, s2[i] * (1.0f / D)), 0.f);
        const unsigned ms = pk_bf16(mean, __builtin_amdgcn_sqrtf(rbf(rbf(var) + 1e-5f)));
        const float rs_ = __builtin_amdgcn_rcpf(bfhi(ms)), mb = bflo(ms);
#pragma unroll
        for (int k = 0; k < 8; ++k) {
          const unsigned w = r[i][k >> 2][k & 3];
          r[i][k >> 2][k & 3] = pk_bf16((bflo(w) - mb) * rs_, (bfhi(w) - mb) * rs_);
        }
        if (pass == 0) {
          if (lane == 0) __hip_atomic_store(sy + SY_ST + row + i, ((u64)tag32 << 32) | ms, RLX_AGENT);
        } else {
          st16_wt(rx, (unsigned)lane * 16u, (unsigned)((row + i) * D) * 2u, r[i][0]);
          st16_wt(rx, (unsigned)lane * 16u + 1024u, (unsigned)((row + i) * D) * 2u, r[i][1]);
        }
      }
    }
    asm volatile("" ::"v"(brow[0][0]), "v"(brow[0][1]), "v"(brow[1][0]), "v"(brow[1][1]));
    wait_vm<0>();
    bar();
    if (wid == 0 && lane == 0) __hip_atomic_store(sy + SY_LN + ri, tag, RLX_AGENT);
    if (wid != 6) {
      const rsrc_t rs = mk_rsrc(a.w1, S * F * 2);
      const int p0_ = 2 * (wid == 7 ? 6 : wid);
      const unsigned v0 = w1_voff(p0_), v1 = w1_voff(p0_ + 1);
#pragma unroll
      for (int t = 0; t < B1_NS - 1; ++t) {
        dma16(rs, v0, t * (32 * F * 2), lds + t * B1_STAGE + p0_ * 1024);
        dma16(rs, v1, t * (32 * F * 2), lds + t * B1_STAGE + p0_ * 1024 + 1024);
      }
    }
    {
      float sums[2];
#pragma unroll
      for (int i = 0; i < 2; ++i) {
        float s = 0.f;
#pragma unroll
        for (int k = 0; k < 8; ++k) { const unsigned w = brow[i][k >> 2][k & 3]; s = dot2(w, 0x3f803f80u, s); }
        sums[i] = wave_sum(s);
      }
      if (lane < 2) __hip_atomic_store(sy + SY_SB + row + lane, ((u64)tag32 << 32) | __float_as_uint(lane ? sums[1] : sums[0]), RLX_AGENT);
    }
  }

  if (wid >= 4) {
    const int pw = wid - 4;
    int lane = __builtin_amdgcn_mbcnt_hi(~0u, __builtin_amdgcn_mbcnt_lo(~0u, 0u));
    asm volatile("" : "+v"(lane));
    if (pw == 3) {
      auto flags_fetch = [&]() {
        dma16c(rsy, (unsigned)lane * 16u, SY_LN * 8, lds + LDS_FLG);
        dma16c(rsy, (unsigned)lane * 16u, SY_LN * 8 + 1024, lds + LDS_FLG + 1024);
      };
      auto flags_check = [&]() {
        bool ok = true;
#pragma unroll
        for (int k = 0; k < 4; ++k) ok &= *(LAS u64*)(lds + LDS_FLG + k * 512 + lane * 8) == tag;
        if (!__all(ok)) wait_flags<4>(sy + SY_LN + lane, true, tag);
      };
      auto xb_clear = [&](int k0) {
        const rsrc_t rxb = mk_rsrc(a.ws + WS_XB, 256 * 16384);
#pragma unroll
        for (int k = 0; k < 4; ++k) st16_wt(rxb, (unsigned)(lane * 16 + k * 1024), (unsigned)(bid * 16384 + k0 * 4096), (u32x4){0u, 0u, 0u, 0u});
      };
      if (lane < 4) *(LAS unsigned*)(size_t)(LDS_CNT + lane * 4) = 0u;
      if (roleA) {
        st16_wt(rsy, (unsigned)lane * 16u, (unsigned)(SY_GR * 8 + (chunk * P + cb * 64) * 16), (u32x4){0u, 0u, 0u, 0u});
        dma4(mk_rsrc(a.Lre, P * 2), (unsigned)(lane & 31) * 4u, cb * 128, lds + LDS_RAW + 3072);
        dma4(mk_rsrc(a.Lim, P * 2), (unsigned)(lane & 31) * 4u, cb * 128, lds + LDS_RAW + 3072 + 256);
        dma4(mk_rsrc(a.logdt, P * 2), (unsigned)(lane & 31) * 4u, cb * 128, lds + LDS_RAW + 3072 + 512);
        HOLD_TAG();
        wait_vm<5 + PF_N0>();
        const int bst = PF_ST0 + ((ri * PF_NST) >> 7);
        auto step = [&](int t) { bar(); if (t == bst) touch(a.w1, S * F * 2, 128 * PF_N0 + (14 - PF_N0) * ri, 14 - PF_N0); };
        step(0); step(1); step(2);
        if (bst <= 2) wait_vm<14 - PF_N0>(); else wait_vm<0>();
        if (lane == 0) __hip_atomic_store(sy + SY_LN + 128 + ri, tag, RLX_AGENT);
        LAS float* cf = (LAS float*)(lds + LDS_COEF) + lane;
        const float lr = bf2f(*(LAS u16*)(lds + LDS_RAW + 3072 + lane * 2)), li = bf2f(*(LAS u16*)(lds + LDS_RAW + 3072 + 256 + lane * 2));
        const float dtt = rbf(expf(bf2f(*(LAS u16*)(lds + LDS_RAW + 3072 + 512 + lane * 2)))), er = expf(dtt * lr);
        float arg = dtt * li;
        asm volatile("" : "+v"(arg));
        step(3);
        float sn, cs;
        sincosf(arg, &sn, &cs);
        float pr = er * cs, pi = er * sn;
        asm volatile("" : "+v"(pr), "+v"(pi));
        step(4);
        const float nr = pr - 1.0f, den = lr * lr + li * li;
        cf[0] = pr; cf[64] = pi;
        cf[128] = 2.0f * (nr * lr + pi * li) / den; cf[192] = 2.0f * (pi * lr - nr * li) / den;
        step(5);
#pragma unroll
        for (int k = 0; k < 8; ++k) {
          const float t = pr * pr - pi * pi; pi = 2.0f * pr * pi; pr = t;
          if (k >= 3) { cf[(4 + 2 * (k - 3)) * 64] = pr; cf[(5 + 2 * (k - 3)) * 64] = pi; }
        }
#pragma unroll 1
        for (int t = 6; t < 12; ++t) { step(t); if (t < 10) xb_clear(t - 6); }
        const unsigned sbo = (unsigned)(SY_SB * 8 + cb * 512 + (lane >> 5) * (P * 8) + (lane & 31) * 16);
#pragma unroll 1
        for (unsigned spins = 0;; ++spins) {
          dma16c(rsy, (unsigned)lane * 16u, (unsigned)(SY_ST * 8 + chunk * 2048), lds + LDS_RAW);
          dma16c(rsy, (unsigned)lane * 16u, (unsigned)(SY_ST * 8 + chunk * 2048 + 1024), lds + LDS_RAW + 1024);
          dma16c(rsy, sbo, 0, lds + LDS_RAW + 2048);
          if (spins == 0) { flags_fetch(); step(12); step(13); }
          wait_vm<0>();
          u32x2 gs[4], gb[2];
          bool ok = true;
#pragma unroll
          for (int k = 0; k < 4; ++k) { gs[k] = *(LAS u32x2*)(lds + LDS_RAW + k * 512 + lane * 8); ok &= gs[k][1] == tag32; }
#pragma unroll
          for (int k = 0; k < 2; ++k) { gb[k] = *(LAS u32x2*)(lds + LDS_RAW + 2048 + k * 512 + lane * 8); ok &= gb[k][1] == tag32; }
          if (__all(ok) || spins > SPIN_CAP) {
#pragma unroll
            for (int k = 0; k < 4; ++k) {
              LAS float* e = (LAS float*)(lds + LDS_STAT + (2 * k + (lane >> 5)) * 272 + (lane & 15) * 16 + ((lane >> 4) & 1) * 4);
              e[0] = -bflo(gs[k][0]); e[2] = __builtin_amdgcn_rcpf(bfhi(gs[k][0]));
            }
#pragma unroll
            for (int k = 0; k < 2; ++k) *(LAS unsigned*)(lds + LDS_SB + k * 256 + lane * 4) = gb[k][0];
            break;
          }
          __builtin_amdgcn_s_sleep(2);
        }
        touch(a.Cre, D * P * 2, 2 * ri, 2); touch(a.Cim, D * P * 2, 2 * ri, 2);
        touch(a.w2, F * S * 2, UA + (PF_WA / 2) * ri, PF_WA / 2); touch(a.w2, F * S * 2, (PF_WA / 2) * ri, PF_WA / 2);
        bar();
        flags_check();
        wait_lgkm0();
        step(15);
      } else {
        wait_vm<0>();
        dma4(mk_rsrc((const void*)(pc0 & ~255ull), CODE_PF_BYTES), (unsigned)(lane * 128), (unsigned)((idx % 5) * 8192), lds + LDS_DUMMY);
#pragma unroll 1
        for (int t = 0; t < 13; ++t) { bar(); if (t >= 4 && !(t & 1) && t < 12) xb_clear((t - 4) >> 1); }
        flags_fetch();
#pragma unroll 1
        for (int t = 13; t < 20; ++t) bar();
        wait_vm<0>();
        flags_check();
        if (bid == 255 && lane == 0) __hip_atomic_store(sy, tag, RLX_AGENT);
#pragma unroll 1
        for (int t = 20; t < B1_NT - PF_BT; ++t) bar();
        touch(a.w2, F * S * 2, UA + WA + (PF_WB / 2) * ri, PF_WB / 2); touch(a.w2, F * S * 2, WA + (PF_WB / 2) * ri, PF_WB / 2);
#pragma unroll 1
        for (int t = 0; t < PF_BT; ++t) bar();
      }
    } else if (roleA) {
      if (pw < 2) {
        const rsrc_t rs = mk_rsrc(a.x, S * D * 2);
        const unsigned vxe = a1_vx(lane), vxo = (vxe ^ 64u) + 64 * D * 2;
        unsigned so = (A1_NS - 1) * 128 + pw * (128 * D * 2), lo = (A1_NS - 1) * A1_STAGE + pw * 16384;
        auto issue = [&]() {
#pragma unroll
          for (int j = 0; j < 16; ++j) dma16(rs, (j & 1) ? vxo : vxe, so + (j >> 1) * (2 * D * 2), lds + lo + j * 1024);
          so += 128; lo = lo >= (A1_NS - 1) * A1_STAGE ? lo - (A1_NS - 1) * A1_STAGE : lo + A1_STAGE;
        };
        HOLD_TAG();
        wait_vm<0>();
        prod_run<16, A1_NS>(A1_NT - (A1_NS - 1), issue);
        prod_tail<16, A1_NS - 2>();
      } else {
        const unsigned ve = a1_vb(lane), vo = ve ^ 64u;
        const rsrc_t rre = mk_rsrc(a.Bre, P * D * 2), rim = mk_rsrc(a.Bim, P * D * 2);
        unsigned so = (A1_NS - 1) * 128, lo = (A1_NS - 1) * A1_STAGE + 32768;
        auto issue = [&]() {
#pragma unroll
          for (int j = 0; j < 8; ++j) {
            dma16(rre, (j & 1) ? vo : ve, so + j * (8 * D * 2), lds + lo + (j >> 1) * 4096 + (j & 1) * 1024);
            dma16(rim, (j & 1) ? vo : ve, so + j * (8 * D * 2), lds + lo + (j >> 1) * 4096 + (j & 1) * 1024 + 2048);
          }
          so += 128; lo = lo >= (A1_NS - 1) * A1_STAGE ? lo - (A1_NS - 1) * A1_STAGE : lo + A1_STAGE;
        };
        HOLD_TAG();
        wait_vm<0>();
        prod_run<16, A1_NS>(A1_NT - (A1_NS - 1), issue);
        prod_tail<16, A1_NS - 2>();
      }
    } else {
      const int dd0 = dt1 * 128;
      if (pw < 2) {
        unsigned voff[7];
#pragma unroll
        for (int j = 0; j < 7; ++j) voff[j] = w1_voff(pw * 7 + j);
        const rsrc_t rs = mk_rsrc(a.w1, S * F * 2);
        unsigned so = (B1_NS - 1) * (32 * F * 2), lo = (B1_NS - 1) * B1_STAGE + pw * 7168;
        auto issue = [&]() {
#pragma unroll
          for (int j = 0; j < 7; ++j) dma16(rs, voff[j], so, lds + lo + j * 1024);
          so += 32 * F * 2; lo = lo >= (B1_NS - 1) * B1_STAGE ? lo - (B1_NS - 1) * B1_STAGE : lo + B1_STAGE;
        };
        wait_vm<0>();
        prod_run<7, B1_NS>(B1_NT - (B1_NS - 1), issue);
        prod_tail<7, B1_NS - 2>();
      } else {
        unsigned voff[2];
#pragma unroll
        for (int j = 0; j < 2; ++j) {
          const int k = j * 8 + (lane >> 4), pos = lane & 15;
          const int c = pos ^ (((k & 3) << 1) | (((k >> 3) & 1) << 3));
          voff[j] = (unsigned)((lane >> 4) * D + dd0 + c * 8) * 2u;
        }
        const rsrc_t rs = mk_rsrc(a.ws + WS_HL, S * D * 2);
        unsigned so = 0, lo = B1_ABYTES;
        auto issue = [&]() {
#pragma unroll
          for (int j = 0; j < 8; ++j) dma16c(rs, voff[(j >> 1) & 1], so + j * 4 * (D * 2), lds + lo + j * 1024);
          so += 32 * D * 2; lo = lo >= (B1_NS - 1) * B1_STAGE ? lo - (B1_NS - 1) * B1_STAGE : lo + B1_STAGE;
        };
        int done = B1_NS + 3;
        wait_flags<1>(sy + SY_LN + lane, lane < 2 * done, tag);
        prod_fill(B1_NS - 1, issue);
        prod_run<8, B1_NS>(1, issue);
        dma16c(rsy, (unsigned)lane * 16u, SY_LN * 8, lds + LDS_RES2);
        prod_run<8, B1_NS>(3, issue);
        wait_vm<24>();
        const u32x4 fl = *(LAS u32x4*)(lds + LDS_RES2 + lane * 16);
        int pre = __all(fl[0] == tag32 && fl[2] == tag32 && fl[1] == (unsigned)(tag >> 32) && fl[3] == (unsigned)(tag >> 32)) ? 128 : 2 * done;
#pragma unroll 1
        while (pre < 128 && done < B1_NT) {
          const int n = pre / 2 - done;
          prod_run<8, B1_NS>(n, issue);
          done += n;
          wait_vm<(B1_NS - 2) * 8>();
          bar();
          pre = flag_prefix<2>(sy + SY_LN + lane, 64, 2 * done + 2, tag, lane);
          issue();
          ++done;
        }
        prod_run<8, B1_NS>(B1_NT - done, issue);
        prod_tail<8, B1_NS - 2>();
      }
    }
    bar();
    const int nU = nt2 - nC;
    if (pw == 1 || pw == 2) {
      const rsrc_t rxx = mk_rsrc(a.x, S * D * 2);
      const int dk = d0 + (roleA ? 0 : 64);
#pragma unroll
      for (int k = 0; k < 8; ++k) dma16(rxx, (unsigned)((lane >> 3) * (D * 2) + ((lane & 7) ^ (lane >> 3)) * 16), (unsigned)((t0 + 64 * (pw - 1) + 8 * k) * D + dk) * 2u, lds + LDS_RES + (8 * (pw - 1) + k) * 1024);
      if (pw == 1) dma16c(rsy, (unsigned)lane * 16u, (unsigned)(SY_ST + t0) * 8u, lds + LDS_RES2);
      else { dma4(mk_rsrc(a.b2, S * 2), (unsigned)lane * 4u, (unsigned)t0 * 2u, lds + LDS_RES2 + 1024); dma4(mk_rsrc(a.Dsk, D * 2), (unsigned)lane * 4u, (unsigned)dk * 2u, lds + LDS_RES2 + 1280); }
      wait_vm<0>();
      if (lane == 0) __hip_atomic_fetch_add((LAS unsigned*)(size_t)(LDS_CNT + 12), 1u, __ATOMIC_RELAXED, __HIP_MEMORY_SCOPE_WORKGROUP);
    }
    if (pw == 0 || pw == 3) {
      const int dh = d0 + (((pw ? 1 : 0) ^ (roleA ? 0 : 1)) << 6), r4 = lane >> 4;
      const unsigned vce = (unsigned)(dh + (lane >> 3)) * (P * 2) + (unsigned)(((lane & 7) ^ (lane >> 4)) * 16), vco = vce ^ 64u;
      const unsigned vu = (unsigned)((dh + (lane >> 2)) * 64 + (((lane & 3) ^ (((r4 & 1) << 1) ^ ((r4 >> 1) * 3))) * 16));
      const rsrc_t ru = mk_rsrc(a.ws + WS_U, D * F * 2);
      rsrc_t rc = mk_rsrc(a.Cre, D * P * 2), rc2 = mk_rsrc(a.Cim, D * P * 2);
      unsigned so = 0, lo = pw ? 8192 : 0, sh = 0;
      auto issueC = [&]() {
        so = ((unsigned)cbs & 15u) * 128;
#pragma unroll
        for (int j = 0; j < 8; ++j) dma16(rc, (j & 1) ? vco : vce, so + j * 8 * (P * 2), lds + lo + j * 1024);
        const rsrc_t t_ = rc; rc = rc2; rc2 = t_;
        cbs >>= sh; sh ^= 4; lo = (lo + P2_STAGE) & (P2_NS * P2_STAGE - 1);
      };
      auto issueU = [&]() {
#pragma unroll
        for (int j = 0; j < 8; ++j) dma16c(ru, vu, so + (j & 1) * (D * 64) + (j >> 1) * 1024, lds + lo + j * 1024);
        so += 2 * D * 64; lo = (lo + P2_STAGE) & (P2_NS * P2_STAGE - 1);
      };
      prod_fill(P2_NS - 1, issueC);
      if (roleA) {
        const int bst = 1 + ((ri * PF_NST2) >> 7);
        prod_run<8, P2_NS>(bst, issueC);
        if (pw == 3) { touch(a.w2, F * S * 2, UA + WA + WB + RB * ri, RB); touch(a.w2, F * S * 2, WA + WB + RA * ri, RA); }
        prod_run<8, P2_NS>(nC - 6 - bst, issueC);
        dma16c(rsy, (unsigned)(lane & 7) * 16u, (unsigned)(SY_U + dt * 16) * 8u, lds + LDS_FLG + (pw ? 1024 : 0));
        prod_run<8, P2_NS>(3, issueC);
      } else {
        prod_run<8, P2_NS>(nC - 3, issueC);
      }
      so = ku0 * (2 * D * 64);
      constexpr int F0 = 2 * NBA / 7;
      const int f0 = roleA ? 0 : F0;
      gu64* uf = sy + SY_U + dt * 16 + ((f0 + lane) & 15);
      LAS char* ufl = lds + LDS_FLG + (pw ? 1024 : 0);
      int done = 0, pre = 0;
      bool look = roleA;
#pragma unroll 1
      while (pre < 128 && done < nU) {
        wait_vm<16>();
        bar();
        const int need = roleA ? 16 : (2 * (ku0 + done) + 1) / 7 + 1 - f0;
        int p2 = 0;
        if (look) {
          const u64 m = ~__ballot(lane >= 16 - f0 || *(LAS u64*)(ufl + ((f0 + lane) & 15) * 8) == tag);
          p2 = m ? __builtin_ctzll(m) : 128;
        }
        pre = p2 >= need ? p2 : flag_prefix<1>(uf, 16 - f0, need, tag, lane);
        issueU();
        ++done;
        if (pre < 128) {
          const int n = ((7 * (f0 + pre)) >> 1) - ku0 - done;
          look = n >= 3;
          prod_run<8, P2_NS>(look ? n - 3 : n, issueU);
          if (look) {
            dma16c(rsy, (unsigned)(lane & 7) * 16u, (unsigned)(SY_U + dt * 16) * 8u, ufl);
            prod_run<8, P2_NS>(3, issueU);
          }
          done += n;
        }
      }
      prod_run<8, P2_NS>(nU - done, issueU);
    } else {
      unsigned voff[2];
#pragma unroll
      for (int j = 0; j < 2; ++j) {
        const int k = j * 8 + (lane >> 4), pos = lane & 15;
        const int c = pos ^ (((k & 3) << 1) | (((k >> 3) & 1) << 3));
        voff[j] = (unsigned)(((pw - 1) * 32 + (lane >> 4)) * S + t0 + c * 8) * 2u;
      }
      const unsigned vh = (unsigned)(lane * 16 + (pw - 1) * 8192);
      const rsrc_t rh = mk_rsrc(a.ws + WS_HT, 2 * P * S * 2), rw = mk_rsrc(a.w2, F * S * 2);
      unsigned so = 0, lo = 16384 + (pw - 1) * 8192, sh = 0;
      const int nf = nC / 2 + (roleA ? 0 : 1);
      const int hfi = (int)((unsigned)(cbs >> (4 * (lane & 15))) & 15u);
      gu64* hf = sy + SY_H + (lane == nC / 2 ? chunk * 16 + cb : (tt >> 1) * 16 + hfi);
      auto issueH = [&]() {
        so = (tt * 32 + sh * 4 + ((unsigned)cbs & 15u)) * 16384;
#pragma unroll
        for (int j = 0; j < 8; ++j) dma16c(rh, vh, so + j * 1024, lds + lo + j * 1024);
        cbs >>= sh; sh ^= 4; lo = (lo + P2_STAGE) & (P2_NS * P2_STAGE - 1);
      };
      auto issueW = [&]() {
#pragma unroll
        for (int j = 0; j < 8; ++j) dma16(rw, voff[(j >> 1) & 1], so + j * 4 * (S * 2), lds + lo + j * 1024);
        so += 64 * S * 2; lo = (lo + P2_STAGE) & (P2_NS * P2_STAGE - 1);
      };
      if (roleA) lds_wait(LDS_CNT, 4u);
      int done = P2_NS - 1, pre = flag_prefix<1>(hf, nf, 2, tag, lane);
      prod_fill(P2_NS - 1, issueH);
#pragma unroll 1
      while (pre < 128 && done < nC) {
        const int n = (2 * pre < nC ? 2 * pre : nC) - done;
        const bool look = n >= 3;
        prod_run<8, P2_NS>(look ? n - 3 : n, issueH);
        if (look) {
          dma16c(rsy, (unsigned)(lane & 7) * 16u, (unsigned)(SY_H + (tt >> 1) * 16) * 8u, lds + LDS_HFL + (pw - 1) * 1024);
          prod_run<8, P2_NS>(3, issueH);
        }
        done += n;
        if (done < nC) {
          wait_vm<16>();
          bar();
          int p2 = 0;
          if (look) {
            const u64 m = ~__ballot(lane >= nf || (lane < nC / 2 && *(LAS u64*)(lds + LDS_HFL + (pw - 1) * 1024 + hfi * 8) == tag));
            p2 = m ? __builtin_ctzll(m) : 128;
          }
          pre = p2 > done / 2 ? p2 : flag_prefix<1>(hf, nf, done / 2 + 1, tag, lane);
          issueH();
          ++done;
        }
      }
      prod_run<8, P2_NS>(nC - done, issueH);
      so = ku0 * (64 * S * 2);
      if (pre < 128) (void)flag_prefix<1>(hf, nf, nf, tag, lane);
      prod_run<8, P2_NS>(nU, issueW);
    }
    prod_tail<8, P2_NS - 2>();
    return;
  }

  const int wm = wid >> 1, wn = wid & 1;
  int cl = lane & 15, g = lane >> 4, q4 = cl >> 2, p4 = lane & 3, ln = lane;
#define RELANE() do { ln = __builtin_amdgcn_mbcnt_hi(~0u, __builtin_amdgcn_mbcnt_lo(~0u, 0u)); asm volatile("" : "+v"(ln)); cl = ln & 15; g = ln >> 4; q4 = cl >> 2; p4 = ln & 3; } while (0)
  const int kh = roleA ? 0 : 1, pa = bid ^ 1;
  const rsrc_t rxb = mk_rsrc(a.ws + WS_XB, 256 * 16384);
  f32x4 res[2][4];
  auto residual = [&]() {
    lds_wait(LDS_CNT + 12, 2u);
#pragma unroll
    for (int n = 0; n < 4; ++n) {
      const int tl = 64 * wn + 16 * n + cl;
      const unsigned ms = *(LAS unsigned*)(lds + LDS_RES2 + tl * 8);
      const float rs_ = __builtin_amdgcn_rcpf(bfhi(ms)), mb = bflo(ms), b2 = bf2f(*(LAS u16*)(lds + LDS_RES2 + 1024 + tl * 2));
#pragma unroll
      for (int mi = 0; mi < 2; ++mi) {
        const int c8 = 4 * wm + 2 * mi + (g >> 1);
        const u32x2 xv = *(LAS u32x2*)(lds + LDS_RES + tl * 128 + ((c8 ^ (tl & 7)) * 16) + (g & 1) * 8);
        const u32x2 dk = *(LAS u32x2*)(lds + LDS_RES2 + 1280 + c8 * 16 + (g & 1) * 8);
#pragma unroll
        for (int h = 0; h < 2; ++h) {
          const float x0 = bflo(xv[h]), x1 = bfhi(xv[h]);
          const unsigned xn = pk_bf16((x0 - mb) * rs_, (x1 - mb) * rs_);
          res[mi][n][2 * h] = fmaf(bflo(xn), bflo(dk[h]), x0 + b2); res[mi][n][2 * h + 1] = fmaf(bfhi(xn), bfhi(dk[h]), x1 + b2);
        }
        asm volatile("" : "+v"(res[mi][n]));
      }
    }
  };

  if (roleA) {
    HOLD_TAG();
    RELANE();
    const int p0 = cb * 64;
    const unsigned rb = (unsigned)(cl * 128 + ((g ^ (cl >> 1)) * 16));
    unsigned pa0 = rb + wm * 16384, pa1 = pa0 ^ 64u, pb0 = rb + 32768 + wn * 8192, pb1 = pb0 ^ 64u;
    f32x4 acc[8][4];
#pragma unroll
    for (int m = 0; m < 8; ++m)
#pragma unroll
      for (int n = 0; n < 4; ++n) acc[m][n] = (f32x4){0.f, 0.f, 0.f, 0.f};
    bf16x8 xa[8], xb[4], ya[8], yb[4];
    auto rd = [&](bf16x8* fa, bf16x8* fb, unsigned pa_, unsigned pb_, int r) {
      if (r == 0) fa[0] = lds_rd128(pa_); else if (r < 5) fb[r - 1] = lds_rd128(pb_ + (r - 1) * 2048); else fa[r - 4] = lds_rd128(pa_ + (r - 4) * 2048);
    };
    int slot = 0, dl = 0;
    wait_vm0_known();
    bar();
#pragma unroll
    for (int r = 0; r < 12; ++r) rd(xa, xb, pa0, pb0, r);
#pragma unroll 1
    for (int t = 0; t < A1_NT; ++t) {
      SCHED_FENCE(); wait_lgkm0(); SCHED_FENCE();
      phase<32>(acc, xa, xb, [&](int i) {
        if (i % A1_RS == 0 && i / A1_RS < 12) rd(ya, yb, pa1, pb1, i / A1_RS);
        if (i == 25) { dl = (slot == A1_NS - 1) ? -(A1_NS - 1) * A1_STAGE : A1_STAGE; slot = (slot == A1_NS - 1) ? 0 : slot + 1; }
        if (i == 27) { pa0 += dl; pb0 += dl; asm volatile("" : "+v"(pa0), "+v"(pb0)); }
        if (i == 29) { pa1 += dl; pb1 += dl; asm volatile("" : "+v"(pa1), "+v"(pb1)); }
      });
      wait_lgkm0();
      bar();
      SCHED_FENCE();
      phase<32>(acc, ya, yb, [&](int i) { if (i % A1_RS == 0 && i / A1_RS < 12) rd(xa, xb, pa0, pb0, i / A1_RS); });
    }
    RELANE();
    float cf[2][14], sb[4];
#pragma unroll
    for (int q = 0; q < 2; ++q) {
#pragma unroll
      for (int k = 0; k < 14; ++k) cf[q][k] = ((LAS float*)(lds + LDS_COEF))[k * 64 + 32 * wn + 16 * q + cl];
      sb[2 * q] = ((LAS float*)(lds + LDS_SB))[32 * wn + 16 * q + cl]; sb[2 * q + 1] = ((LAS float*)(lds + LDS_SB))[64 + 32 * wn + 16 * q + cl];
    }
    f32x2 er[2], ei[2];
#pragma unroll
    for (int q = 0; q < 2; ++q) { er[q] = bc2(0.f); ei[q] = bc2(0.f); }
#pragma unroll
    for (int m = 0; m < 8; ++m)
#pragma unroll
      for (int jj = 0; jj < 2; ++jj) {
        const f32x4 st = *(LAS f32x4*)(lds + LDS_STAT + (4 * wm + g) * 272 + (2 * m + jj) * 16);
        const f32x2 c2 = {st[0], st[1]}, r2 = {st[2], st[3]};
#pragma unroll
        for (int q = 0; q < 2; ++q) {
          const f32x2 ur = fma2(c2, bc2(sb[2 * q]), (f32x2){acc[m][2 * q][2 * jj], acc[m][2 * q][2 * jj + 1]}) * r2;
          const f32x2 ui = fma2(c2, bc2(sb[2 * q + 1]), (f32x2){acc[m][2 * q + 1][2 * jj], acc[m][2 * q + 1][2 * jj + 1]}) * r2;
          acc[m][2 * q][2 * jj] = ur.x; acc[m][2 * q][2 * jj + 1] = ur.y; acc[m][2 * q + 1][2 * jj] = ui.x; acc[m][2 * q + 1][2 * jj + 1] = ui.y;
          const f32x2 nr = fma2(bc2(cf[q][0]), er[q], fma2(bc2(-cf[q][1]), ei[q], ur));
          const f32x2 ni = fma2(bc2(cf[q][0]), ei[q], fma2(bc2(cf[q][1]), er[q], ui));
          er[q] = nr; ei[q] = ni;
        }
        if (jj == 1 && (m & 1)) asm volatile("" : "+v"(er[0]), "+v"(ei[0]), "+v"(er[1]), "+v"(ei[1]));
      }
    float sr[2], si[2], pr_[2], pi_[2];
    LAS float* tot = (LAS float*)(lds + LDS_TOT);
#pragma unroll
    for (int q = 0; q < 2; ++q) {
      float tr, ti;
      CMUL(tr, ti, cf[q][4], cf[q][5], er[q].x, ei[q].x);
      sr[q] = tr + er[q].y; si[q] = ti + ei[q].y;
      float vr = __shfl_up(sr[q], 16), vi = __shfl_up(si[q], 16);
      CMUL(tr, ti, cf[q][6], cf[q][7], vr, vi);
      if (g >= 1) { sr[q] += tr; si[q] += ti; }
      vr = __shfl_up(sr[q], 32); vi = __shfl_up(si[q], 32);
      CMUL(tr, ti, cf[q][8], cf[q][9], vr, vi);
      if (g >= 2) { sr[q] += tr; si[q] += ti; }
      vr = __shfl_up(sr[q], 16); vi = __shfl_up(si[q], 16);
      pr_[q] = g >= 1 ? vr : 0.f; pi_[q] = g >= 1 ? vi : 0.f;
      if (wm == 0 && g == 3) *(LAS f32x2*)(tot + (32 * wn + 16 * q + cl) * 2) = (f32x2){sr[q], si[q]};
    }
    if (wm == 0) {
      wait_lgkm0();
      if (ln == 0) __hip_atomic_store((LAS unsigned*)(size_t)(LDS_CNT + 4 + 4 * wn), 1u, __ATOMIC_RELAXED, __HIP_MEMORY_SCOPE_WORKGROUP);
    } else {
      lds_wait(LDS_CNT + 4 + 4 * wn, 1u);
    }
    f32x2 tq[2];
#pragma unroll
    for (int q = 0; q < 2; ++q) {
      tq[q] = *(LAS f32x2*)(tot + (32 * wn + 16 * q + cl) * 2);
      if (wm == 1 && g == 3 && chunk < 7) {
        float tr, ti;
        CMUL(tr, ti, cf[q][10], cf[q][11], tq[q].x, tq[q].y);
        st16_wt(rsy, (unsigned)(SY_GR * 8 + (chunk * P + p0 + 32 * wn + 16 * q + cl) * 16), 0, (u32x4){__float_as_uint(tr + sr[q]), 1u, __float_as_uint(ti + si[q]), 1u});
      }
    }
#pragma unroll
    for (int m = 0; m < 8; ++m)
#pragma unroll
      for (int q = 0; q < 2; ++q) {
        const f32x4 ur = acc[m][2 * q], ui = acc[m][2 * q + 1];
        acc[m][2 * q] = ur * cf[q][2] - ui * cf[q][3];
        acc[m][2 * q + 1] = ui * (-cf[q][2]) - ur * cf[q][3];
        asm volatile("" : "+v"(acc[m][2 * q]), "+v"(acc[m][2 * q + 1]));
      }
    float hr[2] = {0.f, 0.f}, hi[2] = {0.f, 0.f};
    if (chunk > 0) {
      float gr[2][2], gi[2][2];
#pragma unroll 1
      for (unsigned spins = 0;; ++spins) {
        bool ok = true;
#pragma unroll
        for (int q = 0; q < 2; ++q)
#pragma unroll
          for (int k = 0; k < 2; ++k) {
            const int cc = (g + 4 * k < chunk) ? g + 4 * k : 0;
            const u32x4 v = __builtin_amdgcn_raw_buffer_load_b128(rsy, (unsigned)(SY_GR * 8 + (cc * P + p0 + 32 * wn + 16 * q + cl) * 16), 0, 16);
            ok &= (v[1] == 1u) & (v[3] == 1u);
            gr[q][k] = __uint_as_float(v[0]); gi[q][k] = __uint_as_float(v[2]);
          }
        if (__all(ok) || spins > SPIN_CAP) break;
        __builtin_amdgcn_s_sleep(1);
        asm volatile("" ::: "memory");
      }
#pragma unroll
      for (int q = 0; q < 2; ++q) {
        float ar_ = 0.f, ai_ = 0.f;
#pragma unroll
        for (int k = 0; k < 2; ++k) {
          float vr = gr[q][k], vi = gi[q][k];
          const int e = chunk - 1 - (g + 4 * k);
#pragma unroll
          for (int i = 0; i < 6 - 4 * k; ++i) {
            float tr, ti;
            CMUL(tr, ti, cf[q][12], cf[q][13], vr, vi);
            vr = i < e ? tr : vr; vi = i < e ? ti : vi;
          }
          ar_ += e >= 0 ? vr : 0.f; ai_ += e >= 0 ? vi : 0.f;
        }
        ar_ += __shfl_xor(ar_, 16); ai_ += __shfl_xor(ai_, 16);
        ar_ += __shfl_xor(ar_, 32); ai_ += __shfl_xor(ai_, 32);
        hr[q] = ar_; hi[q] = ai_;
      }
    }
    f32x2 wr[2], wi[2];
#pragma unroll
    for (int q = 0; q < 2; ++q) {
      float tr, ti;
      if (wm == 1) { CMUL(tr, ti, cf[q][10], cf[q][11], hr[q], hi[q]); hr[q] = tr + tq[q].x; hi[q] = ti + tq[q].y; }
      CMUL(tr, ti, cf[q][6], cf[q][7], hr[q], hi[q]);
      if (g & 1) { hr[q] = tr; hi[q] = ti; }
      CMUL(tr, ti, cf[q][8], cf[q][9], hr[q], hi[q]);
      if (g & 2) { hr[q] = tr; hi[q] = ti; }
      hr[q] += pr_[q]; hi[q] += pi_[q];
      CMUL(tr, ti, cf[q][4], cf[q][5], hr[q], hi[q]);
      const f32x2 h0r = {hr[q], tr + er[q].x}, h0i = {hi[q], ti + ei[q].x};
      wr[q] = h0r * cf[q][2] - h0i * cf[q][3];
      wi[q] = h0i * (-cf[q][2]) - h0r * cf[q][3];
    }
    LAS char* scr = lds + wid * P2_STAGE + 16384;
    const unsigned fsw = (unsigned)(((cl & 3) << 1) | (((cl >> 3) & 1) << 3));
    const rsrc_t rh = mk_rsrc(a.ws + WS_HT, 2 * P * S * 2);
#pragma unroll
    for (int q = 0; q < 2; ++q) {
      const f32x2 ar2 = bc2(cf[q][0]), ai2 = bc2(cf[q][1]);
#pragma unroll
      for (int mh = 0; mh < 2; ++mh) {
        u32x4 o[2][2];
#pragma unroll
        for (int mq = 0; mq < 4; ++mq) {
          const int m = 4 * mh + mq;
          const f32x2 u0r = {acc[m][2 * q][0], acc[m][2 * q][1]}, u0i = {acc[m][2 * q + 1][0], acc[m][2 * q + 1][1]};
          const f32x2 u1r = {acc[m][2 * q][2], acc[m][2 * q][3]}, u1i = {acc[m][2 * q + 1][2], acc[m][2 * q + 1][3]};
          const f32x2 n0r = fma2(ar2, wr[q], fma2(ai2, wi[q], u0r)), n0i = fma2(ar2, wi[q], fma2(-ai2, wr[q], u0i));
          const f32x2 n1r = fma2(ar2, n0r, fma2(ai2, n0i, u1r)), n1i = fma2(ar2, n0i, fma2(-ai2, n0r, u1i));
          wr[q] = n1r; wi[q] = n1i;
          o[0][0][mq] = pk_bf16(n0r.x, n1r.x); o[0][1][mq] = pk_bf16(n0r.y, n1r.y);
          o[1][0][mq] = pk_bf16(n0i.x, n1i.x); o[1][1][mq] = pk_bf16(n0i.y, n1i.y);
        }
#pragma unroll
        for (int part = 0; part < 2; ++part)
#pragma unroll
          for (int run = 0; run < 2; ++run)
            *(LAS u32x4*)(scr + (part * 32 + 16 * q + cl) * 256 + (((unsigned)(4 * g + 2 * run + mh) ^ fsw) * 16)) = o[part][run];
      }
#pragma unroll
      for (int i = 0; i < 8; ++i) {
        const int part = i >> 2, pc = 4 * q + (i & 3);
        const u32x4 v = *(LAS u32x4*)(scr + part * 8192 + pc * 1024 + ln * 16);
        st16_wt(rh, (unsigned)(ln * 16 + pc * 1024), (unsigned)(((2 * chunk + wm) * 32 + part * 16 + cb) * 16384 + wn * 8192), v);
      }
      SCHED_FENCE();
    }
  } else {
    RELANE();
    const int f0 = ft * 224, dd0 = dt1 * 128;
    const int k0 = 8 * g + q4;
    unsigned pa0 = (unsigned)(k0 * 448 + ((12 * wm + p4) ^ (3 * (g & 1))) * 16 + (p4 & 1) * 8);
    unsigned pa1 = (unsigned)(k0 * 448 + ((12 * wm + p4) ^ (3 * (g & 1))) * 16 + ((p4 & 1) ^ 1) * 8);
    unsigned pa2 = (unsigned)(k0 * 448 + ((24 + 2 * wm + (p4 >> 1)) ^ (3 * (g & 1))) * 16 + (p4 & 1) * 8);
    unsigned pb[4];
#pragma unroll
    for (int n = 0; n < 4; ++n) pb[n] = (unsigned)(B1_ABYTES + k0 * 256 + ((8 * wn + 2 * n + (p4 >> 1)) ^ ((q4 << 1) | ((g & 1) << 3))) * 16 + (p4 & 1) * 8);
    f32x4 acc[7][4];
#pragma unroll
    for (int m = 0; m < 7; ++m) {
      const u32x2 bv = *(const u32x2*)(a.b1 + f0 + (m < 6 ? 96 * wm + 32 * (m >> 1) + 8 * g + 4 * ((m & 1) ^ (g & 1)) : 192 + 16 * wm + 4 * g));
#pragma unroll
      for (int n = 0; n < 4; ++n) acc[m][n] = (f32x4){bflo(bv[0]), bfhi(bv[0]), bflo(bv[1]), bfhi(bv[1])};
    }
    TrFrag xa[7], xb[4], ya[7], yb[4];
    int slot = 0, dl = 0;
    auto side = [&](TrFrag* fa, TrFrag* fb, int i) {
      const int f = i >> 1, h = i & 1;
      if (f == 0) fa[0].h[h] = lds_rdtr(pa0 + h * 1792);
      else if (f < 5) fb[f - 1].h[h] = lds_rdtr(pb[f - 1] + h * 1024);
      else if (f < 10) fa[f - 4].h[h] = lds_rdtr((((f - 4) & 1) ? pa1 : pa0) + ((f - 4) >> 1) * 64 + h * 1792);
      else if (f == 10) fa[6].h[h] = lds_rdtr(pa2 + h * 1792);
      else if (i == 22) { dl = (slot == B1_NS - 1) ? -(B1_NS - 1) * B1_STAGE : B1_STAGE; slot = (slot == B1_NS - 1) ? 0 : slot + 1; }
      else if (i == 23) { pa0 += dl; pb[0] += dl; asm volatile("" : "+v"(pa0), "+v"(pb[0])); }
      else if (i == 24) { pb[1] += dl; pb[2] += dl; asm volatile("" : "+v"(pb[1]), "+v"(pb[2])); }
      else if (i == 25) { pb[3] += dl; pa1 += dl; asm volatile("" : "+v"(pb[3]), "+v"(pa1)); }
      else if (i == 26) { pa2 += dl; asm volatile("" : "+v"(pa2)); }
    };
    wait_vm0_known();
    bar();
#pragma unroll
    for (int i = 0; i < 28; ++i) side(xa, xb, i);
#pragma unroll 1
    for (int t = 0; t < B1_NT; t += 2) {
      SCHED_FENCE(); wait_lgkm0(); bar(); SCHED_FENCE();
      phase<28>(acc, xa, xb, [&](int i) { side(ya, yb, i); });
      wait_lgkm0(); bar(); SCHED_FENCE();
      phase<28>(acc, ya, yb, [&](int i) { side(xa, xb, i); });
    }
    RELANE();
    const rsrc_t ru = mk_rsrc(a.ws + WS_U, D * F * 2);
    auto act4 = [&](const f32x4& v) {
      const f32x2 v0 = gelu_tanh2((f32x2){v[0], v[1]}), v1 = gelu_tanh2((f32x2){v[2], v[3]});
      return (u32x2){pk_bf16(v0.x, v0.y), pk_bf16(v1.x, v1.y)};
    };
#pragma unroll
    for (int n = 0; n < 4; ++n) *(LAS u32x2*)(lds + LDS_U6 + (64 * wn + 16 * n + cl) * 64 + 32 * wm + 8 * g) = act4(acc[6][n]);
    wait_lgkm0();
    if (ln == 0) __hip_atomic_fetch_add((LAS unsigned*)(size_t)(LDS_CNT + 4 + 4 * wn), 1u, __ATOMIC_RELAXED, __HIP_MEMORY_SCOPE_WORKGROUP);
#pragma unroll
    for (int n = 0; n < 4; ++n) {
      const unsigned vo = (unsigned)((dd0 + 64 * wn + 16 * n + cl) * 64);
#pragma unroll
      for (int mm = 0; mm < 3; ++mm) {
        const u32x2 w0 = act4(acc[2 * mm][n]), w1 = act4(acc[2 * mm + 1][n]);
        const bool sw = g & 1;
        const u32x2 lo = sw ? w1 : w0, hi = sw ? w0 : w1;
        st16_wt(ru, vo + 16 * g, (unsigned)((ft * 7 + 3 * wm + mm) * D * 64), (u32x4){lo[0], lo[1], hi[0], hi[1]});
      }
      if (n == 1) {
        lds_wait(LDS_CNT + 4 + 4 * wn, 2u);
#pragma unroll
        for (int k = 0; k < 2; ++k) {
          const unsigned o = (unsigned)(wn * 4096 + wm * 2048 + k * 1024 + ln * 16);
          st16_wt(ru, o, (unsigned)(((ft * 7 + 6) * D + dd0) * 64), *(LAS u32x4*)(lds + LDS_U6 + o));
        }
      }
    }
    residual();
  }

  publish(LDS_CNT, sy + (roleA ? SY_H + chunk * 16 + cb : SY_U + dt1 * 16 + ft), tag, ln);
  if (roleA) residual();

  f32x4 acc[4][4];
#pragma unroll
  for (int n = 0; n < 4; ++n) { acc[0][n] = res[0][n]; acc[1][n] = res[1][n]; acc[2][n] = (f32x4){0.f, 0.f, 0.f, 0.f}; acc[3][n] = (f32x4){0.f, 0.f, 0.f, 0.f}; }
  const unsigned au0 = (unsigned)(wm * 4096 + cl * 64 + ((g ^ (((q4 & 1) << 1) ^ ((q4 >> 1) * 3))) * 16));
  unsigned pa0 = (unsigned)((wm * 32 + cl) * 128 + ((g ^ (cl >> 1)) * 16)), pa1 = pa0 ^ 64u;
  const unsigned sw0 = pa0 ^ au0 ^ 65536u, sw1 = pa1 ^ (au0 + 1024u) ^ 65536u;
  unsigned pb[4];
#pragma unroll
  for (int n = 0; n < 4; ++n) pb[n] = (unsigned)(16384 + (8 * g + q4) * 256 + ((8 * wn + 2 * n + (p4 >> 1)) ^ ((q4 << 1) | ((g & 1) << 3))) * 16 + (p4 & 1) * 8);
  bf16x8 xa[4], ya[4];
  TrFrag xb[4], yb[4];
  static_assert(P2_NS == 4 && NAB % 2 == 0 && NBA % 2 == 0 && NAB >= P2_NS && NAB <= 16 && PF_NST2 <= 26 - NAB, "phase-2 loop: two stages per trip, 64-KB windows");
  auto rd = [&](bf16x8* fa, TrFrag* fb, int so, int ks, int r) {
    const unsigned pa_ = (ks ? pa1 : pa0) + so;
    if (r == 0) fa[0] = lds_rd128(pa_);
    else if (r < 9) fb[(r - 1) >> 1].h[(r - 1) & 1] = lds_rdtr(pb[(r - 1) >> 1] + so + ks * 8192 + ((r - 1) & 1) * 1024);
    else if (r < 12) fa[r - 8] = lds_rd128(pa_ + ((r - 8) >> 1) * 8192 + ((r - 8) & 1) * 2048);
  };
  bar();
#pragma unroll
  for (int r = 0; r < 12; ++r) rd(xa, xb, 0, 0, r);
#pragma unroll 1
  for (int t = 0; t < nt2; t += 2) {
    unsigned x0, x1;
    SCHED_FENCE(); wait_lgkm0(); SCHED_FENCE();
    phase<16>(acc, xa, xb, [&](int i) { rd(ya, yb, 0, 1, i); });
    wait_lgkm0(); bar(); SCHED_FENCE();
    phase<16>(acc, ya, yb, [&](int i) {
      rd(xa, xb, P2_STAGE, 0, i);
      if (i == 13) { const bool sw = (t + 2 == nC); x0 = sw ? sw0 : 65536u; x1 = sw ? sw1 : 65536u; asm volatile("" : "+v"(x0), "+v"(x1)); }
      if (i == 14) { pa0 ^= x0; asm volatile("" : "+v"(pa0)); }
    });
    wait_lgkm0(); SCHED_FENCE();
    phase<16>(acc, xa, xb, [&](int i) {
      rd(ya, yb, P2_STAGE, 1, i);
      if (i == 12) { pb[0] ^= 65536u; pb[1] ^= 65536u; asm volatile("" : "+v"(pb[0]), "+v"(pb[1])); }
      if (i == 13) { pb[2] ^= 65536u; pb[3] ^= 65536u; asm volatile("" : "+v"(pb[2]), "+v"(pb[3])); }
      if (i == 14) { pa1 ^= x1; asm volatile("" : "+v"(pa1)); }
    });
    wait_lgkm0(); bar(); SCHED_FENCE();
    phase<16>(acc, ya, yb, [&](int i) { rd(xa, xb, 0, 0, i); });
  }
  const unsigned xo = (unsigned)(wid * 4096 + ln * 16);
#pragma unroll
  for (int n = 0; n < 4; ++n) {
    u32x4 w = {pk_bf16(acc[2][n][0], acc[2][n][1]), pk_bf16(acc[2][n][2], acc[2][n][3]), pk_bf16(acc[3][n][0], acc[3][n][1]), pk_bf16(acc[3][n][2], acc[3][n][3])};
#pragma unroll
    for (int k = 0; k < 4; ++k) w[k] = w[k] ? w[k] : 0x80000000u;
    st16_wt(rxb, xo + n * 1024, bid * 16384, w);
  }
  u32x4 rv[4];
#pragma unroll 1
  for (unsigned spins = 0; spins < SPIN_CAP; ++spins) {
    bool ok = true;
#pragma unroll
    for (int n = 0; n < 4; ++n) rv[n] = __builtin_amdgcn_raw_buffer_load_b128(rxb, xo + n * 1024 + pa * 16384, 0, 16);
#pragma unroll
    for (int n = 0; n < 4; ++n) ok &= (rv[n][0] != 0u) & (rv[n][1] != 0u) & (rv[n][2] != 0u) & (rv[n][3] != 0u);
    if (__all(ok)) break;
#pragma unroll 1
    for (; spins < SPIN_CAP; ++spins) {
      __builtin_amdgcn_s_sleep(XB_SLEEP);
      asm volatile("" ::: "memory");
      if (__builtin_amdgcn_readfirstlane(__builtin_amdgcn_raw_buffer_load_b32(rxb, wid * 4096 + 4092 + pa * 16384, 0, 16)) != 0u) break;
    }
    asm volatile("" ::: "memory");
  }
#pragma unroll
  for (int n = 0; n < 4; ++n) {
    acc[0][n] += (f32x4){bflo(rv[n][0]), bfhi(rv[n][0]), bflo(rv[n][1]), bfhi(rv[n][1])};
    acc[1][n] += (f32x4){bflo(rv[n][2]), bfhi(rv[n][2]), bflo(rv[n][3]), bfhi(rv[n][3])};
  }
  {
    LAS char* tb = lds + wid * 8192;
#pragma unroll
    for (int n = 0; n < 4; ++n)
#pragma unroll
      for (int mi = 0; mi < 2; ++mi) *(LAS f32x4*)(tb + (16 * n + cl) * 128 + (((4 * mi + g) ^ (cl & 7)) * 16)) = acc[mi][n];
    const rsrc_t rout = mk_rsrc(a.out, S * D * 4);
#pragma unroll
    for (int k = 0; k < 8; ++k) {
      const u32x4 v = *(LAS u32x4*)(tb + k * 1024 + ln * 16);
      __builtin_amdgcn_raw_buffer_store_b128(v, rout, (unsigned)((t0 + 64 * wn + 8 * k + (ln >> 3)) * D + d0 + 64 * kh + 32 * wm) * 4u + (unsigned)(((ln & 7) ^ (ln >> 3)) * 16), 0, 16);
    }
  }
}

extern "C" void kernel_launch(void* const* d_in, const int* in_sizes, int n_in, void* d_out, int out_size, void* d_ws, size_t ws_size,
                              hipStream_t stream) {
  (void)hipFuncSetAttribute((const void*)fused, hipFuncAttributeMaxDynamicSharedMemorySize, LDS_BYTES);
  Args a;
  a.x = (const u16*)d_in[0]; a.Lre = (const u16*)d_in[1]; a.Lim = (const u16*)d_in[2]; a.Bre = (const u16*)d_in[3]; a.Bim = (const u16*)d_in[4];
  a.Cre = (const u16*)d_in[5]; a.Cim = (const u16*)d_in[6]; a.logdt = (const u16*)d_in[7]; a.Dsk = (const u16*)d_in[8];
  a.w1 = (const u16*)d_in[10]; a.b1 = (const u16*)d_in[11]; a.w2 = (const u16*)d_in[12]; a.b2 = (const u16*)d_in[13];
  a.out = (float*)d_out; a.ws = (char*)d_ws;
  fused<<<256, 512, LDS_BYTES, stream>>>(a);
}
```

```cpp
#include <hip/hip_runtime.h>
#include <stdint.h>

typedef unsigned short u16;
typedef unsigned long long u64;
typedef __bf16 bf16x8 __attribute__((ext_vector_type(8)));
typedef short s16x4 __attribute__((ext_vector_type(4)));
typedef short s16x8 __attribute__((ext_vector_type(8)));
typedef float f32x2 __attribute__((ext_vector_type(2)));
typedef float f32x4 __attribute__((ext_vector_type(4)));
typedef unsigned u32x4 __attribute__((ext_vector_type(4)));
typedef unsigned u32x2 __attribute__((ext_vector_type(2)));
#define LAS __attribute__((address_space(3)))
#define GAS __attribute__((address_space(1)))
typedef GAS unsigned long long gu64;
typedef __amdgpu_buffer_rsrc_t rsrc_t;
#define RLX_AGENT __ATOMIC_RELAXED, __HIP_MEMORY_SCOPE_AGENT

constexpr int S = 2048, D = 1024, P = 1024, F = 3584;
constexpr size_t MB = 1024 * 1024;
constexpr size_t WS_HL = 4 * MB, WS_HT = 8 * MB, WS_U = 16 * MB, WS_XB = 24 * MB, WS_SYNC = 32 * MB;
constexpr int SY_LN = 64, SY_H = 320, SY_U = 448, SY_ST = 1024, SY_SB = 3072, SY_GR = 5120;
constexpr unsigned SYNC_BYTES = (SY_GR + 8 * P * 2) * 8;
constexpr unsigned SPIN_CAP = 1u << 17;
#define NBA 28
#define NAB 6
constexpr unsigned CODE_PF_BYTES = 40 * 1024;
constexpr unsigned long long CB_ORDER = 0x80F7E6D5C491B3A2ull;
#define XB_SLEEP 2
#define LEAD 8
#define PF_NST 11
#define PF_ST0 0
#define PF_DELAY 24
#define PF_N1 0
#define PF_N2 7
#define PF_W2A 0
#define PF_N0 2
#define PF_NST2 20
#define PF_WA 0
#define PF_BT 5
#define PF_WB 4
#define A1_RS 2

__device__ __forceinline__ float bf2f(u16 v) { return __uint_as_float(((unsigned)v) << 16); }
__device__ __forceinline__ float bflo(unsigned v) { return __uint_as_float(v << 16); }
__device__ __forceinline__ float bfhi(unsigned v) { return __uint_as_float(v & 0xffff0000u); }
__device__ __forceinline__ unsigned pk_bf16(float lo, float hi) {
  typedef __bf16 bf16x2 __attribute__((ext_vector_type(2)));
  f32x2 v = {lo, hi};
  bf16x2 r = __builtin_convertvector(v, bf16x2);
  return __builtin_bit_cast(unsigned, r);
}
__device__ __forceinline__ float rbf(float f) { return bflo(pk_bf16(f, f)); }
__device__ __forceinline__ float dot2(unsigned a, unsigned b, float c) {
  typedef __bf16 bf16x2 __attribute__((ext_vector_type(2)));
  return __builtin_amdgcn_fdot2_f32_bf16(__builtin_bit_cast(bf16x2, a), __builtin_bit_cast(bf16x2, b), c, false);
}
__device__ __forceinline__ f32x2 fma2(f32x2 a, f32x2 b, f32x2 c) { return __builtin_elementwise_fma(a, b, c); }
__device__ __forceinline__ f32x2 bc2(float v) { return (f32x2){v, v}; }

__device__ __forceinline__ rsrc_t mk_rsrc(const void* p, unsigned bytes) { return __builtin_amdgcn_make_buffer_rsrc((void*)p, 0, bytes, 0x00020000); }
__device__ __forceinline__ void dma16(rsrc_t rs, unsigned voff, unsigned soff, LAS char* l) { __builtin_amdgcn_raw_ptr_buffer_load_lds(rs, (LAS void*)l, 16, voff, soff, 0, 0); }
__device__ __forceinline__ void dma16c(rsrc_t rs, unsigned voff, unsigned soff, LAS char* l) { __builtin_amdgcn_raw_ptr_buffer_load_lds(rs, (LAS void*)l, 16, voff, soff, 0, 16); }
__device__ __forceinline__ void dma4(rsrc_t rs, unsigned voff, unsigned soff, LAS char* l) { __builtin_amdgcn_raw_ptr_buffer_load_lds(rs, (LAS void*)l, 4, voff, soff, 0, 0); }
__device__ __forceinline__ void st16_wt(rsrc_t rs, unsigned voff, unsigned soff, u32x4 v) { __builtin_amdgcn_raw_buffer_store_b128(v, rs, voff + soff, 0, 16); }
template <int N> __device__ __forceinline__ void wait_vm() { asm volatile("s_waitcnt vmcnt(%0)" ::"n"(N) : "memory"); }
__device__ __forceinline__ void wait_vm0_known() { asm volatile("" ::: "memory"); __builtin_amdgcn_s_waitcnt(0x0F70); asm volatile("" ::: "memory"); }
__device__ __forceinline__ void wait_lgkm0() { asm volatile("" ::: "memory"); __builtin_amdgcn_s_waitcnt(0xC07F); asm volatile("" ::: "memory"); }
__device__ __forceinline__ void bar() {
  asm volatile("" ::: "memory");
  __builtin_amdgcn_s_barrier();
  asm volatile("" ::: "memory");
}
#define SCHED_FENCE() __builtin_amdgcn_sched_barrier(0)
__device__ __forceinline__ bf16x8 lds_rd128(unsigned p) { return *(LAS bf16x8*)(size_t)p; }
__device__ __forceinline__ s16x4 lds_rdtr(unsigned p) { return __builtin_amdgcn_ds_read_tr16_b64_v4i16((LAS s16x4*)(size_t)p); }
struct TrFrag { s16x4 h[2]; };
__device__ __forceinline__ bf16x8 frag(const bf16x8& f) { return f; }
__device__ __forceinline__ bf16x8 frag(const TrFrag& f) { s16x8 ab = __builtin_shufflevector(f.h[0], f.h[1], 0, 1, 2, 3, 4, 5, 6, 7); return __builtin_bit_cast(bf16x8, ab); }
#define MFMA(a, b, c) __builtin_amdgcn_mfma_f32_16x16x32_bf16(frag(a), frag(b), (c), 0, 0, 0)
template <int NM, class FA, class FB, class Side>
__device__ __forceinline__ void phase(f32x4 (*acc)[4], const FA* fa, const FB* fb, Side side) {
#pragma unroll
  for (int i = 0; i < NM; ++i) {
    constexpr int MT = NM / 4;
    const int n_ = i / MT, r_ = i % MT, m_ = (n_ & 1) ? MT - 1 - r_ : r_;
    acc[m_][n_] = MFMA(fa[m_], fb[n_], acc[m_][n_]);
    side(i);
    SCHED_FENCE();
  }
}

template <int NL> __device__ __forceinline__ void wait_flags(gu64* p, bool active, u64 tag) {
#pragma unroll 1
  for (unsigned spins = 0; spins < SPIN_CAP; ++spins) {
    bool ok = true;
    if (active) {
#pragma unroll
      for (int k = 0; k < NL; ++k) ok &= __hip_atomic_load(p + 64 * k, RLX_AGENT) == tag;
    }
    if (__all(ok)) break;
    __builtin_amdgcn_s_sleep(2);
  }
  __builtin_amdgcn_fence(__ATOMIC_ACQUIRE, "wavefront");
}

template <int NL> __device__ __forceinline__ int flag_prefix(gu64* p, int n, int need, u64 tag, int lane) {
  int pre = 0;
#pragma unroll 1
  for (unsigned spins = 0; spins < SPIN_CAP; ++spins) {
    const u64 m0 = ~__ballot(lane >= n || __hip_atomic_load(p, RLX_AGENT) == tag);
    const u64 m1 = NL == 2 ? ~__ballot(__hip_atomic_load(p + 64, RLX_AGENT) == tag) : 0ull;
    pre = m0 ? __builtin_ctzll(m0) : m1 ? 64 + __builtin_ctzll(m1) : 128;
    if (pre >= need) break;
    __builtin_amdgcn_s_sleep(1);
  }
  __builtin_amdgcn_fence(__ATOMIC_ACQUIRE, "wavefront");
  return pre < need ? 128 : pre;
}
__device__ __forceinline__ void lds_wait(unsigned addr, unsigned want) {
#pragma unroll 1
  for (unsigned spins = 0; spins < (SPIN_CAP << 4); ++spins) {
    if (__builtin_amdgcn_readfirstlane(__hip_atomic_load((LAS unsigned*)(size_t)addr, __ATOMIC_RELAXED, __HIP_MEMORY_SCOPE_WORKGROUP)) >= want) break;
    __builtin_amdgcn_s_sleep(1);
  }
  asm volatile("" ::: "memory");
}
__device__ __forceinline__ void publish(unsigned cnt, gu64* flag, u64 tag, int lane) {
  wait_vm<0>();
  wait_vm0_known();
  wait_lgkm0();
  if (lane == 0 && __hip_atomic_fetch_add((LAS unsigned*)(size_t)cnt, 1u, __ATOMIC_RELAXED, __HIP_MEMORY_SCOPE_WORKGROUP) == 3u) __hip_atomic_store(flag, tag, RLX_AGENT);
}

template <class Issue> __device__ __forceinline__ void prod_fill(int n, Issue issue) {
#pragma unroll 1
  for (int t = 0; t < n; ++t) issue();
}
template <int L, int NS, class Issue> __device__ __forceinline__ void prod_run(int n, Issue issue) {
#pragma unroll 1
  for (int t = 0; t < n; ++t) { wait_vm<(NS - 2) * L>(); bar(); issue(); }
}
template <int L, int K, int X = 0> __device__ __forceinline__ void prod_tail() {
  wait_vm<K * L + X>();
  bar();
  if constexpr (K > 0) prod_tail<L, K - 1, X>();
}

__device__ __forceinline__ float wave_sum(float v) {
  v += __builtin_bit_cast(float, __builtin_amdgcn_update_dpp(0, __builtin_bit_cast(int, v), 0xB1, 0xf, 0xf, true));
  v += __builtin_bit_cast(float, __builtin_amdgcn_update_dpp(0, __builtin_bit_cast(int, v), 0x4E, 0xf, 0xf, true));
  v += __builtin_bit_cast(float, __builtin_amdgcn_update_dpp(0, __builtin_bit_cast(int, v), 0x141, 0xf, 0xf, true));
  v += __builtin_bit_cast(float, __builtin_amdgcn_update_dpp(0, __builtin_bit_cast(int, v), 0x140, 0xf, 0xf, true));
  const int iv = __builtin_bit_cast(int, v);
  const float a = __builtin_bit_cast(float, __builtin_amdgcn_readlane(iv, 0)), b = __builtin_bit_cast(float, __builtin_amdgcn_readlane(iv, 16));
  const float c = __builtin_bit_cast(float, __builtin_amdgcn_readlane(iv, 32)), d = __builtin_bit_cast(float, __builtin_amdgcn_readlane(iv, 48));
  return (a + b) + (c + d);
}

struct Args {
  const u16 *x, *Lre, *Lim, *Bre, *Bim, *Cre, *Cim, *logdt, *Dsk, *w1, *b1, *w2, *b2;
  float* out;
  char* ws;
};

constexpr int A1_NS = 3, A1_STAGE = 49152, A1_NT = D / 64;
constexpr int B1_NS = 7, B1_ABYTES = 32 * 448, B1_STAGE = B1_ABYTES + 32 * 256, B1_NT = S / 32;
constexpr int P2_NS = 4, P2_STAGE = 32768;
constexpr int LDS_BYTES = 160 * 1024;
constexpr int LDS_COEF = A1_NS * A1_STAGE;
constexpr int LDS_TOT = LDS_COEF + 14 * 256;
constexpr int LDS_SB = LDS_TOT + 512;
constexpr int LDS_STAT = LDS_SB + 512;
constexpr int LDS_RAW = LDS_STAT + 8 * 272;
constexpr int LDS_RES = P2_NS * P2_STAGE;
constexpr int LDS_U6 = LDS_COEF;
constexpr int LDS_HFL = LDS_COEF + 8192;
constexpr int LDS_RES2 = 158208;
constexpr int LDS_CNT = LDS_BYTES - 256 - 2048 - 64;
constexpr int LDS_FLG = LDS_BYTES - 256 - 2048;
constexpr int LDS_DUMMY = LDS_BYTES - 256;
static_assert(LDS_RAW + 3072 + 768 <= LDS_CNT && B1_NS * B1_STAGE <= LDS_CNT && LDS_RES + 16384 <= LDS_COEF && LDS_RAW + 3840 <= LDS_RES2 && LDS_RES2 + 1536 <= LDS_CNT && P2_NS * P2_STAGE <= LDS_COEF && LDS_RAW % 16 == 0, "lds");

constexpr int UA = NBA * 32, WA = PF_WA * 128 / 2, WB = PF_WB * 128 / 2;
constexpr int RA = (UA - WA - WB + 127) / 128, RB = (1792 - UA - WA - WB + 127) / 128;

__device__ __forceinline__ f32x2 gelu_tanh2(f32x2 x) {
  const f32x2 z = x * fma2(x * x, bc2(-0.10294323967f), bc2(-2.30220819813f));
  const f32x2 d = (f32x2){__builtin_amdgcn_exp2f(z.x), __builtin_amdgcn_exp2f(z.y)} + bc2(1.0f);
  return x * (f32x2){__builtin_amdgcn_rcpf(d.x), __builtin_amdgcn_rcpf(d.y)};
}
#define CMUL(or_, oi_, ar_, ai_, br_, bi_) do { const float t_ = (ar_) * (br_) - (ai_) * (bi_); oi_ = (ar_) * (bi_) + (ai_) * (br_); or_ = t_; } while (0)

__global__ __launch_bounds__(512, 2) void fused(Args a) {
  extern __shared__ __attribute__((aligned(16))) char smem[];
  LAS char* lds = (LAS char*)smem;
  u64 pc0;
  asm volatile("s_getpc_b64 %0" : "=s"(pc0));
  const int wid = __builtin_amdgcn_readfirstlane(threadIdx.x >> 6), lane = threadIdx.x & 63;
  const int bid = blockIdx.x, xc = bid & 7, idx = bid >> 3, xg = xc >> 1;
  const bool roleA = !(xc & 1);
  const int ri = bid >> 1;
  gu64* sy = (gu64*)(a.ws + WS_SYNC);
  u64 tag = 0;
#define LOAD_TAG() do { const __attribute__((address_space(4))) u64* ep_ = (const __attribute__((address_space(4))) u64*)(a.ws + WS_SYNC); \
    asm volatile("" : "+s"(ep_)::"memory"); tag = *ep_ + 1ull; } while (0)
#define HOLD_TAG() asm volatile("" ::"s"(tag))
#define tag32 ((unsigned)tag)
  const int chunk = 4 * (xg & 1) + (idx & 3), cb = 8 * (xg >> 1) + (idx >> 2);
  const int ft = 4 * xg + (idx & 3), dt1 = idx >> 2;
  const int tt = 4 * xg + (idx & 3), dt = idx >> 2;
  const int t0 = tt * 128, d0 = dt * 128;
  u64 cbs = roleA ? CB_ORDER : CB_ORDER >> (2 * (32 - NAB));
  const int nC = roleA ? 32 - NAB : NAB, ku0 = roleA ? 0 : NBA, nt2 = nC + (roleA ? NBA : 56 - NBA);
  const rsrc_t rsy = mk_rsrc(a.ws + WS_SYNC, SYNC_BYTES);

  auto touch = [&](const u16* base, unsigned bytes, int u0, int n) {
    const rsrc_t rs = mk_rsrc(base, bytes);
#pragma unroll 1
    for (int k = 0; k < n; ++k) dma4(rs, (unsigned)(lane * 128), (unsigned)(u0 + k) * 8192u, lds + LDS_DUMMY);
  };
  auto b1_stage = [&](int s_) { return (s_ & 7) == dt1 ? (s_ + LEAD) & 63 : s_; };
  auto w1_voff = [&](int piece) {
    const int ch = piece * 64 + lane, k = ch / 28, pos = ch - k * 28;
    return (unsigned)(k * F + ft * 224 + (pos ^ (3 * ((k >> 3) & 1))) * 8) * 2u;
  };
  auto a1_vx = [&](int l) { return (unsigned)((chunk * 256 + 32 * (l >> 5) + 16 * ((l >> 3) & 1) + ((l >> 4) & 1)) * D * 2 + ((l & 7) ^ (l >> 4)) * 16); };
  auto a1_vb = [&](int l) { return (unsigned)((cb * 64 + (l >> 3)) * D * 2 + ((l & 7) ^ (l >> 4)) * 16); };
  if (roleA) {
    const rsrc_t rs = mk_rsrc(a.x, S * D * 2), rre = mk_rsrc(a.Bre, P * D * 2), rim = mk_rsrc(a.Bim, P * D * 2);
    const unsigned vxe = a1_vx(lane), vxo = (vxe ^ 64u) + 64 * D * 2, vbe = a1_vb(lane);
    const unsigned sx = (unsigned)((wid >> 2) * 128 + (wid & 3) * 4) * (D * 2), sbb = (unsigned)wid * (8 * D * 2), vb = (wid & 1) ? vbe ^ 64u : vbe;
#pragma unroll
    for (int t = 0; t < A1_NS - 1; ++t) {
#pragma unroll
      for (int j = 0; j < 4; ++j) dma16(rs, (j & 1) ? vxo : vxe, sx + (j >> 1) * (2 * D * 2) + t * 128, lds + t * A1_STAGE + wid * 4096 + j * 1024);
      dma16(rre, vb, sbb + t * 128, lds + t * A1_STAGE + 32768 + (wid >> 1) * 4096 + (wid & 1) * 1024);
      dma16(rim, vb, sbb + t * 128, lds + t * A1_STAGE + 32768 + (wid >> 1) * 4096 + (wid & 1) * 1024 + 2048);
    }
    if (wid == 7) {
      dma4(mk_rsrc((const void*)(pc0 & ~255ull), CODE_PF_BYTES), (unsigned)(lane * 128), (unsigned)((idx % 5) * 8192), lds + LDS_DUMMY);
      __builtin_amdgcn_s_sleep(PF_DELAY);
      touch(a.w1, S * F * 2, PF_N0 * ri, PF_N0);
    }
    LOAD_TAG();
  }
  int isB = xc & 1;
  asm volatile("" : "+s"(isB));
  if (isB) {
    const int row = ri * 16 + wid * 2;
    u32x4 r[2][2], brow[2][2];
    {
      const u16* xp = a.x + (size_t)row * D + lane * 8;
      const u16* bp = (ri < 64 ? a.Bre : a.Bim) + (size_t)(row & 1023) * D + lane * 8;
#pragma unroll
      for (int i = 0; i < 4; ++i) r[i >> 1][i & 1] = *(const u32x4*)(xp + i * 512);
#pragma unroll
      for (int i = 0; i < 4; ++i) brow[i >> 1][i & 1] = *(const u32x4*)(bp + i * 512);
    }
    LOAD_TAG();
    const rsrc_t rx = mk_rsrc(a.ws + WS_HL, S * D * 2);
#pragma unroll
    for (int pass = 0; pass < 2; ++pass) {
      float s1[2], s2[2];
#pragma unroll
      for (int i = 0; i < 2; ++i) {
        float a1 = 0.f, a2 = 0.f;
#pragma unroll
        for (int k = 0; k < 8; ++k) { const unsigned w = r[i][k >> 2][k & 3]; a1 = dot2(w, 0x3f803f80u, a1); a2 = dot2(w, w, a2); }
        s1[i] = a1; s2[i] = a2;
      }
#pragma unroll
      for (int i = 0; i < 2; ++i) { s1[i] = wave_sum(s1[i]); s2[i] = wave_sum(s2[i]); }
#pragma unroll
      for (int i = 0; i < 2; ++i) {
        const float mean = s1[i] * (1.0f / D), var = fmaxf(fmaf(-mean, mean, s2[i] * (1.0f / D)), 0.f);
        const unsigned ms = pk_bf16(mean, __builtin_amdgcn_sqrtf(rbf(rbf(var) + 1e-5f)));
        const float rs_ = __builtin_amdgcn_rcpf(bfhi(ms)), mb = bflo(ms);
#pragma unroll
        for (int k = 0; k < 8; ++k) {
          const unsigned w = r[i][k >> 2][k & 3];
          r[i][k >> 2][k & 3] = pk_bf16((bflo(w) - mb) * rs_, (bfhi(w) - mb) * rs_);
        }
        if (pass == 0) {
          if (lane == 0) __hip_atomic_store(sy + SY_ST + row + i, ((u64)tag32 << 32) | ms, RLX_AGENT);
        } else {
          st16_wt(rx, (unsigned)lane * 16u, (unsigned)((row + i) * D) * 2u, r[i][0]);
          st16_wt(rx, (unsigned)lane * 16u + 1024u, (unsigned)((row + i) * D) * 2u, r[i][1]);
        }
      }
    }
    asm volatile("" ::"v"(brow[0][0]), "v"(brow[0][1]), "v"(brow[1][0]), "v"(brow[1][1]));
    wait_vm<0>();
    bar();
    if (wid == 0 && lane == 0) __hip_atomic_store(sy + SY_LN + ri, tag, RLX_AGENT);
    if (wid != 6) {
      const rsrc_t rs = mk_rsrc(a.w1, S * F * 2);
      const int p0_ = 2 * (wid == 7 ? 6 : wid);
      const unsigned v0 = w1_voff(p0_), v1 = w1_voff(p0_ + 1);
#pragma unroll
      for (int t = 0; t < B1_NS - 1; ++t) {
        dma16(rs, v0, b1_stage(t) * (32 * F * 2), lds + t * B1_STAGE + p0_ * 1024);
        dma16(rs, v1, b1_stage(t) * (32 * F * 2), lds + t * B1_STAGE + p0_ * 1024 + 1024);
      }
    }
    {
      float sums[2];
#pragma unroll
      for (int i = 0; i < 2; ++i) {
        float s = 0.f;
#pragma unroll
        for (int k = 0; k < 8; ++k) { const unsigned w = brow[i][k >> 2][k & 3]; s = dot2(w, 0x3f803f80u, s); }
        sums[i] = wave_sum(s);
      }
      if (lane < 2) __hip_atomic_store(sy + SY_SB + row + lane, ((u64)tag32 << 32) | __float_as_uint(lane ? sums[1] : sums[0]), RLX_AGENT);
    }
  }

  if (wid >= 4) {
    const int pw = wid - 4;
    int lane = __builtin_amdgcn_mbcnt_hi(~0u, __builtin_amdgcn_mbcnt_lo(~0u, 0u));
    asm volatile("" : "+v"(lane));
    if (pw == 3) {
      auto flags_fetch = [&]() {
        dma16c(rsy, (unsigned)lane * 16u, SY_LN * 8, lds + LDS_FLG);
        dma16c(rsy, (unsigned)lane * 16u, SY_LN * 8 + 1024, lds + LDS_FLG + 1024);
      };
      auto flags_check = [&]() {
        bool ok = true;
#pragma unroll
        for (int k = 0; k < 4; ++k) ok &= *(LAS u64*)(lds + LDS_FLG + k * 512 + lane * 8) == tag;
        if (!__all(ok)) wait_flags<4>(sy + SY_LN + lane, true, tag);
      };
      auto xb_clear = [&](int k0) {
        const rsrc_t rxb = mk_rsrc(a.ws + WS_XB, 256 * 16384);
#pragma unroll
        for (int k = 0; k < 4; ++k) st16_wt(rxb, (unsigned)(lane * 16 + k * 1024), (unsigned)(bid * 16384 + k0 * 4096), (u32x4){0u, 0u, 0u, 0u});
      };
      if (lane < 4) *(LAS unsigned*)(size_t)(LDS_CNT + lane * 4) = 0u;
      if (roleA) {
        st16_wt(rsy, (unsigned)lane * 16u, (unsigned)(SY_GR * 8 + (chunk * P + cb * 64) * 16), (u32x4){0u, 0u, 0u, 0u});
        dma4(mk_rsrc(a.Lre, P * 2), (unsigned)(lane & 31) * 4u, cb * 128, lds + LDS_RAW + 3072);
        dma4(mk_rsrc(a.Lim, P * 2), (unsigned)(lane & 31) * 4u, cb * 128, lds + LDS_RAW + 3072 + 256);
        dma4(mk_rsrc(a.logdt, P * 2), (unsigned)(lane & 31) * 4u, cb * 128, lds + LDS_RAW + 3072 + 512);
        HOLD_TAG();
        wait_vm<5 + PF_N0>();
        const int bst = PF_ST0 + ((ri * PF_NST) >> 7);
        auto step = [&](int t) { bar(); if (t == bst) touch(a.w1, S * F * 2, 128 * PF_N0 + PF_N1 * ri, PF_N1); };
        step(0); step(1); step(2);
        if (bst <= 2) wait_vm<PF_N1>(); else wait_vm<0>();
        if (lane == 0) __hip_atomic_store(sy + SY_LN + 128 + ri, tag, RLX_AGENT);
        LAS float* cf = (LAS float*)(lds + LDS_COEF) + lane;
        const float lr = bf2f(*(LAS u16*)(lds + LDS_RAW + 3072 + lane * 2)), li = bf2f(*(LAS u16*)(lds + LDS_RAW + 3072 + 256 + lane * 2));
        const float dtt = rbf(expf(bf2f(*(LAS u16*)(lds + LDS_RAW + 3072 + 512 + lane * 2)))), er = expf(dtt * lr);
        float arg = dtt * li;
        asm volatile("" : "+v"(arg));
        step(3);
        float sn, cs;
        sincosf(arg, &sn, &cs);
        float pr = er * cs, pi = er * sn;
        asm volatile("" : "+v"(pr), "+v"(pi));
        step(4);
        const float nr = pr - 1.0f, den = lr * lr + li * li;
        cf[0] = pr; cf[64] = pi;
        cf[128] = 2.0f * (nr * lr + pi * li) / den; cf[192] = 2.0f * (pi * lr - nr * li) / den;
        step(5);
#pragma unroll
        for (int k = 0; k < 8; ++k) {
          const float t = pr * pr - pi * pi; pi = 2.0f * pr * pi; pr = t;
          if (k >= 3) { cf[(4 + 2 * (k - 3)) * 64] = pr; cf[(5 + 2 * (k - 3)) * 64] = pi; }
        }
#pragma unroll 1
        for (int t = 6; t < 12; ++t) { step(t); if (t < 10) xb_clear(t - 6); }
        const unsigned sbo = (unsigned)(SY_SB * 8 + cb * 512 + (lane >> 5) * (P * 8) + (lane & 31) * 16);
#pragma unroll 1
        for (unsigned spins = 0;; ++spins) {
          dma16c(rsy, (unsigned)lane * 16u, (unsigned)(SY_ST * 8 + chunk * 2048), lds + LDS_RAW);
          dma16c(rsy, (unsigned)lane * 16u, (unsigned)(SY_ST * 8 + chunk * 2048 + 1024), lds + LDS_RAW + 1024);
          dma16c(rsy, sbo, 0, lds + LDS_RAW + 2048);
          if (spins == 0) { flags_fetch(); step(12); step(13); }
          wait_vm<0>();
          u32x2 gs[4], gb[2];
          bool ok = true;
#pragma unroll
          for (int k = 0; k < 4; ++k) { gs[k] = *(LAS u32x2*)(lds + LDS_RAW + k * 512 + lane * 8); ok &= gs[k][1] == tag32; }
#pragma unroll
          for (int k = 0; k < 2; ++k) { gb[k] = *(LAS u32x2*)(lds + LDS_RAW + 2048 + k * 512 + lane * 8); ok &= gb[k][1] == tag32; }
          if (__all(ok) || spins > SPIN_CAP) {
#pragma unroll
            for (int k = 0; k < 4; ++k) {
              LAS float* e = (LAS float*)(lds + LDS_STAT + (2 * k + (lane >> 5)) * 272 + (lane & 15) * 16 + ((lane >> 4) & 1) * 4);
              e[0] = -bflo(gs[k][0]); e[2] = __builtin_amdgcn_rcpf(bfhi(gs[k][0]));
            }
#pragma unroll
            for (int k = 0; k < 2; ++k) *(LAS unsigned*)(lds + LDS_SB + k * 256 + lane * 4) = gb[k][0];
            break;
          }
          __builtin_amdgcn_s_sleep(2);
        }
        touch(a.w1, S * F * 2, 128 * (14 - PF_N2) + PF_N2 * ri, PF_N2);
        touch(a.Cre, D * P * 2, 2 * ri, 2); touch(a.Cim, D * P * 2, 2 * ri, 2);
        touch(a.w2, F * S * 2, UA + (PF_WA / 2) * ri, PF_WA / 2); touch(a.w2, F * S * 2, (PF_WA / 2) * ri, PF_WA / 2);
        bar();
        flags_check();
        wait_lgkm0();
        step(15);
      } else {
        wait_vm<0>();
        dma4(mk_rsrc((const void*)(pc0 & ~255ull), CODE_PF_BYTES), (unsigned)(lane * 128), (unsigned)((idx % 5) * 8192), lds + LDS_DUMMY);
#pragma unroll 1
        for (int t = 0; t < 13; ++t) { bar(); if (t >= 4 && !(t & 1) && t < 12) xb_clear((t - 4) >> 1); }
        flags_fetch();
#pragma unroll 1
        for (int t = 13; t < 20; ++t) bar();
        wait_vm<0>();
        flags_check();
        if (bid == 255 && lane == 0) __hip_atomic_store(sy, tag, RLX_AGENT);
#pragma unroll 1
        for (int t = 20; t < B1_NT - PF_BT; ++t) bar();
        touch(a.w2, F * S * 2, UA + WA + (PF_WB / 2) * ri, PF_WB / 2); touch(a.w2, F * S * 2, WA + (PF_WB / 2) * ri, PF_WB / 2);
#pragma unroll 1
        for (int t = 0; t < PF_BT; ++t) bar();
      }
    } else if (roleA) {
      if (pw < 2) {
        const rsrc_t rs = mk_rsrc(a.x, S * D * 2);
        const unsigned vxe = a1_vx(lane), vxo = (vxe ^ 64u) + 64 * D * 2;
        unsigned so = (A1_NS - 1) * 128 + pw * (128 * D * 2), lo = (A1_NS - 1) * A1_STAGE + pw * 16384;
        auto issue = [&]() {
#pragma unroll
          for (int j = 0; j < 16; ++j) dma16(rs, (j & 1) ? vxo : vxe, so + (j >> 1) * (2 * D * 2), lds + lo + j * 1024);
          so += 128; lo = lo >= (A1_NS - 1) * A1_STAGE ? lo - (A1_NS - 1) * A1_STAGE : lo + A1_STAGE;
        };
        HOLD_TAG();
        wait_vm<0>();
        prod_run<16, A1_NS>(A1_NT - (A1_NS - 1), issue);
        prod_tail<16, A1_NS - 2>();
      } else {
        const unsigned ve = a1_vb(lane), vo = ve ^ 64u;
        const rsrc_t rre = mk_rsrc(a.Bre, P * D * 2), rim = mk_rsrc(a.Bim, P * D * 2);
        unsigned so = (A1_NS - 1) * 128, lo = (A1_NS - 1) * A1_STAGE + 32768;
        auto issue = [&]() {
#pragma unroll
          for (int j = 0; j < 8; ++j) {
            dma16(rre, (j & 1) ? vo : ve, so + j * (8 * D * 2), lds + lo + (j >> 1) * 4096 + (j & 1) * 1024);
            dma16(rim, (j & 1) ? vo : ve, so + j * (8 * D * 2), lds + lo + (j >> 1) * 4096 + (j & 1) * 1024 + 2048);
          }
          so += 128; lo = lo >= (A1_NS - 1) * A1_STAGE ? lo - (A1_NS - 1) * A1_STAGE : lo + A1_STAGE;
        };
        HOLD_TAG();
        wait_vm<0>();
        prod_run<16, A1_NS>(A1_NT - (A1_NS - 1), issue);
        prod_tail<16, A1_NS - 2>();
      }
    } else {
      const int dd0 = dt1 * 128;
      if (pw < 2) {
        unsigned voff[7];
#pragma unroll
        for (int j = 0; j < 7; ++j) voff[j] = w1_voff(pw * 7 + j);
        const rsrc_t rs = mk_rsrc(a.w1, S * F * 2);
        int st = B1_NS - 1;
        unsigned so = b1_stage(st) * (32 * F * 2), lo = (B1_NS - 1) * B1_STAGE + pw * 7168;
        auto issue = [&]() {
#pragma unroll
          for (int j = 0; j < 7; ++j) dma16(rs, voff[j], so, lds + lo + j * 1024);
          so = b1_stage(++st) * (32 * F * 2); lo = lo >= (B1_NS - 1) * B1_STAGE ? lo - (B1_NS - 1) * B1_STAGE : lo + B1_STAGE;
        };
        wait_vm<0>();
        prod_run<7, B1_NS>(B1_NT - (B1_NS - 1), issue);
        prod_tail<7, B1_NS - 2>();
      } else {
        unsigned voff[2];
#pragma unroll
        for (int j = 0; j < 2; ++j) {
          const int k = j * 8 + (lane >> 4), pos = lane & 15;
          const int c = pos ^ (((k & 3) << 1) | (((k >> 3) & 1) << 3));
          voff[j] = (unsigned)((lane >> 4) * D + dd0 + c * 8) * 2u;
        }
        const rsrc_t rs = mk_rsrc(a.ws + WS_HL, S * D * 2);
        int st = 0;
        unsigned so = b1_stage(0) * (32 * D * 2), lo = B1_ABYTES;
        auto issue = [&]() {
#pragma unroll
          for (int j = 0; j < 8; ++j) dma16c(rs, voff[(j >> 1) & 1], so + j * 4 * (D * 2), lds + lo + j * 1024);
          so = b1_stage(++st) * (32 * D * 2); lo = lo >= (B1_NS - 1) * B1_STAGE ? lo - (B1_NS - 1) * B1_STAGE : lo + B1_STAGE;
        };
        int done = B1_NS + 3;
        wait_flags<1>(sy + SY_LN + lane, lane < 2 * (done + LEAD), tag);
        prod_fill(B1_NS - 1, issue);
        prod_run<8, B1_NS>(1, issue);
        dma16c(rsy, (unsigned)lane * 16u, SY_LN * 8, lds + LDS_RES2);
        prod_run<8, B1_NS>(3, issue);
        wait_vm<24>();
        const u32x4 fl = *(LAS u32x4*)(lds + LDS_RES2 + lane * 16);
        int pre = __all(fl[0] == tag32 && fl[2] == tag32 && fl[1] == (unsigned)(tag >> 32) && fl[3] == (unsigned)(tag >> 32)) ? 128 : 2 * (done + LEAD);
#pragma unroll 1
        while (pre < 128 && done < B1_NT) {
          const int n = pre / 2 - LEAD - done;
          prod_run<8, B1_NS>(n, issue);
          done += n;
          wait_vm<(B1_NS - 2) * 8>();
          bar();
          pre = flag_prefix<2>(sy + SY_LN + lane, 64, 2 * (done + LEAD) + 2, tag, lane);
          issue();
          ++done;
        }
        prod_run<8, B1_NS>(B1_NT - done, issue);
        prod_tail<8, B1_NS - 2>();
      }
    }
    bar();
    const int nU = nt2 - nC;
    int us = 0;
    auto u_tile = [&](int s_) { return ku0 + ((s_ & 7) == dt ? (s_ + 8 < nU ? s_ + 8 : dt) : s_); };
    if (pw == 1 || pw == 2) {
      const rsrc_t rxx = mk_rsrc(a.x, S * D * 2);
      const int dk = d0 + (roleA ? 0 : 64);
#pragma unroll
      for (int k = 0; k < 8; ++k) dma16(rxx, (unsigned)((lane >> 3) * (D * 2) + ((lane & 7) ^ (lane >> 3)) * 16), (unsigned)((t0 + 64 * (pw - 1) + 8 * k) * D + dk) * 2u, lds + LDS_RES + (8 * (pw - 1) + k) * 1024);
      if (pw == 1) dma16c(rsy, (unsigned)lane * 16u, (unsigned)(SY_ST + t0) * 8u, lds + LDS_RES2);
      else { dma4(mk_rsrc(a.b2, S * 2), (unsigned)lane * 4u, (unsigned)t0 * 2u, lds + LDS_RES2 + 1024); dma4(mk_rsrc(a.Dsk, D * 2), (unsigned)lane * 4u, (unsigned)dk * 2u, lds + LDS_RES2 + 1280); }
      wait_vm<0>();
      if (lane == 0) __hip_atomic_fetch_add((LAS unsigned*)(size_t)(LDS_CNT + 12), 1u, __ATOMIC_RELAXED, __HIP_MEMORY_SCOPE_WORKGROUP);
    }
    if (pw == 0 || pw == 3) {
      const int dh = d0 + (((pw ? 1 : 0) ^ (roleA ? 0 : 1)) << 6), r4 = lane >> 4;
      const unsigned vce = (unsigned)(dh + (lane >> 3)) * (P * 2) + (unsigned)(((lane & 7) ^ (lane >> 4)) * 16), vco = vce ^ 64u;
      const unsigned vu = (unsigned)((dh + (lane >> 2)) * 64 + (((lane & 3) ^ (((r4 & 1) << 1) ^ ((r4 >> 1) * 3))) * 16));
      const rsrc_t ru = mk_rsrc(a.ws + WS_U, D * F * 2);
      rsrc_t rc = mk_rsrc(a.Cre, D * P * 2), rc2 = mk_rsrc(a.Cim, D * P * 2);
      unsigned so = 0, lo = pw ? 8192 : 0, sh = 0;
      auto issueC = [&]() {
        so = ((unsigned)cbs & 15u) * 128;
#pragma unroll
        for (int j = 0; j < 8; ++j) dma16(rc, (j & 1) ? vco : vce, so + j * 8 * (P * 2), lds + lo + j * 1024);
        const rsrc_t t_ = rc; rc = rc2; rc2 = t_;
        cbs >>= sh; sh ^= 4; lo = (lo + P2_STAGE) & (P2_NS * P2_STAGE - 1);
      };
      auto issueU = [&]() {
#pragma unroll
        for (int j = 0; j < 8; ++j) dma16c(ru, vu, so + (j & 1) * (D * 64) + (j >> 1) * 1024, lds + lo + j * 1024);
        so = u_tile(++us) * (2 * D * 64); lo = (lo + P2_STAGE) & (P2_NS * P2_STAGE - 1);
      };
      prod_fill(P2_NS - 1, issueC);
      if (roleA) {
        prod_run<8, P2_NS>(nC - 6, issueC);
        dma16c(rsy, (unsigned)(lane & 7) * 16u, (unsigned)(SY_U + dt * 16) * 8u, lds + LDS_FLG + (pw ? 1024 : 0));
        prod_run<8, P2_NS>(3, issueC);
      } else {
        prod_run<8, P2_NS>(nC - 3, issueC);
      }
      so = u_tile(0) * (2 * D * 64);
      constexpr int F0 = 2 * NBA / 7;
      const int f0 = roleA ? 0 : F0;
      gu64* uf = sy + SY_U + dt * 16 + ((f0 + lane) & 15);
      LAS char* ufl = lds + LDS_FLG + (pw ? 1024 : 0);
      int done = 0, pre = 0;
      bool look = roleA;
#pragma unroll 1
      while (pre < 128 && done < nU) {
        wait_vm<16>();
        bar();
        const int kn = ku0 + done + 8 < 55 ? ku0 + done + 8 : 55, need = roleA ? 16 : (2 * kn + 1) / 7 + 1 - f0;
        int p2 = 0;
        if (look) {
          const u64 m = ~__ballot(lane >= 16 - f0 || *(LAS u64*)(ufl + ((f0 + lane) & 15) * 8) == tag);
          p2 = m ? __builtin_ctzll(m) : 128;
        }
        pre = p2 >= need ? p2 : flag_prefix<1>(uf, 16 - f0, need, tag, lane);
        issueU();
        ++done;
        if (pre < 128) {
          const int n = ((7 * (f0 + pre)) >> 1) - ku0 - 8 - done;
          look = n >= 3;
          prod_run<8, P2_NS>(look ? n - 3 : n, issueU);
          if (look) {
            dma16c(rsy, (unsigned)(lane & 7) * 16u, (unsigned)(SY_U + dt * 16) * 8u, ufl);
            prod_run<8, P2_NS>(3, issueU);
          }
          done += n;
        }
      }
      prod_run<8, P2_NS>(nU - done, issueU);
    } else {
      unsigned voff[2];
#pragma unroll
      for (int j = 0; j < 2; ++j) {
        const int k = j * 8 + (lane >> 4), pos = lane & 15;
        const int c = pos ^ (((k & 3) << 1) | (((k >> 3) & 1) << 3));
        voff[j] = (unsigned)(((pw - 1) * 32 + (lane >> 4)) * S + t0 + c * 8) * 2u;
      }
      const unsigned vh = (unsigned)(lane * 16 + (pw - 1) * 8192);
      const rsrc_t rh = mk_rsrc(a.ws + WS_HT, 2 * P * S * 2), rw = mk_rsrc(a.w2, F * S * 2);
      unsigned so = 0, lo = 16384 + (pw - 1) * 8192, sh = 0;
      const int nf = nC / 2 + (roleA ? 0 : 1);
      const int hfi = (int)((unsigned)(cbs >> (4 * (lane & 15))) & 15u);
      gu64* hf = sy + SY_H + (lane == nC / 2 ? chunk * 16 + cb : (tt >> 1) * 16 + hfi);
      auto issueH = [&]() {
        so = (tt * 32 + sh * 4 + ((unsigned)cbs & 15u)) * 16384;
#pragma unroll
        for (int j = 0; j < 8; ++j) dma16c(rh, vh, so + j * 1024, lds + lo + j * 1024);
        cbs >>= sh; sh ^= 4; lo = (lo + P2_STAGE) & (P2_NS * P2_STAGE - 1);
      };
      auto issueW = [&]() {
#pragma unroll
        for (int j = 0; j < 8; ++j) dma16(rw, voff[(j >> 1) & 1], so + j * 4 * (S * 2), lds + lo + j * 1024);
        so = u_tile(++us) * (64 * S * 2); lo = (lo + P2_STAGE) & (P2_NS * P2_STAGE - 1);
      };
      if (roleA) lds_wait(LDS_CNT, 4u);
      int done = P2_NS - 1, pre = flag_prefix<1>(hf, nf, 2, tag, lane);
      prod_fill(P2_NS - 1, issueH);
#pragma unroll 1
      while (pre < 128 && done < nC) {
        const int n = (2 * pre < nC ? 2 * pre : nC) - done;
        const bool look = n >= 3;
        prod_run<8, P2_NS>(look ? n - 3 : n, issueH);
        if (look) {
          dma16c(rsy, (unsigned)(lane & 7) * 16u, (unsigned)(SY_H + (tt >> 1) * 16) * 8u, lds + LDS_HFL + (pw - 1) * 1024);
          prod_run<8, P2_NS>(3, issueH);
        }
        done += n;
        if (done < nC) {
          wait_vm<16>();
          bar();
          int p2 = 0;
          if (look) {
            const u64 m = ~__ballot(lane >= nf || (lane < nC / 2 && *(LAS u64*)(lds + LDS_HFL + (pw - 1) * 1024 + hfi * 8) == tag));
            p2 = m ? __builtin_ctzll(m) : 128;
          }
          pre = p2 > done / 2 ? p2 : flag_prefix<1>(hf, nf, done / 2 + 1, tag, lane);
          issueH();
          ++done;
        }
      }
      prod_run<8, P2_NS>(nC - done, issueH);
      so = u_tile(0) * (64 * S * 2);
      if (pre < 128) (void)flag_prefix<1>(hf, nf, nf, tag, lane);
      prod_run<8, P2_NS>(nU, issueW);
    }
    prod_tail<8, P2_NS - 2>();
    return;
  }

  const int wm = wid >> 1, wn = wid & 1;
  int cl = lane & 15, g = lane >> 4, q4 = cl >> 2, p4 = lane & 3, ln = lane;
#define RELANE() do { ln = __builtin_amdgcn_mbcnt_hi(~0u, __builtin_amdgcn_mbcnt_lo(~0u, 0u)); asm volatile("" : "+v"(ln)); cl = ln & 15; g = ln >> 4; q4 = cl >> 2; p4 = ln & 3; } while (0)
  const int kh = roleA ? 0 : 1, pa = bid ^ 1;
  const rsrc_t rxb = mk_rsrc(a.ws + WS_XB, 256 * 16384);
  f32x4 res[2][4];
  auto residual = [&]() {
    lds_wait(LDS_CNT + 12, 2u);
#pragma unroll
    for (int n = 0; n < 4; ++n) {
      const int tl = 64 * wn + 16 * n + cl;
      const unsigned ms = *(LAS unsigned*)(lds + LDS_RES2 + tl * 8);
      const float rs_ = __builtin_amdgcn_rcpf(bfhi(ms)), mb = bflo(ms), b2 = bf2f(*(LAS u16*)(lds + LDS_RES2 + 1024 + tl * 2));
#pragma unroll
      for (int mi = 0; mi < 2; ++mi) {
        const int c8 = 4 * wm + 2 * mi + (g >> 1);
        const u32x2 xv = *(LAS u32x2*)(lds + LDS_RES + tl * 128 + ((c8 ^ (tl & 7)) * 16) + (g & 1) * 8);
        const u32x2 dk = *(LAS u32x2*)(lds + LDS_RES2 + 1280 + c8 * 16 + (g & 1) * 8);
#pragma unroll
        for (int h = 0; h < 2; ++h) {
          const float x0 = bflo(xv[h]), x1 = bfhi(xv[h]);
          const unsigned xn = pk_bf16((x0 - mb) * rs_, (x1 - mb) * rs_);
          res[mi][n][2 * h] = fmaf(bflo(xn), bflo(dk[h]), x0 + b2); res[mi][n][2 * h + 1] = fmaf(bfhi(xn), bfhi(dk[h]), x1 + b2);
        }
        asm volatile("" : "+v"(res[mi][n]));
      }
    }
  };

  if (roleA) {
    HOLD_TAG();
    RELANE();
    const int p0 = cb * 64;
    const unsigned rb = (unsigned)(cl * 128 + ((g ^ (cl >> 1)) * 16));
    unsigned pa0 = rb + wm * 16384, pa1 = pa0 ^ 64u, pb0 = rb + 32768 + wn * 8192, pb1 = pb0 ^ 64u;
    f32x4 acc[8][4];
#pragma unroll
    for (int m = 0; m < 8; ++m)
#pragma unroll
      for (int n = 0; n < 4; ++n) acc[m][n] = (f32x4){0.f, 0.f, 0.f, 0.f};
    bf16x8 xa[8], xb[4], ya[8], yb[4];
    auto rd = [&](bf16x8* fa, bf16x8* fb, unsigned pa_, unsigned pb_, int r) {
      if (r == 0) fa[0] = lds_rd128(pa_); else if (r < 5) fb[r - 1] = lds_rd128(pb_ + (r - 1) * 2048); else fa[r - 4] = lds_rd128(pa_ + (r - 4) * 2048);
    };
    int slot = 0, dl = 0;
    wait_vm0_known();
    bar();
#pragma unroll
    for (int r = 0; r < 12; ++r) rd(xa, xb, pa0, pb0, r);
#pragma unroll 1
    for (int t = 0; t < A1_NT; ++t) {
      SCHED_FENCE(); wait_lgkm0(); SCHED_FENCE();
      phase<32>(acc, xa, xb, [&](int i) {
        if (i % A1_RS == 0 && i / A1_RS < 12) rd(ya, yb, pa1, pb1, i / A1_RS);
        if (i == 25) { dl = (slot == A1_NS - 1) ? -(A1_NS - 1) * A1_STAGE : A1_STAGE; slot = (slot == A1_NS - 1) ? 0 : slot + 1; }
        if (i == 27) { pa0 += dl; pb0 += dl; asm volatile("" : "+v"(pa0), "+v"(pb0)); }
        if (i == 29) { pa1 += dl; pb1 += dl; asm volatile("" : "+v"(pa1), "+v"(pb1)); }
      });
      wait_lgkm0();
      bar();
      SCHED_FENCE();
      phase<32>(acc, ya, yb, [&](int i) { if (i % A1_RS == 0 && i / A1_RS < 12) rd(xa, xb, pa0, pb0, i / A1_RS); });
    }
    RELANE();
    float cf[2][14], sb[4];
#pragma unroll
    for (int q = 0; q < 2; ++q) {
#pragma unroll
      for (int k = 0; k < 14; ++k) cf[q][k] = ((LAS float*)(lds + LDS_COEF))[k * 64 + 32 * wn + 16 * q + cl];
      sb[2 * q] = ((LAS float*)(lds + LDS_SB))[32 * wn + 16 * q + cl]; sb[2 * q + 1] = ((LAS float*)(lds + LDS_SB))[64 + 32 * wn + 16 * q + cl];
    }
    f32x2 er[2], ei[2];
#pragma unroll
    for (int q = 0; q < 2; ++q) { er[q] = bc2(0.f); ei[q] = bc2(0.f); }
#pragma unroll
    for (int m = 0; m < 8; ++m)
#pragma unroll
      for (int jj = 0; jj < 2; ++jj) {
        const f32x4 st = *(LAS f32x4*)(lds + LDS_STAT + (4 * wm + g) * 272 + (2 * m + jj) * 16);
        const f32x2 c2 = {st[0], st[1]}, r2 = {st[2], st[3]};
#pragma unroll
        for (int q = 0; q < 2; ++q) {
          const f32x2 ur = fma2(c2, bc2(sb[2 * q]), (f32x2){acc[m][2 * q][2 * jj], acc[m][2 * q][2 * jj + 1]}) * r2;
          const f32x2 ui = fma2(c2, bc2(sb[2 * q + 1]), (f32x2){acc[m][2 * q + 1][2 * jj], acc[m][2 * q + 1][2 * jj + 1]}) * r2;
          acc[m][2 * q][2 * jj] = ur.x; acc[m][2 * q][2 * jj + 1] = ur.y; acc[m][2 * q + 1][2 * jj] = ui.x; acc[m][2 * q + 1][2 * jj + 1] = ui.y;
          const f32x2 nr = fma2(bc2(cf[q][0]), er[q], fma2(bc2(-cf[q][1]), ei[q], ur));
          const f32x2 ni = fma2(bc2(cf[q][0]), ei[q], fma2(bc2(cf[q][1]), er[q], ui));
          er[q] = nr; ei[q] = ni;
        }
        if (jj == 1 && (m & 1)) asm volatile("" : "+v"(er[0]), "+v"(ei[0]), "+v"(er[1]), "+v"(ei[1]));
      }
    float sr[2], si[2], pr_[2], pi_[2];
    LAS float* tot = (LAS float*)(lds + LDS_TOT);
#pragma unroll
    for (int q = 0; q < 2; ++q) {
      float tr, ti;
      CMUL(tr, ti, cf[q][4], cf[q][5], er[q].x, ei[q].x);
      sr[q] = tr + er[q].y; si[q] = ti + ei[q].y;
      float vr = __shfl_up(sr[q], 16), vi = __shfl_up(si[q], 16);
      CMUL(tr, ti, cf[q][6], cf[q][7], vr, vi);
      if (g >= 1) { sr[q] += tr; si[q] += ti; }
      vr = __shfl_up(sr[q], 32); vi = __shfl_up(si[q], 32);
      CMUL(tr, ti, cf[q][8], cf[q][9], vr, vi);
      if (g >= 2) { sr[q] += tr; si[q] += ti; }
      vr = __shfl_up(sr[q], 16); vi = __shfl_up(si[q], 16);
      pr_[q] = g >= 1 ? vr : 0.f; pi_[q] = g >= 1 ? vi : 0.f;
      if (wm == 0 && g == 3) *(LAS f32x2*)(tot + (32 * wn + 16 * q + cl) * 2) = (f32x2){sr[q], si[q]};
    }
    if (wm == 0) {
      wait_lgkm0();
      if (ln == 0) __hip_atomic_store((LAS unsigned*)(size_t)(LDS_CNT + 4 + 4 * wn), 1u, __ATOMIC_RELAXED, __HIP_MEMORY_SCOPE_WORKGROUP);
    } else {
      lds_wait(LDS_CNT + 4 + 4 * wn, 1u);
    }
    f32x2 tq[2];
#pragma unroll
    for (int q = 0; q < 2; ++q) {
      tq[q] = *(LAS f32x2*)(tot + (32 * wn + 16 * q + cl) * 2);
      if (wm == 1 && g == 3 && chunk < 7) {
        float tr, ti;
        CMUL(tr, ti, cf[q][10], cf[q][11], tq[q].x, tq[q].y);
        st16_wt(rsy, (unsigned)(SY_GR * 8 + (chunk * P + p0 + 32 * wn + 16 * q + cl) * 16), 0, (u32x4){__float_as_uint(tr + sr[q]), 1u, __float_as_uint(ti + si[q]), 1u});
      }
    }
#pragma unroll
    for (int m = 0; m < 8; ++m)
#pragma unroll
      for (int q = 0; q < 2; ++q) {
        const f32x4 ur = acc[m][2 * q], ui = acc[m][2 * q + 1];
        acc[m][2 * q] = ur * cf[q][2] - ui * cf[q][3];
        acc[m][2 * q + 1] = ui * (-cf[q][2]) - ur * cf[q][3];
        asm volatile("" : "+v"(acc[m][2 * q]), "+v"(acc[m][2 * q + 1]));
      }
    float hr[2] = {0.f, 0.f}, hi[2] = {0.f, 0.f};
    if (chunk > 0) {
      float gr[2][2], gi[2][2];
#pragma unroll 1
      for (unsigned spins = 0;; ++spins) {
        bool ok = true;
#pragma unroll
        for (int q = 0; q < 2; ++q)
#pragma unroll
          for (int k = 0; k < 2; ++k) {
            const int cc = (g + 4 * k < chunk) ? g + 4 * k : 0;
            const u32x4 v = __builtin_amdgcn_raw_buffer_load_b128(rsy, (unsigned)(SY_GR * 8 + (cc * P + p0 + 32 * wn + 16 * q + cl) * 16), 0, 16);
            ok &= (v[1] == 1u) & (v[3] == 1u);
            gr[q][k] = __uint_as_float(v[0]); gi[q][k] = __uint_as_float(v[2]);
          }
        if (__all(ok) || spins > SPIN_CAP) break;
        __builtin_amdgcn_s_sleep(1);
        asm volatile("" ::: "memory");
      }
#pragma unroll
      for (int q = 0; q < 2; ++q) {
        float ar_ = 0.f, ai_ = 0.f;
#pragma unroll
        for (int k = 0; k < 2; ++k) {
          float vr = gr[q][k], vi = gi[q][k];
          const int e = chunk - 1 - (g + 4 * k);
#pragma unroll
          for (int i = 0; i < 6 - 4 * k; ++i) {
            float tr, ti;
            CMUL(tr, ti, cf[q][12], cf[q][13], vr, vi);
            vr = i < e ? tr : vr; vi = i < e ? ti : vi;
          }
          ar_ += e >= 0 ? vr : 0.f; ai_ += e >= 0 ? vi : 0.f;
        }
        ar_ += __shfl_xor(ar_, 16); ai_ += __shfl_xor(ai_, 16);
        ar_ += __shfl_xor(ar_, 32); ai_ += __shfl_xor(ai_, 32);
        hr[q] = ar_; hi[q] = ai_;
      }
    }
    f32x2 wr[2], wi[2];
#pragma unroll
    for (int q = 0; q < 2; ++q) {
      float tr, ti;
      if (wm == 1) { CMUL(tr, ti, cf[q][10], cf[q][11], hr[q], hi[q]); hr[q] = tr + tq[q].x; hi[q] = ti + tq[q].y; }
      CMUL(tr, ti, cf[q][6], cf[q][7], hr[q], hi[q]);
      if (g & 1) { hr[q] = tr; hi[q] = ti; }
      CMUL(tr, ti, cf[q][8], cf[q][9], hr[q], hi[q]);
      if (g & 2) { hr[q] = tr; hi[q] = ti; }
      hr[q] += pr_[q]; hi[q] += pi_[q];
      CMUL(tr, ti, cf[q][4], cf[q][5], hr[q], hi[q]);
      const f32x2 h0r = {hr[q], tr + er[q].x}, h0i = {hi[q], ti + ei[q].x};
      wr[q] = h0r * cf[q][2] - h0i * cf[q][3];
      wi[q] = h0i * (-cf[q][2]) - h0r * cf[q][3];
    }
    LAS char* scr = lds + wid * P2_STAGE + 16384;
    const unsigned fsw = (unsigned)(((cl & 3) << 1) | (((cl >> 3) & 1) << 3));
    const rsrc_t rh = mk_rsrc(a.ws + WS_HT, 2 * P * S * 2);
#pragma unroll
    for (int q = 0; q < 2; ++q) {
      const f32x2 ar2 = bc2(cf[q][0]), ai2 = bc2(cf[q][1]);
#pragma unroll
      for (int mh = 0; mh < 2; ++mh) {
        u32x4 o[2][2];
#pragma unroll
        for (int mq = 0; mq < 4; ++mq) {
          const int m = 4 * mh + mq;
          const f32x2 u0r = {acc[m][2 * q][0], acc[m][2 * q][1]}, u0i = {acc[m][2 * q + 1][0], acc[m][2 * q + 1][1]};
          const f32x2 u1r = {acc[m][2 * q][2], acc[m][2 * q][3]}, u1i = {acc[m][2 * q + 1][2], acc[m][2 * q + 1][3]};
          const f32x2 n0r = fma2(ar2, wr[q], fma2(ai2, wi[q], u0r)), n0i = fma2(ar2, wi[q], fma2(-ai2, wr[q], u0i));
          const f32x2 n1r = fma2(ar2, n0r, fma2(ai2, n0i, u1r)), n1i = fma2(ar2, n0i, fma2(-ai2, n0r, u1i));
          wr[q] = n1r; wi[q] = n1i;
          o[0][0][mq] = pk_bf16(n0r.x, n1r.x); o[0][1][mq] = pk_bf16(n0r.y, n1r.y);
          o[1][0][mq] = pk_bf16(n0i.x, n1i.x); o[1][1][mq] = pk_bf16(n0i.y, n1i.y);
        }
#pragma unroll
        for (int part = 0; part < 2; ++part)
#pragma unroll
          for (int run = 0; run < 2; ++run)
            *(LAS u32x4*)(scr + (part * 32 + 16 * q + cl) * 256 + (((unsigned)(4 * g + 2 * run + mh) ^ fsw) * 16)) = o[part][run];
      }
#pragma unroll
      for (int i = 0; i < 8; ++i) {
        const int part = i >> 2, pc = 4 * q + (i & 3);
        const u32x4 v = *(LAS u32x4*)(scr + part * 8192 + pc * 1024 + ln * 16);
        st16_wt(rh, (unsigned)(ln * 16 + pc * 1024), (unsigned)(((2 * chunk + wm) * 32 + part * 16 + cb) * 16384 + wn * 8192), v);
      }
      SCHED_FENCE();
    }
  } else {
    RELANE();
    const int f0 = ft * 224, dd0 = dt1 * 128;
    const int k0 = 8 * g + q4;
    unsigned pa0 = (unsigned)(k0 * 448 + ((12 * wm + p4) ^ (3 * (g & 1))) * 16 + (p4 & 1) * 8);
    unsigned pa1 = (unsigned)(k0 * 448 + ((12 * wm + p4) ^ (3 * (g & 1))) * 16 + ((p4 & 1) ^ 1) * 8);
    unsigned pa2 = (unsigned)(k0 * 448 + ((24 + 2 * wm + (p4 >> 1)) ^ (3 * (g & 1))) * 16 + (p4 & 1) * 8);
    unsigned pb[4];
#pragma unroll
    for (int n = 0; n < 4; ++n) pb[n] = (unsigned)(B1_ABYTES + k0 * 256 + ((8 * wn + 2 * n + (p4 >> 1)) ^ ((q4 << 1) | ((g & 1) << 3))) * 16 + (p4 & 1) * 8);
    f32x4 acc[7][4];
#pragma unroll
    for (int m = 0; m < 7; ++m) {
      const u32x2 bv = *(const u32x2*)(a.b1 + f0 + (m < 6 ? 96 * wm + 32 * (m >> 1) + 8 * g + 4 * ((m & 1) ^ (g & 1)) : 192 + 16 * wm + 4 * g));
#pragma unroll
      for (int n = 0; n < 4; ++n) acc[m][n] = (f32x4){bflo(bv[0]), bfhi(bv[0]), bflo(bv[1]), bfhi(bv[1])};
    }
    TrFrag xa[7], xb[4], ya[7], yb[4];
    int slot = 0, dl = 0;
    auto side = [&](TrFrag* fa, TrFrag* fb, int i) {
      const int f = i >> 1, h = i & 1;
      if (f == 0) fa[0].h[h] = lds_rdtr(pa0 + h * 1792);
      else if (f < 5) fb[f - 1].h[h] = lds_rdtr(pb[f - 1] + h * 1024);
      else if (f < 10) fa[f - 4].h[h] = lds_rdtr((((f - 4) & 1) ? pa1 : pa0) + ((f - 4) >> 1) * 64 + h * 1792);
      else if (f == 10) fa[6].h[h] = lds_rdtr(pa2 + h * 1792);
      else if (i == 22) { dl = (slot == B1_NS - 1) ? -(B1_NS - 1) * B1_STAGE : B1_STAGE; slot = (slot == B1_NS - 1) ? 0 : slot + 1; }
      else if (i == 23) { pa0 += dl; pb[0] += dl; asm volatile("" : "+v"(pa0), "+v"(pb[0])); }
      else if (i == 24) { pb[1] += dl; pb[2] += dl; asm volatile("" : "+v"(pb[1]), "+v"(pb[2])); }
      else if (i == 25) { pb[3] += dl; pa1 += dl; asm volatile("" : "+v"(pb[3]), "+v"(pa1)); }
      else if (i == 26) { pa2 += dl; asm volatile("" : "+v"(pa2)); }
    };
    wait_vm0_known();
    bar();
#pragma unroll
    for (int i = 0; i < 28; ++i) side(xa, xb, i);
#pragma unroll 1
    for (int t = 0; t < B1_NT; t += 2) {
      SCHED_FENCE(); wait_lgkm0(); bar(); SCHED_FENCE();
      phase<28>(acc, xa, xb, [&](int i) { side(ya, yb, i); });
      wait_lgkm0(); bar(); SCHED_FENCE();
      phase<28>(acc, ya, yb, [&](int i) { side(xa, xb, i); });
    }
    RELANE();
    const rsrc_t ru = mk_rsrc(a.ws + WS_U, D * F * 2);
    auto act4 = [&](const f32x4& v) {
      const f32x2 v0 = gelu_tanh2((f32x2){v[0], v[1]}), v1 = gelu_tanh2((f32x2){v[2], v[3]});
      return (u32x2){pk_bf16(v0.x, v0.y), pk_bf16(v1.x, v1.y)};
    };
#pragma unroll
    for (int n = 0; n < 4; ++n) *(LAS u32x2*)(lds + LDS_U6 + (64 * wn + 16 * n + cl) * 64 + 32 * wm + 8 * g) = act4(acc[6][n]);
    wait_lgkm0();
    if (ln == 0) __hip_atomic_fetch_add((LAS unsigned*)(size_t)(LDS_CNT + 4 + 4 * wn), 1u, __ATOMIC_RELAXED, __HIP_MEMORY_SCOPE_WORKGROUP);
#pragma unroll
    for (int n = 0; n < 4; ++n) {
      const unsigned vo = (unsigned)((dd0 + 64 * wn + 16 * n + cl) * 64);
#pragma unroll
      for (int mm = 0; mm < 3; ++mm) {
        const u32x2 w0 = act4(acc[2 * mm][n]), w1 = act4(acc[2 * mm + 1][n]);
        const bool sw = g & 1;
        const u32x2 lo = sw ? w1 : w0, hi = sw ? w0 : w1;
        st16_wt(ru, vo + 16 * g, (unsigned)((ft * 7 + 3 * wm + mm) * D * 64), (u32x4){lo[0], lo[1], hi[0], hi[1]});
      }
      if (n == 1) {
        lds_wait(LDS_CNT + 4 + 4 * wn, 2u);
#pragma unroll
        for (int k = 0; k < 2; ++k) {
          const unsigned o = (unsigned)(wn * 4096 + wm * 2048 + k * 1024 + ln * 16);
          st16_wt(ru, o, (unsigned)(((ft * 7 + 6) * D + dd0) * 64), *(LAS u32x4*)(lds + LDS_U6 + o));
        }
      }
    }
    residual();
  }

  publish(LDS_CNT, sy + (roleA ? SY_H + chunk * 16 + cb : SY_U + dt1 * 16 + ft), tag, ln);
  if (roleA) residual();

  f32x4 acc[4][4];
#pragma unroll
  for (int n = 0; n < 4; ++n) { acc[0][n] = res[0][n]; acc[1][n] = res[1][n]; acc[2][n] = (f32x4){0.f, 0.f, 0.f, 0.f}; acc[3][n] = (f32x4){0.f, 0.f, 0.f, 0.f}; }
  const unsigned au0 = (unsigned)(wm * 4096 + cl * 64 + ((g ^ (((q4 & 1) << 1) ^ ((q4 >> 1) * 3))) * 16));
  unsigned pa0 = (unsigned)((wm * 32 + cl) * 128 + ((g ^ (cl >> 1)) * 16)), pa1 = pa0 ^ 64u;
  const unsigned sw0 = pa0 ^ au0 ^ 65536u, sw1 = pa1 ^ (au0 + 1024u) ^ 65536u;
  unsigned pb[4];
#pragma unroll
  for (int n = 0; n < 4; ++n) pb[n] = (unsigned)(16384 + (8 * g + q4) * 256 + ((8 * wn + 2 * n + (p4 >> 1)) ^ ((q4 << 1) | ((g & 1) << 3))) * 16 + (p4 & 1) * 8);
  bf16x8 xa[4], ya[4];
  TrFrag xb[4], yb[4];
  static_assert(P2_NS == 4 && NAB % 2 == 0 && NBA % 2 == 0 && NAB >= P2_NS && NAB <= 16 && PF_NST2 <= 26 - NAB, "phase-2 loop: two stages per trip, 64-KB windows");
  auto rd = [&](bf16x8* fa, TrFrag* fb, int so, int ks, int r) {
    const unsigned pa_ = (ks ? pa1 : pa0) + so;
    if (r == 0) fa[0] = lds_rd128(pa_);
    else if (r < 9) fb[(r - 1) >> 1].h[(r - 1) & 1] = lds_rdtr(pb[(r - 1) >> 1] + so + ks * 8192 + ((r - 1) & 1) * 1024);
    else if (r < 12) fa[r - 8] = lds_rd128(pa_ + ((r - 8) >> 1) * 8192 + ((r - 8) & 1) * 2048);
  };
  bar();
#pragma unroll
  for (int r = 0; r < 12; ++r) rd(xa, xb, 0, 0, r);
#pragma unroll 1
  for (int t = 0; t < nt2; t += 2) {
    unsigned x0, x1;
    SCHED_FENCE(); wait_lgkm0(); SCHED_FENCE();
    phase<16>(acc, xa, xb, [&](int i) { rd(ya, yb, 0, 1, i); });
    wait_lgkm0(); bar(); SCHED_FENCE();
    phase<16>(acc, ya, yb, [&](int i) {
      rd(xa, xb, P2_STAGE, 0, i);
      if (i == 13) { const bool sw = (t + 2 == nC); x0 = sw ? sw0 : 65536u; x1 = sw ? sw1 : 65536u; asm volatile("" : "+v"(x0), "+v"(x1)); }
      if (i == 14) { pa0 ^= x0; asm volatile("" : "+v"(pa0)); }
    });
    wait_lgkm0(); SCHED_FENCE();
    phase<16>(acc, xa, xb, [&](int i) {
      rd(ya, yb, P2_STAGE, 1, i);
      if (i == 12) { pb[0] ^= 65536u; pb[1] ^= 65536u; asm volatile("" : "+v"(pb[0]), "+v"(pb[1])); }
      if (i == 13) { pb[2] ^= 65536u; pb[3] ^= 65536u; asm volatile("" : "+v"(pb[2]), "+v"(pb[3])); }
      if (i == 14) { pa1 ^= x1; asm volatile("" : "+v"(pa1)); }
    });
    wait_lgkm0(); bar(); SCHED_FENCE();
    phase<16>(acc, ya, yb, [&](int i) { rd(xa, xb, 0, 0, i); });
  }
  const unsigned xo = (unsigned)(wid * 4096 + ln * 16);
#pragma unroll
  for (int n = 0; n < 4; ++n) {
    u32x4 w = {pk_bf16(acc[2][n][0], acc[2][n][1]), pk_bf16(acc[2][n][2], acc[2][n][3]), pk_bf16(acc[3][n][0], acc[3][n][1]), pk_bf16(acc[3][n][2], acc[3][n][3])};
#pragma unroll
    for (int k = 0; k < 4; ++k) w[k] = w[k] ? w[k] : 0x80000000u;
    st16_wt(rxb, xo + n * 1024, bid * 16384, w);
  }
  u32x4 rv[4];
#pragma unroll 1
  for (unsigned spins = 0; spins < SPIN_CAP; ++spins) {
    bool ok = true;
#pragma unroll
    for (int n = 0; n < 4; ++n) rv[n] = __builtin_amdgcn_raw_buffer_load_b128(rxb, xo + n * 1024 + pa * 16384, 0, 16);
#pragma unroll
    for (int n = 0; n < 4; ++n) ok &= (rv[n][0] != 0u) & (rv[n][1] != 0u) & (rv[n][2] != 0u) & (rv[n][3] != 0u);
    if (__all(ok)) break;
#pragma unroll 1
    for (; spins < SPIN_CAP; ++spins) {
      __builtin_amdgcn_s_sleep(XB_SLEEP);
      asm volatile("" ::: "memory");
      if (__builtin_amdgcn_readfirstlane(__builtin_amdgcn_raw_buffer_load_b32(rxb, wid * 4096 + 4092 + pa * 16384, 0, 16)) != 0u) break;
    }
    asm volatile("" ::: "memory");
  }
#pragma unroll
  for (int n = 0; n < 4; ++n) {
    acc[0][n] += (f32x4){bflo(rv[n][0]), bfhi(rv[n][0]), bflo(rv[n][1]), bfhi(rv[n][1])};
    acc[1][n] += (f32x4){bflo(rv[n][2]), bfhi(rv[n][2]), bflo(rv[n][3]), bfhi(rv[n][3])};
  }
  {
    LAS char* tb = lds + wid * 8192;
#pragma unroll
    for (int n = 0; n < 4; ++n)
#pragma unroll
      for (int mi = 0; mi < 2; ++mi) *(LAS f32x4*)(tb + (16 * n + cl) * 128 + (((4 * mi + g) ^ (cl & 7)) * 16)) = acc[mi][n];
    const rsrc_t rout = mk_rsrc(a.out, S * D * 4);
#pragma unroll
    for (int k = 0; k < 8; ++k) {
      const u32x4 v = *(LAS u32x4*)(tb + k * 1024 + ln * 16);
      __builtin_amdgcn_raw_buffer_store_b128(v, rout, (unsigned)((t0 + 64 * wn + 8 * k + (ln >> 3)) * D + d0 + 64 * kh + 32 * wm) * 4u + (unsigned)(((ln & 7) ^ (ln >> 3)) * 16), 0, 16);
    }
  }
}

extern "C" void kernel_launch(void* const* d_in, const int* in_sizes, int n_in, void* d_out, int out_size, void* d_ws, size_t ws_size,
                              hipStream_t stream) {
  (void)hipFuncSetAttribute((const void*)fused, hipFuncAttributeMaxDynamicSharedMemorySize, LDS_BYTES);
  Args a;
  a.x = (const u16*)d_in[0]; a.Lre = (const u16*)d_in[1]; a.Lim = (const u16*)d_in[2]; a.Bre = (const u16*)d_in[3]; a.Bim = (const u16*)d_in[4];
  a.Cre = (const u16*)d_in[5]; a.Cim = (const u16*)d_in[6]; a.logdt = (const u16*)d_in[7]; a.Dsk = (const u16*)d_in[8];
  a.w1 = (const u16*)d_in[10]; a.b1 = (const u16*)d_in[11]; a.w2 = (const u16*)d_in[12]; a.b2 = (const u16*)d_in[13];
  a.out = (float*)d_out; a.ws = (char*)d_ws;
  fused<<<256, 512, LDS_BYTES, stream>>>(a);
}
```

```cpp
#include <hip/hip_runtime.h>
#include <stdint.h>

typedef unsigned short u16;
typedef unsigned long long u64;
typedef __bf16 bf16x8 __attribute__((ext_vector_type(8)));
typedef short s16x4 __attribute__((ext_vector_type(4)));
typedef short s16x8 __attribute__((ext_vector_type(8)));
typedef float f32x2 __attribute__((ext_vector_type(2)));
typedef float f32x4 __attribute__((ext_vector_type(4)));
typedef unsigned u32x4 __attribute__((ext_vector_type(4)));
typedef unsigned u32x2 __attribute__((ext_vector_type(2)));
#define LAS __attribute__((address_space(3)))
#define GAS __attribute__((address_space(1)))
typedef GAS unsigned long long gu64;
typedef __amdgpu_buffer_rsrc_t rsrc_t;
#define RLX_AGENT __ATOMIC_RELAXED, __HIP_MEMORY_SCOPE_AGENT

constexpr int S = 2048, D = 1024, P = 1024, F = 3584;
constexpr size_t MB = 1024 * 1024;
constexpr size_t WS_HL = 4 * MB, WS_HT = 8 * MB, WS_U = 16 * MB, WS_XB = 24 * MB, WS_SYNC = 32 * MB;
constexpr int SY_LN = 64, SY_H = 320, SY_U = 448, SY_ST = 1024, SY_SB = 3072, SY_GR = 5120;
constexpr unsigned SYNC_BYTES = (SY_GR + 8 * P * 2) * 8;
constexpr unsigned SPIN_CAP = 1u << 17;
constexpr int NBA = 28;
constexpr int NAB = 6;
constexpr unsigned long long CB_ORDER = 0x80F7E6D5C491B3A2ull;
constexpr unsigned CODE_PF_BYTES = 40 * 1024;
constexpr int LEAD = 8, LLIM = 32;
constexpr int PF_N0 = 2;
constexpr int PF_N2 = 7;
constexpr int PF_WB = 2;
constexpr int A1_RS = 2;

__device__ __forceinline__ float bf2f(u16 v) { return __uint_as_float(((unsigned)v) << 16); }
__device__ __forceinline__ float bflo(unsigned v) { return __uint_as_float(v << 16); }
__device__ __forceinline__ float bfhi(unsigned v) { return __uint_as_float(v & 0xffff0000u); }
__device__ __forceinline__ unsigned pk_bf16(float lo, float hi) {
  typedef __bf16 bf16x2 __attribute__((ext_vector_type(2)));
  f32x2 v = {lo, hi};
  bf16x2 r = __builtin_convertvector(v, bf16x2);
  return __builtin_bit_cast(unsigned, r);
}
__device__ __forceinline__ float rbf(float f) { return bflo(pk_bf16(f, f)); }
__device__ __forceinline__ float dot2(unsigned a, unsigned b, float c) {
  typedef __bf16 bf16x2 __attribute__((ext_vector_type(2)));
  return __builtin_amdgcn_fdot2_f32_bf16(__builtin_bit_cast(bf16x2, a), __builtin_bit_cast(bf16x2, b), c, false);
}
__device__ __forceinline__ f32x2 fma2(f32x2 a, f32x2 b, f32x2 c) { return __builtin_elementwise_fma(a, b, c); }
__device__ __forceinline__ f32x2 bc2(float v) { return (f32x2){v, v}; }

__device__ __forceinline__ rsrc_t mk_rsrc(const void* p, unsigned bytes) { return __builtin_amdgcn_make_buffer_rsrc((void*)p, 0, bytes, 0x00020000); }
__device__ __forceinline__ void dma16(rsrc_t rs, unsigned voff, unsigned soff, LAS char* l) { __builtin_amdgcn_raw_ptr_buffer_load_lds(rs, (LAS void*)l, 16, voff, soff, 0, 0); }
__device__ __forceinline__ void dma16c(rsrc_t rs, unsigned voff, unsigned soff, LAS char* l) { __builtin_amdgcn_raw_ptr_buffer_load_lds(rs, (LAS void*)l, 16, voff, soff, 0, 16); }
__device__ __forceinline__ void dma4(rsrc_t rs, unsigned voff, unsigned soff, LAS char* l) { __builtin_amdgcn_raw_ptr_buffer_load_lds(rs, (LAS void*)l, 4, voff, soff, 0, 0); }
__device__ __forceinline__ void st16_wt(rsrc_t rs, unsigned voff, unsigned soff, u32x4 v) { __builtin_amdgcn_raw_buffer_store_b128(v, rs, voff + soff, 0, 16); }
template <int N> __device__ __forceinline__ void wait_vm() { asm volatile("s_waitcnt vmcnt(%0)" ::"n"(N) : "memory"); }
__device__ __forceinline__ void wait_vm0_known() { asm volatile("" ::: "memory"); __builtin_amdgcn_s_waitcnt(0x0F70); asm volatile("" ::: "memory"); }
__device__ __forceinline__ void wait_lgkm0() { asm volatile("" ::: "memory"); __builtin_amdgcn_s_waitcnt(0xC07F); asm volatile("" ::: "memory"); }
__device__ __forceinline__ void bar() {
  asm volatile("" ::: "memory");
  __builtin_amdgcn_s_barrier();
  asm volatile("" ::: "memory");
}
#define SCHED_FENCE() __builtin_amdgcn_sched_barrier(0)
__device__ __forceinline__ bf16x8 lds_rd128(unsigned p) { return *(LAS bf16x8*)(size_t)p; }
__device__ __forceinline__ s16x4 lds_rdtr(unsigned p) { return __builtin_amdgcn_ds_read_tr16_b64_v4i16((LAS s16x4*)(size_t)p); }
struct TrFrag { s16x4 h[2]; };
__device__ __forceinline__ bf16x8 frag(const bf16x8& f) { return f; }
__device__ __forceinline__ bf16x8 frag(const TrFrag& f) { s16x8 ab = __builtin_shufflevector(f.h[0], f.h[1], 0, 1, 2, 3, 4, 5, 6, 7); return __builtin_bit_cast(bf16x8, ab); }
#define MFMA(a, b, c) __builtin_amdgcn_mfma_f32_16x16x32_bf16(frag(a), frag(b), (c), 0, 0, 0)
template <int NM, class FA, class FB, class Side>
__device__ __forceinline__ void phase(f32x4 (*acc)[4], const FA* fa, const FB* fb, Side side) {
#pragma unroll
  for (int i = 0; i < NM; ++i) {
    constexpr int MT = NM / 4;
    const int n_ = i / MT, r_ = i % MT, m_ = (n_ & 1) ? MT - 1 - r_ : r_;
    acc[m_][n_] = MFMA(fa[m_], fb[n_], acc[m_][n_]);
    side(i);
    SCHED_FENCE();
  }
}

template <int NL> __device__ __forceinline__ void wait_flags(gu64* p, bool active, u64 tag) {
#pragma unroll 1
  for (unsigned spins = 0; spins < SPIN_CAP; ++spins) {
    bool ok = true;
    if (active) {
#pragma unroll
      for (int k = 0; k < NL; ++k) ok &= __hip_atomic_load(p + 64 * k, RLX_AGENT) == tag;
    }
    if (__all(ok)) break;
    __builtin_amdgcn_s_sleep(2);
  }
  __builtin_amdgcn_fence(__ATOMIC_ACQUIRE, "wavefront");
}

template <int NL> __device__ __forceinline__ int flag_prefix(gu64* p, int n, int need, u64 tag, int lane) {
  int pre = 0;
#pragma unroll 1
  for (unsigned spins = 0; spins < SPIN_CAP; ++spins) {
    const u64 m0 = ~__ballot(lane >= n || __hip_atomic_load(p, RLX_AGENT) == tag);
    const u64 m1 = NL == 2 ? ~__ballot(__hip_atomic_load(p + 64, RLX_AGENT) == tag) : 0ull;
    pre = m0 ? __builtin_ctzll(m0) : m1 ? 64 + __builtin_ctzll(m1) : 128;
    if (pre >= need) break;
    __builtin_amdgcn_s_sleep(1);
  }
  __builtin_amdgcn_fence(__ATOMIC_ACQUIRE, "wavefront");
  return pre < need ? 128 : pre;
}
__device__ __forceinline__ void lds_wait(unsigned addr, unsigned want) {
#pragma unroll 1
  for (unsigned spins = 0; spins < (SPIN_CAP << 4); ++spins) {
    if (__builtin_amdgcn_readfirstlane(__hip_atomic_load((LAS unsigned*)(size_t)addr, __ATOMIC_RELAXED, __HIP_MEMORY_SCOPE_WORKGROUP)) >= want) break;
    __builtin_amdgcn_s_sleep(1);
  }
  asm volatile("" ::: "memory");
}
__device__ __forceinline__ void publish(unsigned cnt, gu64* flag, u64 tag, int lane) {
  wait_vm<0>();
  wait_vm0_known();
  wait_lgkm0();
  if (lane == 0 && __hip_atomic_fetch_add((LAS unsigned*)(size_t)cnt, 1u, __ATOMIC_RELAXED, __HIP_MEMORY_SCOPE_WORKGROUP) == 3u) __hip_atomic_store(flag, tag, RLX_AGENT);
}

template <class Issue> __device__ __forceinline__ void prod_fill(int n, Issue issue) {
#pragma unroll 1
  for (int t = 0; t < n; ++t) issue();
}
template <int L, int NS, class Issue> __device__ __forceinline__ void prod_run(int n, Issue issue) {
#pragma unroll 1
  for (int t = 0; t < n; ++t) { wait_vm<(NS - 2) * L>(); bar(); issue(); }
}
template <int L, int K, int X = 0> __device__ __forceinline__ void prod_tail() {
  wait_vm<K * L + X>();
  bar();
  if constexpr (K > 0) prod_tail<L, K - 1, X>();
}

__device__ __forceinline__ float wave_sum(float v) {
  v += __builtin_bit_cast(float, __builtin_amdgcn_update_dpp(0, __builtin_bit_cast(int, v), 0xB1, 0xf, 0xf, true));
  v += __builtin_bit_cast(float, __builtin_amdgcn_update_dpp(0, __builtin_bit_cast(int, v), 0x4E, 0xf, 0xf, true));
  v += __builtin_bit_cast(float, __builtin_amdgcn_update_dpp(0, __builtin_bit_cast(int, v), 0x141, 0xf, 0xf, true));
  v += __builtin_bit_cast(float, __builtin_amdgcn_update_dpp(0, __builtin_bit_cast(int, v), 0x140, 0xf, 0xf, true));
  const int iv = __builtin_bit_cast(int, v);
  const float a = __builtin_bit_cast(float, __builtin_amdgcn_readlane(iv, 0)), b = __builtin_bit_cast(float, __builtin_amdgcn_readlane(iv, 16));
  const float c = __builtin_bit_cast(float, __builtin_amdgcn_readlane(iv, 32)), d = __builtin_bit_cast(float, __builtin_amdgcn_readlane(iv, 48));
  return (a + b) + (c + d);
}

struct Args {
  const u16 *x, *Lre, *Lim, *Bre, *Bim, *Cre, *Cim, *logdt, *Dsk, *w1, *b1, *w2, *b2;
  float* out;
  char* ws;
};

constexpr int A1_NS = 3, A1_STAGE = 49152, A1_NT = D / 64;
constexpr int B1_NS = 7, B1_ABYTES = 32 * 448, B1_STAGE = B1_ABYTES + 32 * 256, B1_NT = S / 32;
constexpr int P2_NS = 4, P2_STAGE = 32768;
constexpr int LDS_BYTES = 160 * 1024;
constexpr int LDS_COEF = A1_NS * A1_STAGE;
constexpr int LDS_TOT = LDS_COEF + 14 * 256;
constexpr int LDS_SB = LDS_TOT + 512;
constexpr int LDS_STAT = LDS_SB + 512;
constexpr int LDS_RAW = LDS_STAT + 8 * 272;
constexpr int LDS_RES = P2_NS * P2_STAGE;
constexpr int LDS_U6 = LDS_COEF;
constexpr int LDS_HFL = LDS_COEF + 8192;
constexpr int LDS_RES2 = 158208;
constexpr int LDS_CNT = LDS_BYTES - 256 - 2048 - 64;
constexpr int LDS_FLG = LDS_BYTES - 256 - 2048;
constexpr int LDS_DUMMY = LDS_BYTES - 256;
static_assert(LDS_RAW + 3072 + 768 <= LDS_CNT && B1_NS * B1_STAGE <= LDS_CNT && LDS_RES + 16384 <= LDS_COEF && LDS_RAW + 3840 <= LDS_RES2 && LDS_RES2 + 1536 <= LDS_CNT && P2_NS * P2_STAGE <= LDS_COEF && LDS_RAW % 16 == 0, "lds");

__device__ __forceinline__ f32x2 gelu_tanh2(f32x2 x) {
  const f32x2 z = x * fma2(x * x, bc2(-0.10294323967f), bc2(-2.30220819813f));
  const f32x2 d = (f32x2){__builtin_amdgcn_exp2f(z.x), __builtin_amdgcn_exp2f(z.y)} + bc2(1.0f);
  return x * (f32x2){__builtin_amdgcn_rcpf(d.x), __builtin_amdgcn_rcpf(d.y)};
}
#define CMUL(or_, oi_, ar_, ai_, br_, bi_) do { const float t_ = (ar_) * (br_) - (ai_) * (bi_); oi_ = (ar_) * (bi_) + (ai_) * (br_); or_ = t_; } while (0)

__global__ __launch_bounds__(512, 2) void fused(Args a) {
  extern __shared__ __attribute__((aligned(16))) char smem[];
  LAS char* lds = (LAS char*)smem;
  u64 pc0;
  asm volatile("s_getpc_b64 %0" : "=s"(pc0));
  const int wid = __builtin_amdgcn_readfirstlane(threadIdx.x >> 6), lane = threadIdx.x & 63;
  const int bid = blockIdx.x, xc = bid & 7, idx = bid >> 3, xg = xc >> 1;
  const bool roleA = !(xc & 1);
  const int ri = bid >> 1;
  gu64* sy = (gu64*)(a.ws + WS_SYNC);
  u64 tag = 0;
#define LOAD_TAG() do { const __attribute__((address_space(4))) u64* ep_ = (const __attribute__((address_space(4))) u64*)(a.ws + WS_SYNC); \
    asm volatile("" : "+s"(ep_)::"memory"); tag = *ep_ + 1ull; } while (0)
#define HOLD_TAG() asm volatile("" ::"s"(tag))
#define tag32 ((unsigned)tag)
  const int chunk = 4 * (xg & 1) + (idx & 3), cb = 8 * (xg >> 1) + (idx >> 2);
  const int ft = 4 * xg + (idx & 3), dt1 = idx >> 2;
  const int tt = 4 * xg + (idx & 3), dt = idx >> 2;
  const int t0 = tt * 128, d0 = dt * 128;
  u64 cbs = roleA ? CB_ORDER : CB_ORDER >> (2 * (32 - NAB));
  const int nC = roleA ? 32 - NAB : NAB, ku0 = roleA ? 0 : NBA, nt2 = nC + (roleA ? NBA : 56 - NBA);
  const rsrc_t rsy = mk_rsrc(a.ws + WS_SYNC, SYNC_BYTES);

  auto touch = [&](const u16* base, unsigned bytes, int u0, int n) {
    const rsrc_t rs = mk_rsrc(base, bytes);
#pragma unroll 1
    for (int k = 0; k < n; ++k) dma4(rs, (unsigned)(lane * 128), (unsigned)(u0 + k) * 8192u, lds + LDS_DUMMY);
  };
  auto b1_stage = [&](int s_) { return (s_ & 7) == dt1 && s_ < LLIM ? (s_ + LEAD < LLIM ? s_ + LEAD : dt1) : s_; };
  auto w1_voff = [&](int piece) {
    const int ch = piece * 64 + lane, k = ch / 28, pos = ch - k * 28;
    return (unsigned)(k * F + ft * 224 + (pos ^ (3 * ((k >> 3) & 1))) * 8) * 2u;
  };
  auto a1_vx = [&](int l) { return (unsigned)((chunk * 256 + 32 * (l >> 5) + 16 * ((l >> 3) & 1) + ((l >> 4) & 1)) * D * 2 + ((l & 7) ^ (l >> 4)) * 16); };
  auto a1_vb = [&](int l) { return (unsigned)((cb * 64 + (l >> 3)) * D * 2 + ((l & 7) ^ (l >> 4)) * 16); };
  if (roleA) {
    const rsrc_t rs = mk_rsrc(a.x, S * D * 2), rre = mk_rsrc(a.Bre, P * D * 2), rim = mk_rsrc(a.Bim, P * D * 2);
    const unsigned vxe = a1_vx(lane), vxo = (vxe ^ 64u) + 64 * D * 2, vbe = a1_vb(lane);
    const unsigned sx = (unsigned)((wid >> 2) * 128 + (wid & 3) * 4) * (D * 2), sbb = (unsigned)wid * (8 * D * 2), vb = (wid & 1) ? vbe ^ 64u : vbe;
#pragma unroll
    for (int t = 0; t < A1_NS - 1; ++t) {
#pragma unroll
      for (int j = 0; j < 4; ++j) dma16(rs, (j & 1) ? vxo : vxe, sx + (j >> 1) * (2 * D * 2) + t * 128, lds + t * A1_STAGE + wid * 4096 + j * 1024);
      dma16(rre, vb, sbb + t * 128, lds + t * A1_STAGE + 32768 + (wid >> 1) * 4096 + (wid & 1) * 1024);
      dma16(rim, vb, sbb + t * 128, lds + t * A1_STAGE + 32768 + (wid >> 1) * 4096 + (wid & 1) * 1024 + 2048);
    }
    if (wid == 7) {
      dma4(mk_rsrc((const void*)(pc0 & ~255ull), CODE_PF_BYTES), (unsigned)(lane * 128), (unsigned)((idx % 5) * 8192), lds + LDS_DUMMY);
      __builtin_amdgcn_s_sleep(24);
      touch(a.w1, S * F * 2, PF_N0 * ri, PF_N0);
    }
    LOAD_TAG();
  }
  int isB = xc & 1;
  asm volatile("" : "+s"(isB));
  if (isB) {
    const int row = ri * 16 + wid * 2;
    u32x4 r[2][2], brow[2][2];
    {
      const u16* xp = a.x + (size_t)row * D + lane * 8;
      const u16* bp = (ri < 64 ? a.Bre : a.Bim) + (size_t)(row & 1023) * D + lane * 8;
#pragma unroll
      for (int i = 0; i < 4; ++i) r[i >> 1][i & 1] = *(const u32x4*)(xp + i * 512);
#pragma unroll
      for (int i = 0; i < 4; ++i) brow[i >> 1][i & 1] = *(const u32x4*)(bp + i * 512);
    }
    LOAD_TAG();
    const rsrc_t rx = mk_rsrc(a.ws + WS_HL, S * D * 2);
#pragma unroll
    for (int pass = 0; pass < 2; ++pass) {
      float s1[2], s2[2];
#pragma unroll
      for (int i = 0; i < 2; ++i) {
        float a1 = 0.f, a2 = 0.f;
#pragma unroll
        for (int k = 0; k < 8; ++k) { const unsigned w = r[i][k >> 2][k & 3]; a1 = dot2(w, 0x3f803f80u, a1); a2 = dot2(w, w, a2); }
        s1[i] = a1; s2[i] = a2;
      }
#pragma unroll
      for (int i = 0; i < 2; ++i) { s1[i] = wave_sum(s1[i]); s2[i] = wave_sum(s2[i]); }
#pragma unroll
      for (int i = 0; i < 2; ++i) {
        const float mean = s1[i] * (1.0f / D), var = fmaxf(fmaf(-mean, mean, s2[i] * (1.0f / D)), 0.f);
        const unsigned ms = pk_bf16(mean, __builtin_amdgcn_sqrtf(rbf(rbf(var) + 1e-5f)));
        const float rs_ = __builtin_amdgcn_rcpf(bfhi(ms)), mb = bflo(ms);
#pragma unroll
        for (int k = 0; k < 8; ++k) {
          const unsigned w = r[i][k >> 2][k & 3];
          r[i][k >> 2][k & 3] = pk_bf16((bflo(w) - mb) * rs_, (bfhi(w) - mb) * rs_);
        }
        if (pass == 0) {
          if (lane == 0) __hip_atomic_store(sy + SY_ST + row + i, ((u64)tag32 << 32) | ms, RLX_AGENT);
        } else {
          st16_wt(rx, (unsigned)lane * 16u, (unsigned)((row + i) * D) * 2u, r[i][0]);
          st16_wt(rx, (unsigned)lane * 16u + 1024u, (unsigned)((row + i) * D) * 2u, r[i][1]);
        }
      }
    }
    asm volatile("" ::"v"(brow[0][0]), "v"(brow[0][1]), "v"(brow[1][0]), "v"(brow[1][1]));
    wait_vm<0>();
    bar();
    if (wid == 0 && lane == 0) __hip_atomic_store(sy + SY_LN + ri, tag, RLX_AGENT);
    if (wid != 6) {
      const rsrc_t rs = mk_rsrc(a.w1, S * F * 2);
      const int p0_ = 2 * (wid == 7 ? 6 : wid);
      const unsigned v0 = w1_voff(p0_), v1 = w1_voff(p0_ + 1);
#pragma unroll
      for (int t = 0; t < B1_NS - 1; ++t) {
        dma16(rs, v0, b1_stage(t) * (32 * F * 2), lds + t * B1_STAGE + p0_ * 1024);
        dma16(rs, v1, b1_stage(t) * (32 * F * 2), lds + t * B1_STAGE + p0_ * 1024 + 1024);
      }
    }
    {
      float sums[2];
#pragma unroll
      for (int i = 0; i < 2; ++i) {
        float s = 0.f;
#pragma unroll
        for (int k = 0; k < 8; ++k) { const unsigned w = brow[i][k >> 2][k & 3]; s = dot2(w, 0x3f803f80u, s); }
        sums[i] = wave_sum(s);
      }
      if (lane < 2) __hip_atomic_store(sy + SY_SB + row + lane, ((u64)tag32 << 32) | __float_as_uint(lane ? sums[1] : sums[0]), RLX_AGENT);
    }
  }

  if (wid >= 4) {
    const int pw = wid - 4;
    int lane = __builtin_amdgcn_mbcnt_hi(~0u, __builtin_amdgcn_mbcnt_lo(~0u, 0u));
    asm volatile("" : "+v"(lane));
    if (pw == 3) {
      auto flags_fetch = [&]() {
        dma16c(rsy, (unsigned)lane * 16u, SY_LN * 8, lds + LDS_FLG);
        dma16c(rsy, (unsigned)lane * 16u, SY_LN * 8 + 1024, lds + LDS_FLG + 1024);
      };
      auto flags_check = [&]() {
        bool ok = true;
#pragma unroll
        for (int k = 0; k < 4; ++k) ok &= *(LAS u64*)(lds + LDS_FLG + k * 512 + lane * 8) == tag;
        if (!__all(ok)) wait_flags<4>(sy + SY_LN + lane, true, tag);
      };
      auto xb_clear = [&](int k0) {
        const rsrc_t rxb = mk_rsrc(a.ws + WS_XB, 256 * 16384);
#pragma unroll
        for (int k = 0; k < 4; ++k) st16_wt(rxb, (unsigned)(lane * 16 + k * 1024), (unsigned)(bid * 16384 + k0 * 4096), (u32x4){0u, 0u, 0u, 0u});
      };
      if (lane < 4) *(LAS unsigned*)(size_t)(LDS_CNT + lane * 4) = 0u;
      if (roleA) {
        st16_wt(rsy, (unsigned)lane * 16u, (unsigned)(SY_GR * 8 + (chunk * P + cb * 64) * 16), (u32x4){0u, 0u, 0u, 0u});
        dma4(mk_rsrc(a.Lre, P * 2), (unsigned)(lane & 31) * 4u, cb * 128, lds + LDS_RAW + 3072);
        dma4(mk_rsrc(a.Lim, P * 2), (unsigned)(lane & 31) * 4u, cb * 128, lds + LDS_RAW + 3072 + 256);
        dma4(mk_rsrc(a.logdt, P * 2), (unsigned)(lane & 31) * 4u, cb * 128, lds + LDS_RAW + 3072 + 512);
        HOLD_TAG();
        wait_vm<5 + PF_N0>();
        bar(); bar(); bar();
        wait_vm<0>();
        if (lane == 0) __hip_atomic_store(sy + SY_LN + 128 + ri, tag, RLX_AGENT);
        LAS float* cf = (LAS float*)(lds + LDS_COEF) + lane;
        const float lr = bf2f(*(LAS u16*)(lds + LDS_RAW + 3072 + lane * 2)), li = bf2f(*(LAS u16*)(lds + LDS_RAW + 3072 + 256 + lane * 2));
        const float dtt = rbf(expf(bf2f(*(LAS u16*)(lds + LDS_RAW + 3072 + 512 + lane * 2)))), er = expf(dtt * lr);
        float arg = dtt * li;
        asm volatile("" : "+v"(arg));
        bar();
        float sn, cs;
        sincosf(arg, &sn, &cs);
        float pr = er * cs, pi = er * sn;
        asm volatile("" : "+v"(pr), "+v"(pi));
        bar();
        const float nr = pr - 1.0f, den = lr * lr + li * li;
        cf[0] = pr; cf[64] = pi;
        cf[128] = 2.0f * (nr * lr + pi * li) / den; cf[192] = 2.0f * (pi * lr - nr * li) / den;
        bar();
#pragma unroll
        for (int k = 0; k < 8; ++k) {
          const float t = pr * pr - pi * pi; pi = 2.0f * pr * pi; pr = t;
          if (k >= 3) { cf[(4 + 2 * (k - 3)) * 64] = pr; cf[(5 + 2 * (k - 3)) * 64] = pi; }
        }
#pragma unroll 1
        for (int t = 6; t < 12; ++t) { bar(); if (t < 10) xb_clear(t - 6); }
        const unsigned sbo = (unsigned)(SY_SB * 8 + cb * 512 + (lane >> 5) * (P * 8) + (lane & 31) * 16);
#pragma unroll 1
        for (unsigned spins = 0;; ++spins) {
          dma16c(rsy, (unsigned)lane * 16u, (unsigned)(SY_ST * 8 + chunk * 2048), lds + LDS_RAW);
          dma16c(rsy, (unsigned)lane * 16u, (unsigned)(SY_ST * 8 + chunk * 2048 + 1024), lds + LDS_RAW + 1024);
          dma16c(rsy, sbo, 0, lds + LDS_RAW + 2048);
          if (spins == 0) { flags_fetch(); bar(); bar(); }
          wait_vm<0>();
          u32x2 gs[4], gb[2];
          bool ok = true;
#pragma unroll
          for (int k = 0; k < 4; ++k) { gs[k] = *(LAS u32x2*)(lds + LDS_RAW + k * 512 + lane * 8); ok &= gs[k][1] == tag32; }
#pragma unroll
          for (int k = 0; k < 2; ++k) { gb[k] = *(LAS u32x2*)(lds + LDS_RAW + 2048 + k * 512 + lane * 8); ok &= gb[k][1] == tag32; }
          if (__all(ok) || spins > SPIN_CAP) {
#pragma unroll
            for (int k = 0; k < 4; ++k) {
              LAS float* e = (LAS float*)(lds + LDS_STAT + (2 * k + (lane >> 5)) * 272 + (lane & 15) * 16 + ((lane >> 4) & 1) * 4);
              e[0] = -bflo(gs[k][0]); e[2] = __builtin_amdgcn_rcpf(bfhi(gs[k][0]));
            }
#pragma unroll
            for (int k = 0; k < 2; ++k) *(LAS unsigned*)(lds + LDS_SB + k * 256 + lane * 4) = gb[k][0];
            break;
          }
          __builtin_amdgcn_s_sleep(2);
        }
        touch(a.w1, S * F * 2, 128 * (14 - PF_N2) + PF_N2 * ri, PF_N2);
        touch(a.Cre, D * P * 2, 2 * ri, 2); touch(a.Cim, D * P * 2, 2 * ri, 2);
        bar();
        flags_check();
        wait_lgkm0();
        bar();
      } else {
        wait_vm<0>();
        dma4(mk_rsrc((const void*)(pc0 & ~255ull), CODE_PF_BYTES), (unsigned)(lane * 128), (unsigned)((idx % 5) * 8192), lds + LDS_DUMMY);
#pragma unroll 1
        for (int t = 0; t < 13; ++t) { bar(); if (t >= 4 && !(t & 1) && t < 12) xb_clear((t - 4) >> 1); }
        flags_fetch();
#pragma unroll 1
        for (int t = 13; t < 20; ++t) bar();
        wait_vm<0>();
        flags_check();
        if (bid == 255 && lane == 0) __hip_atomic_store(sy, tag, RLX_AGENT);
#pragma unroll 1
        for (int t = 20; t < B1_NT - 5; ++t) bar();
        touch(a.w2, F * S * 2, NBA * 32 + PF_WB * ri, PF_WB); touch(a.w2, F * S * 2, PF_WB * ri, PF_WB);
#pragma unroll 1
        for (int t = 0; t < 5; ++t) bar();
      }
    } else if (roleA) {
      if (pw < 2) {
        const rsrc_t rs = mk_rsrc(a.x, S * D * 2);
        const unsigned vxe = a1_vx(lane), vxo = (vxe ^ 64u) + 64 * D * 2;
        unsigned so = (A1_NS - 1) * 128 + pw * (128 * D * 2), lo = (A1_NS - 1) * A1_STAGE + pw * 16384;
        auto issue = [&]() {
#pragma unroll
          for (int j = 0; j < 16; ++j) dma16(rs, (j & 1) ? vxo : vxe, so + (j >> 1) * (2 * D * 2), lds + lo + j * 1024);
          so += 128; lo = lo >= (A1_NS - 1) * A1_STAGE ? lo - (A1_NS - 1) * A1_STAGE : lo + A1_STAGE;
        };
        HOLD_TAG();
        wait_vm<0>();
        prod_run<16, A1_NS>(A1_NT - (A1_NS - 1), issue);
        prod_tail<16, A1_NS - 2>();
      } else {
        const unsigned ve = a1_vb(lane), vo = ve ^ 64u;
        const rsrc_t rre = mk_rsrc(a.Bre, P * D * 2), rim = mk_rsrc(a.Bim, P * D * 2);
        unsigned so = (A1_NS - 1) * 128, lo = (A1_NS - 1) * A1_STAGE + 32768;
        auto issue = [&]() {
#pragma unroll
          for (int j = 0; j < 8; ++j) {
            dma16(rre, (j & 1) ? vo : ve, so + j * (8 * D * 2), lds + lo + (j >> 1) * 4096 + (j & 1) * 1024);
            dma16(rim, (j & 1) ? vo : ve, so + j * (8 * D * 2), lds + lo + (j >> 1) * 4096 + (j & 1) * 1024 + 2048);
          }
          so += 128; lo = lo >= (A1_NS - 1) * A1_STAGE ? lo - (A1_NS - 1) * A1_STAGE : lo + A1_STAGE;
        };
        HOLD_TAG();
        wait_vm<0>();
        prod_run<16, A1_NS>(A1_NT - (A1_NS - 1), issue);
        prod_tail<16, A1_NS - 2>();
      }
    } else {
      const int dd0 = dt1 * 128;
      if (pw < 2) {
        unsigned voff[7];
#pragma unroll
        for (int j = 0; j < 7; ++j) voff[j] = w1_voff(pw * 7 + j);
        const rsrc_t rs = mk_rsrc(a.w1, S * F * 2);
        int st = B1_NS - 1;
        unsigned so = b1_stage(st) * (32 * F * 2), lo = (B1_NS - 1) * B1_STAGE + pw * 7168;
        auto issue = [&]() {
#pragma unroll
          for (int j = 0; j < 7; ++j) dma16(rs, voff[j], so, lds + lo + j * 1024);
          so = b1_stage(++st) * (32 * F * 2); lo = lo >= (B1_NS - 1) * B1_STAGE ? lo - (B1_NS - 1) * B1_STAGE : lo + B1_STAGE;
        };
        wait_vm<0>();
        prod_run<7, B1_NS>(B1_NT - (B1_NS - 1), issue);
        prod_tail<7, B1_NS - 2>();
      } else {
        unsigned voff[2];
#pragma unroll
        for (int j = 0; j < 2; ++j) {
          const int k = j * 8 + (lane >> 4), pos = lane & 15;
          const int c = pos ^ (((k & 3) << 1) | (((k >> 3) & 1) << 3));
          voff[j] = (unsigned)((lane >> 4) * D + dd0 + c * 8) * 2u;
        }
        const rsrc_t rs = mk_rsrc(a.ws + WS_HL, S * D * 2);
        int st = 0;
        unsigned so = b1_stage(0) * (32 * D * 2), lo = B1_ABYTES;
        auto issue = [&]() {
#pragma unroll
          for (int j = 0; j < 8; ++j) dma16c(rs, voff[(j >> 1) & 1], so + j * 4 * (D * 2), lds + lo + j * 1024);
          so = b1_stage(++st) * (32 * D * 2); lo = lo >= (B1_NS - 1) * B1_STAGE ? lo - (B1_NS - 1) * B1_STAGE : lo + B1_STAGE;
        };
        int done = B1_NS + 3;
        wait_flags<1>(sy + SY_LN + lane, lane < 2 * (done + LEAD), tag);
        prod_fill(B1_NS - 1, issue);
        prod_run<8, B1_NS>(1, issue);
        dma16c(rsy, (unsigned)lane * 16u, SY_LN * 8, lds + LDS_RES2);
        prod_run<8, B1_NS>(3, issue);
        wait_vm<24>();
        const u32x4 fl = *(LAS u32x4*)(lds + LDS_RES2 + lane * 16);
        int pre = __all(fl[0] == tag32 && fl[2] == tag32 && fl[1] == (unsigned)(tag >> 32) && fl[3] == (unsigned)(tag >> 32)) ? 128 : 2 * (done + LEAD);
#pragma unroll 1
        while (pre < 128 && done < B1_NT) {
          const int n = pre / 2 - LEAD - done;
          prod_run<8, B1_NS>(n, issue);
          done += n;
          wait_vm<(B1_NS - 2) * 8>();
          bar();
          pre = flag_prefix<2>(sy + SY_LN + lane, 64, 2 * (done + LEAD) + 2, tag, lane);
          issue();
          ++done;
        }
        prod_run<8, B1_NS>(B1_NT - done, issue);
        prod_tail<8, B1_NS - 2>();
      }
    }
    bar();
    const int nU = nt2 - nC;
    int us = 0;
    auto u_tile = [&](int s_) { return ku0 + ((s_ & 7) == dt ? (s_ + 8 < nU ? s_ + 8 : dt) : s_); };
    if (pw == 1 || pw == 2) {
      const rsrc_t rxx = mk_rsrc(a.x, S * D * 2);
      const int dk = d0 + (roleA ? 0 : 64);
#pragma unroll
      for (int k = 0; k < 8; ++k) dma16(rxx, (unsigned)((lane >> 3) * (D * 2) + ((lane & 7) ^ (lane >> 3)) * 16), (unsigned)((t0 + 64 * (pw - 1) + 8 * k) * D + dk) * 2u, lds + LDS_RES + (8 * (pw - 1) + k) * 1024);
      if (pw == 1) dma16c(rsy, (unsigned)lane * 16u, (unsigned)(SY_ST + t0) * 8u, lds + LDS_RES2);
      else { dma4(mk_rsrc(a.b2, S * 2), (unsigned)lane * 4u, (unsigned)t0 * 2u, lds + LDS_RES2 + 1024); dma4(mk_rsrc(a.Dsk, D * 2), (unsigned)lane * 4u, (unsigned)dk * 2u, lds + LDS_RES2 + 1280); }
      wait_vm<0>();
      if (lane == 0) __hip_atomic_fetch_add((LAS unsigned*)(size_t)(LDS_CNT + 12), 1u, __ATOMIC_RELAXED, __HIP_MEMORY_SCOPE_WORKGROUP);
    }
    if (pw == 0 || pw == 3) {
      const int dh = d0 + (((pw ? 1 : 0) ^ (roleA ? 0 : 1)) << 6), r4 = lane >> 4;
      const unsigned vce = (unsigned)(dh + (lane >> 3)) * (P * 2) + (unsigned)(((lane & 7) ^ (lane >> 4)) * 16), vco = vce ^ 64u;
      const unsigned vu = (unsigned)((dh + (lane >> 2)) * 64 + (((lane & 3) ^ (((r4 & 1) << 1) ^ ((r4 >> 1) * 3))) * 16));
      const rsrc_t ru = mk_rsrc(a.ws + WS_U, D * F * 2);
      rsrc_t rc = mk_rsrc(a.Cre, D * P * 2), rc2 = mk_rsrc(a.Cim, D * P * 2);
      unsigned so = 0, lo = pw ? 8192 : 0, sh = 0;
      auto issueC = [&]() {
        so = ((unsigned)cbs & 15u) * 128;
#pragma unroll
        for (int j = 0; j < 8; ++j) dma16(rc, (j & 1) ? vco : vce, so + j * 8 * (P * 2), lds + lo + j * 1024);
        const rsrc_t t_ = rc; rc = rc2; rc2 = t_;
        cbs >>= sh; sh ^= 4; lo = (lo + P2_STAGE) & (P2_NS * P2_STAGE - 1);
      };
      auto issueU = [&]() {
#pragma unroll
        for (int j = 0; j < 8; ++j) dma16c(ru, vu, so + (j & 1) * (D * 64) + (j >> 1) * 1024, lds + lo + j * 1024);
        so = u_tile(++us) * (2 * D * 64); lo = (lo + P2_STAGE) & (P2_NS * P2_STAGE - 1);
      };
      prod_fill(P2_NS - 1, issueC);
      if (roleA) {
        prod_run<8, P2_NS>(nC - 6, issueC);
        dma16c(rsy, (unsigned)(lane & 7) * 16u, (unsigned)(SY_U + dt * 16) * 8u, lds + LDS_FLG + (pw ? 1024 : 0));
        prod_run<8, P2_NS>(3, issueC);
      } else {
        prod_run<8, P2_NS>(nC - 3, issueC);
      }
      so = u_tile(0) * (2 * D * 64);
      constexpr int F0 = 2 * NBA / 7;
      const int f0 = roleA ? 0 : F0;
      gu64* uf = sy + SY_U + dt * 16 + ((f0 + lane) & 15);
      LAS char* ufl = lds + LDS_FLG + (pw ? 1024 : 0);
      int done = 0, pre = 0;
      bool look = roleA;
#pragma unroll 1
      while (pre < 128 && done < nU) {
        wait_vm<16>();
        bar();
        const int kn = ku0 + done + 8 < 55 ? ku0 + done + 8 : 55, need = roleA ? 16 : (2 * kn + 1) / 7 + 1 - f0;
        int p2 = 0;
        if (look) {
          const u64 m = ~__ballot(lane >= 16 - f0 || *(LAS u64*)(ufl + ((f0 + lane) & 15) * 8) == tag);
          p2 = m ? __builtin_ctzll(m) : 128;
        }
        pre = p2 >= need ? p2 : flag_prefix<1>(uf, 16 - f0, need, tag, lane);
        issueU();
        ++done;
        if (pre < 128) {
          const int n = ((7 * (f0 + pre)) >> 1) - ku0 - 8 - done;
          look = n >= 3;
          prod_run<8, P2_NS>(look ? n - 3 : n, issueU);
          if (look) {
            dma16c(rsy, (unsigned)(lane & 7) * 16u, (unsigned)(SY_U + dt * 16) * 8u, ufl);
            prod_run<8, P2_NS>(3, issueU);
          }
          done += n;
        }
      }
      prod_run<8, P2_NS>(nU - done, issueU);
    } else {
      unsigned voff[2];
#pragma unroll
      for (int j = 0; j < 2; ++j) {
        const int k = j * 8 + (lane >> 4), pos = lane & 15;
        const int c = pos ^ (((k & 3) << 1) | (((k >> 3) & 1) << 3));
        voff[j] = (unsigned)(((pw - 1) * 32 + (lane >> 4)) * S + t0 + c * 8) * 2u;
      }
      const unsigned vh = (unsigned)(lane * 16 + (pw - 1) * 8192);
      const rsrc_t rh = mk_rsrc(a.ws + WS_HT, 2 * P * S * 2), rw = mk_rsrc(a.w2, F * S * 2);
      unsigned so = 0, lo = 16384 + (pw - 1) * 8192, sh = 0;
      const int nf = nC / 2 + (roleA ? 0 : 1);
      const int hfi = (int)((unsigned)(cbs >> (4 * (lane & 15))) & 15u);
      gu64* hf = sy + SY_H + (lane == nC / 2 ? chunk * 16 + cb : (tt >> 1) * 16 + hfi);
      auto issueH = [&]() {
        so = (tt * 32 + sh * 4 + ((unsigned)cbs & 15u)) * 16384;
#pragma unroll
        for (int j = 0; j < 8; ++j) dma16c(rh, vh, so + j * 1024, lds + lo + j * 1024);
        cbs >>= sh; sh ^= 4; lo = (lo + P2_STAGE) & (P2_NS * P2_STAGE - 1);
      };
      auto issueW = [&]() {
#pragma unroll
        for (int j = 0; j < 8; ++j) dma16(rw, voff[(j >> 1) & 1], so + j * 4 * (S * 2), lds + lo + j * 1024);
        so = u_tile(++us) * (64 * S * 2); lo = (lo + P2_STAGE) & (P2_NS * P2_STAGE - 1);
      };
      if (roleA) lds_wait(LDS_CNT, 4u);
      int done = P2_NS - 1, pre = flag_prefix<1>(hf, nf, 2, tag, lane);
      prod_fill(P2_NS - 1, issueH);
#pragma unroll 1
      while (pre < 128 && done < nC) {
        const int n = (2 * pre < nC ? 2 * pre : nC) - done;
        const bool look = n >= 3;
        prod_run<8, P2_NS>(look ? n - 3 : n, issueH);
        if (look) {
          dma16c(rsy, (unsigned)(lane & 7) * 16u, (unsigned)(SY_H + (tt >> 1) * 16) * 8u, lds + LDS_HFL + (pw - 1) * 1024);
          prod_run<8, P2_NS>(3, issueH);
        }
        done += n;
        if (done < nC) {
          wait_vm<16>();
          bar();
          int p2 = 0;
          if (look) {
            const u64 m = ~__ballot(lane >= nf || (lane < nC / 2 && *(LAS u64*)(lds + LDS_HFL + (pw - 1) * 1024 + hfi * 8) == tag));
            p2 = m ? __builtin_ctzll(m) : 128;
          }
          pre = p2 > done / 2 ? p2 : flag_prefix<1>(hf, nf, done / 2 + 1, tag, lane);
          issueH();
          ++done;
        }
      }
      prod_run<8, P2_NS>(nC - done, issueH);
      so = u_tile(0) * (64 * S * 2);
      if (pre < 128) (void)flag_prefix<1>(hf, nf, nf, tag, lane);
      prod_run<8, P2_NS>(nU, issueW);
    }
    prod_tail<8, P2_NS - 2>();
    return;
  }

  const int wm = wid >> 1, wn = wid & 1;
  int cl = lane & 15, g = lane >> 4, q4 = cl >> 2, p4 = lane & 3, ln = lane;
#define RELANE() do { ln = __builtin_amdgcn_mbcnt_hi(~0u, __builtin_amdgcn_mbcnt_lo(~0u, 0u)); asm volatile("" : "+v"(ln)); cl = ln & 15; g = ln >> 4; q4 = cl >> 2; p4 = ln & 3; } while (0)
  const int kh = roleA ? 0 : 1, pa = bid ^ 1;
  const rsrc_t rxb = mk_rsrc(a.ws + WS_XB, 256 * 16384);
  f32x4 res[2][4];
  auto residual = [&]() {
    lds_wait(LDS_CNT + 12, 2u);
#pragma unroll
    for (int n = 0; n < 4; ++n) {
      const int tl = 64 * wn + 16 * n + cl;
      const unsigned ms = *(LAS unsigned*)(lds + LDS_RES2 + tl * 8);
      const float rs_ = __builtin_amdgcn_rcpf(bfhi(ms)), mb = bflo(ms), b2 = bf2f(*(LAS u16*)(lds + LDS_RES2 + 1024 + tl * 2));
#pragma unroll
      for (int mi = 0; mi < 2; ++mi) {
        const int c8 = 4 * wm + 2 * mi + (g >> 1);
        const u32x2 xv = *(LAS u32x2*)(lds + LDS_RES + tl * 128 + ((c8 ^ (tl & 7)) * 16) + (g & 1) * 8);
        const u32x2 dk = *(LAS u32x2*)(lds + LDS_RES2 + 1280 + c8 * 16 + (g & 1) * 8);
#pragma unroll
        for (int h = 0; h < 2; ++h) {
          const float x0 = bflo(xv[h]), x1 = bfhi(xv[h]);
          const unsigned xn = pk_bf16((x0 - mb) * rs_, (x1 - mb) * rs_);
          res[mi][n][2 * h] = fmaf(bflo(xn), bflo(dk[h]), x0 + b2); res[mi][n][2 * h + 1] = fmaf(bfhi(xn), bfhi(dk[h]), x1 + b2);
        }
        asm volatile("" : "+v"(res[mi][n]));
      }
    }
  };

  if (roleA) {
    HOLD_TAG();
    RELANE();
    const int p0 = cb * 64;
    const unsigned rb = (unsigned)(cl * 128 + ((g ^ (cl >> 1)) * 16));
    unsigned pa0 = rb + wm * 16384, pa1 = pa0 ^ 64u, pb0 = rb + 32768 + wn * 8192, pb1 = pb0 ^ 64u;
    f32x4 acc[8][4];
#pragma unroll
    for (int m = 0; m < 8; ++m)
#pragma unroll
      for (int n = 0; n < 4; ++n) acc[m][n] = (f32x4){0.f, 0.f, 0.f, 0.f};
    bf16x8 xa[8], xb[4], ya[8], yb[4];
    auto rd = [&](bf16x8* fa, bf16x8* fb, unsigned pa_, unsigned pb_, int r) {
      if (r == 0) fa[0] = lds_rd128(pa_); else if (r < 5) fb[r - 1] = lds_rd128(pb_ + (r - 1) * 2048); else fa[r - 4] = lds_rd128(pa_ + (r - 4) * 2048);
    };
    int slot = 0, dl = 0;
    wait_vm0_known();
    bar();
#pragma unroll
    for (int r = 0; r < 12; ++r) rd(xa, xb, pa0, pb0, r);
#pragma unroll 1
    for (int t = 0; t < A1_NT; ++t) {
      SCHED_FENCE(); wait_lgkm0(); SCHED_FENCE();
      phase<32>(acc, xa, xb, [&](int i) {
        if (i % A1_RS == 0 && i / A1_RS < 12) rd(ya, yb, pa1, pb1, i / A1_RS);
        if (i == 25) { dl = (slot == A1_NS - 1) ? -(A1_NS - 1) * A1_STAGE : A1_STAGE; slot = (slot == A1_NS - 1) ? 0 : slot + 1; }
        if (i == 27) { pa0 += dl; pb0 += dl; asm volatile("" : "+v"(pa0), "+v"(pb0)); }
        if (i == 29) { pa1 += dl; pb1 += dl; asm volatile("" : "+v"(pa1), "+v"(pb1)); }
      });
      wait_lgkm0();
      bar();
      SCHED_FENCE();
      phase<32>(acc, ya, yb, [&](int i) { if (i % A1_RS == 0 && i / A1_RS < 12) rd(xa, xb, pa0, pb0, i / A1_RS); });
    }
    RELANE();
    float cf[2][14], sb[4];
#pragma unroll
    for (int q = 0; q < 2; ++q) {
#pragma unroll
      for (int k = 0; k < 14; ++k) cf[q][k] = ((LAS float*)(lds + LDS_COEF))[k * 64 + 32 * wn + 16 * q + cl];
      sb[2 * q] = ((LAS float*)(lds + LDS_SB))[32 * wn + 16 * q + cl]; sb[2 * q + 1] = ((LAS float*)(lds + LDS_SB))[64 + 32 * wn + 16 * q + cl];
    }
    f32x2 er[2], ei[2];
#pragma unroll
    for (int q = 0; q < 2; ++q) { er[q] = bc2(0.f); ei[q] = bc2(0.f); }
#pragma unroll
    for (int m = 0; m < 8; ++m)
#pragma unroll
      for (int jj = 0; jj < 2; ++jj) {
        const f32x4 st = *(LAS f32x4*)(lds + LDS_STAT + (4 * wm + g) * 272 + (2 * m + jj) * 16);
        const f32x2 c2 = {st[0], st[1]}, r2 = {st[2], st[3]};
#pragma unroll
        for (int q = 0; q < 2; ++q) {
          const f32x2 ur = fma2(c2, bc2(sb[2 * q]), (f32x2){acc[m][2 * q][2 * jj], acc[m][2 * q][2 * jj + 1]}) * r2;
          const f32x2 ui = fma2(c2, bc2(sb[2 * q + 1]), (f32x2){acc[m][2 * q + 1][2 * jj], acc[m][2 * q + 1][2 * jj + 1]}) * r2;
          acc[m][2 * q][2 * jj] = ur.x; acc[m][2 * q][2 * jj + 1] = ur.y; acc[m][2 * q + 1][2 * jj] = ui.x; acc[m][2 * q + 1][2 * jj + 1] = ui.y;
          const f32x2 nr = fma2(bc2(cf[q][0]), er[q], fma2(bc2(-cf[q][1]), ei[q], ur));
          const f32x2 ni = fma2(bc2(cf[q][0]), ei[q], fma2(bc2(cf[q][1]), er[q], ui));
          er[q] = nr; ei[q] = ni;
        }
        if (jj == 1 && (m & 1)) asm volatile("" : "+v"(er[0]), "+v"(ei[0]), "+v"(er[1]), "+v"(ei[1]));
      }
    float sr[2], si[2], pr_[2], pi_[2];
    LAS float* tot = (LAS float*)(lds + LDS_TOT);
#pragma unroll
    for (int q = 0; q < 2; ++q) {
      float tr, ti;
      CMUL(tr, ti, cf[q][4], cf[q][5], er[q].x, ei[q].x);
      sr[q] = tr + er[q].y; si[q] = ti + ei[q].y;
      float vr = __shfl_up(sr[q], 16), vi = __shfl_up(si[q], 16);
      CMUL(tr, ti, cf[q][6], cf[q][7], vr, vi);
      if (g >= 1) { sr[q] += tr; si[q] += ti; }
      vr = __shfl_up(sr[q], 32); vi = __shfl_up(si[q], 32);
      CMUL(tr, ti, cf[q][8], cf[q][9], vr, vi);
      if (g >= 2) { sr[q] += tr; si[q] += ti; }
      vr = __shfl_up(sr[q], 16); vi = __shfl_up(si[q], 16);
      pr_[q] = g >= 1 ? vr : 0.f; pi_[q] = g >= 1 ? vi : 0.f;
      if (wm == 0 && g == 3) *(LAS f32x2*)(tot + (32 * wn + 16 * q + cl) * 2) = (f32x2){sr[q], si[q]};
    }
    if (wm == 0) {
      wait_lgkm0();
      if (ln == 0) __hip_atomic_store((LAS unsigned*)(size_t)(LDS_CNT + 4 + 4 * wn), 1u, __ATOMIC_RELAXED, __HIP_MEMORY_SCOPE_WORKGROUP);
    } else {
      lds_wait(LDS_CNT + 4 + 4 * wn, 1u);
    }
    f32x2 tq[2];
#pragma unroll
    for (int q = 0; q < 2; ++q) {
      tq[q] = *(LAS f32x2*)(tot + (32 * wn + 16 * q + cl) * 2);
      if (wm == 1 && g == 3 && chunk < 7) {
        float tr, ti;
        CMUL(tr, ti, cf[q][10], cf[q][11], tq[q].x, tq[q].y);
        st16_wt(rsy, (unsigned)(SY_GR * 8 + (chunk * P + p0 + 32 * wn + 16 * q + cl) * 16), 0, (u32x4){__float_as_uint(tr + sr[q]), 1u, __float_as_uint(ti + si[q]), 1u});
      }
    }
#pragma unroll
    for (int m = 0; m < 8; ++m)
#pragma unroll
      for (int q = 0; q < 2; ++q) {
        const f32x4 ur = acc[m][2 * q], ui = acc[m][2 * q + 1];
        acc[m][2 * q] = ur * cf[q][2] - ui * cf[q][3];
        acc[m][2 * q + 1] = ui * (-cf[q][2]) - ur * cf[q][3];
        asm volatile("" : "+v"(acc[m][2 * q]), "+v"(acc[m][2 * q + 1]));
      }
    float hr[2] = {0.f, 0.f}, hi[2] = {0.f, 0.f};
    if (chunk > 0) {
      float gr[2][2], gi[2][2];
#pragma unroll 1
      for (unsigned spins = 0;; ++spins) {
        bool ok = true;
#pragma unroll
        for (int q = 0; q < 2; ++q)
#pragma unroll
          for (int k = 0; k < 2; ++k) {
            const int cc = (g + 4 * k < chunk) ? g + 4 * k : 0;
            const u32x4 v = __builtin_amdgcn_raw_buffer_load_b128(rsy, (unsigned)(SY_GR * 8 + (cc * P + p0 + 32 * wn + 16 * q + cl) * 16), 0, 16);
            ok &= (v[1] == 1u) & (v[3] == 1u);
            gr[q][k] = __uint_as_float(v[0]); gi[q][k] = __uint_as_float(v[2]);
          }
        if (__all(ok) || spins > SPIN_CAP) break;
        __builtin_amdgcn_s_sleep(1);
        asm volatile("" ::: "memory");
      }
#pragma unroll
      for (int q = 0; q < 2; ++q) {
        float ar_ = 0.f, ai_ = 0.f;
#pragma unroll
        for (int k = 0; k < 2; ++k) {
          float vr = gr[q][k], vi = gi[q][k];
          const int e = chunk - 1 - (g + 4 * k);
#pragma unroll
          for (int i = 0; i < 6 - 4 * k; ++i) {
            float tr, ti;
            CMUL(tr, ti, cf[q][12], cf[q][13], vr, vi);
            vr = i < e ? tr : vr; vi = i < e ? ti : vi;
          }
          ar_ += e >= 0 ? vr : 0.f; ai_ += e >= 0 ? vi : 0.f;
        }
        ar_ += __shfl_xor(ar_, 16); ai_ += __shfl_xor(ai_, 16);
        ar_ += __shfl_xor(ar_, 32); ai_ += __shfl_xor(ai_, 32);
        hr[q] = ar_; hi[q] = ai_;
      }
    }
    f32x2 wr[2], wi[2];
#pragma unroll
    for (int q = 0; q < 2; ++q) {
      float tr, ti;
      if (wm == 1) { CMUL(tr, ti, cf[q][10], cf[q][11], hr[q], hi[q]); hr[q] = tr + tq[q].x; hi[q] = ti + tq[q].y; }
      CMUL(tr, ti, cf[q][6], cf[q][7], hr[q], hi[q]);
      if (g & 1) { hr[q] = tr; hi[q] = ti; }
      CMUL(tr, ti, cf[q][8], cf[q][9], hr[q], hi[q]);
      if (g & 2) { hr[q] = tr; hi[q] = ti; }
      hr[q] += pr_[q]; hi[q] += pi_[q];
      CMUL(tr, ti, cf[q][4], cf[q][5], hr[q], hi[q]);
      const f32x2 h0r = {hr[q], tr + er[q].x}, h0i = {hi[q], ti + ei[q].x};
      wr[q] = h0r * cf[q][2] - h0i * cf[q][3];
      wi[q] = h0i * (-cf[q][2]) - h0r * cf[q][3];
    }
    LAS char* scr = lds + wid * P2_STAGE + 16384;
    const unsigned fsw = (unsigned)(((cl & 3) << 1) | (((cl >> 3) & 1) << 3));
    const rsrc_t rh = mk_rsrc(a.ws + WS_HT, 2 * P * S * 2);
#pragma unroll
    for (int q = 0; q < 2; ++q) {
      const f32x2 ar2 = bc2(cf[q][0]), ai2 = bc2(cf[q][1]);
#pragma unroll
      for (int mh = 0; mh < 2; ++mh) {
        u32x4 o[2][2];
#pragma unroll
        for (int mq = 0; mq < 4; ++mq) {
          const int m = 4 * mh + mq;
          const f32x2 u0r = {acc[m][2 * q][0], acc[m][2 * q][1]}, u0i = {acc[m][2 * q + 1][0], acc[m][2 * q + 1][1]};
          const f32x2 u1r = {acc[m][2 * q][2], acc[m][2 * q][3]}, u1i = {acc[m][2 * q + 1][2], acc[m][2 * q + 1][3]};
          const f32x2 n0r = fma2(ar2, wr[q], fma2(ai2, wi[q], u0r)), n0i = fma2(ar2, wi[q], fma2(-ai2, wr[q], u0i));
          const f32x2 n1r = fma2(ar2, n0r, fma2(ai2, n0i, u1r)), n1i = fma2(ar2, n0i, fma2(-ai2, n0r, u1i));
          wr[q] = n1r; wi[q] = n1i;
          o[0][0][mq] = pk_bf16(n0r.x, n1r.x); o[0][1][mq] = pk_bf16(n0r.y, n1r.y);
          o[1][0][mq] = pk_bf16(n0i.x, n1i.x); o[1][1][mq] = pk_bf16(n0i.y, n1i.y);
        }
#pragma unroll
        for (int part = 0; part < 2; ++part)
#pragma unroll
          for (int run = 0; run < 2; ++run)
            *(LAS u32x4*)(scr + (part * 32 + 16 * q + cl) * 256 + (((unsigned)(4 * g + 2 * run + mh) ^ fsw) * 16)) = o[part][run];
      }
#pragma unroll
      for (int i = 0; i < 8; ++i) {
        const int part = i >> 2, pc = 4 * q + (i & 3);
        const u32x4 v = *(LAS u32x4*)(scr + part * 8192 + pc * 1024 + ln * 16);
        st16_wt(rh, (unsigned)(ln * 16 + pc * 1024), (unsigned)(((2 * chunk + wm) * 32 + part * 16 + cb) * 16384 + wn * 8192), v);
      }
      SCHED_FENCE();
    }
  } else {
    RELANE();
    const int f0 = ft * 224, dd0 = dt1 * 128;
    const int k0 = 8 * g + q4;
    unsigned pa0 = (unsigned)(k0 * 448 + ((12 * wm + p4) ^ (3 * (g & 1))) * 16 + (p4 & 1) * 8);
    unsigned pa1 = (unsigned)(k0 * 448 + ((12 * wm + p4) ^ (3 * (g & 1))) * 16 + ((p4 & 1) ^ 1) * 8);
    unsigned pa2 = (unsigned)(k0 * 448 + ((24 + 2 * wm + (p4 >> 1)) ^ (3 * (g & 1))) * 16 + (p4 & 1) * 8);
    unsigned pb[4];
#pragma unroll
    for (int n = 0; n < 4; ++n) pb[n] = (unsigned)(B1_ABYTES + k0 * 256 + ((8 * wn + 2 * n + (p4 >> 1)) ^ ((q4 << 1) | ((g & 1) << 3))) * 16 + (p4 & 1) * 8);
    f32x4 acc[7][4];
#pragma unroll
    for (int m = 0; m < 7; ++m) {
      const u32x2 bv = *(const u32x2*)(a.b1 + f0 + (m < 6 ? 96 * wm + 32 * (m >> 1) + 8 * g + 4 * ((m & 1) ^ (g & 1)) : 192 + 16 * wm + 4 * g));
#pragma unroll
      for (int n = 0; n < 4; ++n) acc[m][n] = (f32x4){bflo(bv[0]), bfhi(bv[0]), bflo(bv[1]), bfhi(bv[1])};
    }
    TrFrag xa[7], xb[4], ya[7], yb[4];
    int slot = 0, dl = 0;
    auto side = [&](TrFrag* fa, TrFrag* fb, int i) {
      const int f = i >> 1, h = i & 1;
      if (f == 0) fa[0].h[h] = lds_rdtr(pa0 + h * 1792);
      else if (f < 5) fb[f - 1].h[h] = lds_rdtr(pb[f - 1] + h * 1024);
      else if (f < 10) fa[f - 4].h[h] = lds_rdtr((((f - 4) & 1) ? pa1 : pa0) + ((f - 4) >> 1) * 64 + h * 1792);
      else if (f == 10) fa[6].h[h] = lds_rdtr(pa2 + h * 1792);
      else if (i == 22) { dl = (slot == B1_NS - 1) ? -(B1_NS - 1) * B1_STAGE : B1_STAGE; slot = (slot == B1_NS - 1) ? 0 : slot + 1; }
      else if (i == 23) { pa0 += dl; pb[0] += dl; asm volatile("" : "+v"(pa0), "+v"(pb[0])); }
      else if (i == 24) { pb[1] += dl; pb[2] += dl; asm volatile("" : "+v"(pb[1]), "+v"(pb[2])); }
      else if (i == 25) { pb[3] += dl; pa1 += dl; asm volatile("" : "+v"(pb[3]), "+v"(pa1)); }
      else if (i == 26) { pa2 += dl; asm volatile("" : "+v"(pa2)); }
    };
    wait_vm0_known();
    bar();
#pragma unroll
    for (int i = 0; i < 28; ++i) side(xa, xb, i);
#pragma unroll 1
    for (int t = 0; t < B1_NT; t += 2) {
      SCHED_FENCE(); wait_lgkm0(); bar(); SCHED_FENCE();
      phase<28>(acc, xa, xb, [&](int i) { side(ya, yb, i); });
      wait_lgkm0(); bar(); SCHED_FENCE();
      phase<28>(acc, ya, yb, [&](int i) { side(xa, xb, i); });
    }
    RELANE();
    const rsrc_t ru = mk_rsrc(a.ws + WS_U, D * F * 2);
    auto act4 = [&](const f32x4& v) {
      const f32x2 v0 = gelu_tanh2((f32x2){v[0], v[1]}), v1 = gelu_tanh2((f32x2){v[2], v[3]});
      return (u32x2){pk_bf16(v0.x, v0.y), pk_bf16(v1.x, v1.y)};
    };
#pragma unroll
    for (int n = 0; n < 4; ++n) *(LAS u32x2*)(lds + LDS_U6 + (64 * wn + 16 * n + cl) * 64 + 32 * wm + 8 * g) = act4(acc[6][n]);
    wait_lgkm0();
    if (ln == 0) __hip_atomic_fetch_add((LAS unsigned*)(size_t)(LDS_CNT + 4 + 4 * wn), 1u, __ATOMIC_RELAXED, __HIP_MEMORY_SCOPE_WORKGROUP);
#pragma unroll
    for (int n = 0; n < 4; ++n) {
      const unsigned vo = (unsigned)((dd0 + 64 * wn + 16 * n + cl) * 64);
#pragma unroll
      for (int mm = 0; mm < 3; ++mm) {
        const u32x2 w0 = act4(acc[2 * mm][n]), w1 = act4(acc[2 * mm + 1][n]);
        const bool sw = g & 1;
        const u32x2 lo = sw ? w1 : w0, hi = sw ? w0 : w1;
        st16_wt(ru, vo + 16 * g, (unsigned)((ft * 7 + 3 * wm + mm) * D * 64), (u32x4){lo[0], lo[1], hi[0], hi[1]});
      }
      if (n == 1) {
        lds_wait(LDS_CNT + 4 + 4 * wn, 2u);
#pragma unroll
        for (int k = 0; k < 2; ++k) {
          const unsigned o = (unsigned)(wn * 4096 + wm * 2048 + k * 1024 + ln * 16);
          st16_wt(ru, o, (unsigned)(((ft * 7 + 6) * D + dd0) * 64), *(LAS u32x4*)(lds + LDS_U6 + o));
        }
      }
    }
    residual();
  }

  publish(LDS_CNT, sy + (roleA ? SY_H + chunk * 16 + cb : SY_U + dt1 * 16 + ft), tag, ln);
  if (roleA) residual();

  f32x4 acc[4][4];
#pragma unroll
  for (int n = 0; n < 4; ++n) { acc[0][n] = res[0][n]; acc[1][n] = res[1][n]; acc[2][n] = (f32x4){0.f, 0.f, 0.f, 0.f}; acc[3][n] = (f32x4){0.f, 0.f, 0.f, 0.f}; }
  const unsigned au0 = (unsigned)(wm * 4096 + cl * 64 + ((g ^ (((q4 & 1) << 1) ^ ((q4 >> 1) * 3))) * 16));
  unsigned pa0 = (unsigned)((wm * 32 + cl) * 128 + ((g ^ (cl >> 1)) * 16)), pa1 = pa0 ^ 64u;
  const unsigned sw0 = pa0 ^ au0 ^ 65536u, sw1 = pa1 ^ (au0 + 1024u) ^ 65536u;
  unsigned pb[4];
#pragma unroll
  for (int n = 0; n < 4; ++n) pb[n] = (unsigned)(16384 + (8 * g + q4) * 256 + ((8 * wn + 2 * n + (p4 >> 1)) ^ ((q4 << 1) | ((g & 1) << 3))) * 16 + (p4 & 1) * 8);
  bf16x8 xa[4], ya[4];
  TrFrag xb[4], yb[4];
  static_assert(P2_NS == 4 && NAB % 2 == 0 && NBA % 2 == 0 && NAB >= P2_NS && NAB <= 16, "phase-2 loop: two stages per trip, 64-KB windows");
  auto rd = [&](bf16x8* fa, TrFrag* fb, int so, int ks, int r) {
    const unsigned pa_ = (ks ? pa1 : pa0) + so;
    if (r == 0) fa[0] = lds_rd128(pa_);
    else if (r < 9) fb[(r - 1) >> 1].h[(r - 1) & 1] = lds_rdtr(pb[(r - 1) >> 1] + so + ks * 8192 + ((r - 1) & 1) * 1024);
    else if (r < 12) fa[r - 8] = lds_rd128(pa_ + ((r - 8) >> 1) * 8192 + ((r - 8) & 1) * 2048);
  };
  bar();
#pragma unroll
  for (int r = 0; r < 12; ++r) rd(xa, xb, 0, 0, r);
#pragma unroll 1
  for (int t = 0; t < nt2; t += 2) {
    unsigned x0, x1;
    SCHED_FENCE(); wait_lgkm0(); SCHED_FENCE();
    phase<16>(acc, xa, xb, [&](int i) { rd(ya, yb, 0, 1, i); });
    wait_lgkm0(); bar(); SCHED_FENCE();
    phase<16>(acc, ya, yb, [&](int i) {
      rd(xa, xb, P2_STAGE, 0, i);
      if (i == 13) { const bool sw = (t + 2 == nC); x0 = sw ? sw0 : 65536u; x1 = sw ? sw1 : 65536u; asm volatile("" : "+v"(x0), "+v"(x1)); }
      if (i == 14) { pa0 ^= x0; asm volatile("" : "+v"(pa0)); }
    });
    wait_lgkm0(); SCHED_FENCE();
    phase<16>(acc, xa, xb, [&](int i) {
      rd(ya, yb, P2_STAGE, 1, i);
      if (i == 12) { pb[0] ^= 65536u; pb[1] ^= 65536u; asm volatile("" : "+v"(pb[0]), "+v"(pb[1])); }
      if (i == 13) { pb[2] ^= 65536u; pb[3] ^= 65536u; asm volatile("" : "+v"(pb[2]), "+v"(pb[3])); }
      if (i == 14) { pa1 ^= x1; asm volatile("" : "+v"(pa1)); }
    });
    wait_lgkm0(); bar(); SCHED_FENCE();
    phase<16>(acc, ya, yb, [&](int i) { rd(xa, xb, 0, 0, i); });
  }
  const unsigned xo = (unsigned)(wid * 4096 + ln * 16);
#pragma unroll
  for (int n = 0; n < 4; ++n) {
    u32x4 w = {pk_bf16(acc[2][n][0], acc[2][n][1]), pk_bf16(acc[2][n][2], acc[2][n][3]), pk_bf16(acc[3][n][0], acc[3][n][1]), pk_bf16(acc[3][n][2], acc[3][n][3])};
#pragma unroll
    for (int k = 0; k < 4; ++k) w[k] = w[k] ? w[k] : 0x80000000u;
    st16_wt(rxb, xo + n * 1024, bid * 16384, w);
  }
  u32x4 rv[4];
#pragma unroll 1
  for (unsigned spins = 0; spins < SPIN_CAP; ++spins) {
    bool ok = true;
#pragma unroll
    for (int n = 0; n < 4; ++n) rv[n] = __builtin_amdgcn_raw_buffer_load_b128(rxb, xo + n * 1024 + pa * 16384, 0, 16);
#pragma unroll
    for (int n = 0; n < 4; ++n) ok &= (rv[n][0] != 0u) & (rv[n][1] != 0u) & (rv[n][2] != 0u) & (rv[n][3] != 0u);
    if (__all(ok)) break;
#pragma unroll 1
    for (; spins < SPIN_CAP; ++spins) {
      __builtin_amdgcn_s_sleep(2);
      asm volatile("" ::: "memory");
      if (__builtin_amdgcn_readfirstlane(__builtin_amdgcn_raw_buffer_load_b32(rxb, wid * 4096 + 4092 + pa * 16384, 0, 16)) != 0u) break;
    }
    asm volatile("" ::: "memory");
  }
#pragma unroll
  for (int n = 0; n < 4; ++n) {
    acc[0][n] += (f32x4){bflo(rv[n][0]), bfhi(rv[n][0]), bflo(rv[n][1]), bfhi(rv[n][1])};
    acc[1][n] += (f32x4){bflo(rv[n][2]), bfhi(rv[n][2]), bflo(rv[n][3]), bfhi(rv[n][3])};
  }
  {
    LAS char* tb = lds + wid * 8192;
#pragma unroll
    for (int n = 0; n < 4; ++n)
#pragma unroll
      for (int mi = 0; mi < 2; ++mi) *(LAS f32x4*)(tb + (16 * n + cl) * 128 + (((4 * mi + g) ^ (cl & 7)) * 16)) = acc[mi][n];
    const rsrc_t rout = mk_rsrc(a.out, S * D * 4);
#pragma unroll
    for (int k = 0; k < 8; ++k) {
      const u32x4 v = *(LAS u32x4*)(tb + k * 1024 + ln * 16);
      __builtin_amdgcn_raw_buffer_store_b128(v, rout, (unsigned)((t0 + 64 * wn + 8 * k + (ln >> 3)) * D + d0 + 64 * kh + 32 * wm) * 4u + (unsigned)(((ln & 7) ^ (ln >> 3)) * 16), 0, 16);
    }
  }
}

extern "C" void kernel_launch(void* const* d_in, const int* in_sizes, int n_in, void* d_out, int out_size, void* d_ws, size_t ws_size,
                              hipStream_t stream) {
  (void)hipFuncSetAttribute((const void*)fused, hipFuncAttributeMaxDynamicSharedMemorySize, LDS_BYTES);
  Args a;
  a.x = (const u16*)d_in[0]; a.Lre = (const u16*)d_in[1]; a.Lim = (const u16*)d_in[2]; a.Bre = (const u16*)d_in[3]; a.Bim = (const u16*)d_in[4];
  a.Cre = (const u16*)d_in[5]; a.Cim = (const u16*)d_in[6]; a.logdt = (const u16*)d_in[7]; a.Dsk = (const u16*)d_in[8];
  a.w1 = (const u16*)d_in[10]; a.b1 = (const u16*)d_in[11]; a.w2 = (const u16*)d_in[12]; a.b2 = (const u16*)d_in[13];
  a.out = (float*)d_out; a.ws = (char*)d_ws;
  fused<<<256, 512, LDS_BYTES, stream>>>(a);
}
```

```cpp
#include <hip/hip_runtime.h>
#include <stdint.h>

typedef unsigned short u16;
typedef unsigned long long u64;
typedef __bf16 bf16x8 __attribute__((ext_vector_type(8)));
typedef short s16x4 __attribute__((ext_vector_type(4)));
typedef short s16x8 __attribute__((ext_vector_type(8)));
typedef float f32x2 __attribute__((ext_vector_type(2)));
typedef float f32x4 __attribute__((ext_vector_type(4)));
typedef unsigned u32x4 __attribute__((ext_vector_type(4)));
typedef unsigned u32x2 __attribute__((ext_vector_type(2)));
#define LAS __attribute__((address_space(3)))
#define GAS __attribute__((address_space(1)))
typedef GAS unsigned long long gu64;
typedef __amdgpu_buffer_rsrc_t rsrc_t;
#define RLX_AGENT __ATOMIC_RELAXED, __HIP_MEMORY_SCOPE_AGENT

constexpr int S = 2048, D = 1024, P = 1024, F = 3584;
constexpr size_t MB = 1024 * 1024;
constexpr size_t WS_HL = 4 * MB, WS_HT = 8 * MB, WS_U = 16 * MB, WS_XB = 24 * MB, WS_SYNC = 32 * MB;
constexpr int SY_LN = 64, SY_H = 320, SY_U = 448, SY_ST = 1024, SY_SB = 3072, SY_GR = 5120;
constexpr unsigned SYNC_BYTES = (SY_GR + 8 * P * 2) * 8;
constexpr unsigned SPIN_CAP = 1u << 17;
constexpr int NBA = 26;
constexpr int NAB = 6;
constexpr unsigned long long CB_ORDER = 0x80F7E6D5C491B3A2ull;
constexpr unsigned CODE_PF_BYTES = 40 * 1024;
constexpr int LEAD = 8, LLIM = 32;
constexpr int PF_N0 = 2;
constexpr int PF_N2 = 7;
constexpr int PF_WB = 2;
constexpr int A1_RS = 2;

__device__ __forceinline__ float bf2f(u16 v) { return __uint_as_float(((unsigned)v) << 16); }
__device__ __forceinline__ float bflo(unsigned v) { return __uint_as_float(v << 16); }
__device__ __forceinline__ float bfhi(unsigned v) { return __uint_as_float(v & 0xffff0000u); }
__device__ __forceinline__ unsigned pk_bf16(float lo, float hi) {
  typedef __bf16 bf16x2 __attribute__((ext_vector_type(2)));
  f32x2 v = {lo, hi};
  bf16x2 r = __builtin_convertvector(v, bf16x2);
  return __builtin_bit_cast(unsigned, r);
}
__device__ __forceinline__ float rbf(float f) { return bflo(pk_bf16(f, f)); }
__device__ __forceinline__ float dot2(unsigned a, unsigned b, float c) {
  typedef __bf16 bf16x2 __attribute__((ext_vector_type(2)));
  return __builtin_amdgcn_fdot2_f32_bf16(__builtin_bit_cast(bf16x2, a), __builtin_bit_cast(bf16x2, b), c, false);
}
__device__ __forceinline__ f32x2 fma2(f32x2 a, f32x2 b, f32x2 c) { return __builtin_elementwise_fma(a, b, c); }
__device__ __forceinline__ f32x2 bc2(float v) { return (f32x2){v, v}; }

__device__ __forceinline__ rsrc_t mk_rsrc(const void* p, unsigned bytes) { return __builtin_amdgcn_make_buffer_rsrc((void*)p, 0, bytes, 0x00020000); }
__device__ __forceinline__ void dma16(rsrc_t rs, unsigned voff, unsigned soff, LAS char* l) { __builtin_amdgcn_raw_ptr_buffer_load_lds(rs, (LAS void*)l, 16, voff, soff, 0, 0); }
__device__ __forceinline__ void dma16c(rsrc_t rs, unsigned voff, unsigned soff, LAS char* l) { __builtin_amdgcn_raw_ptr_buffer_load_lds(rs, (LAS void*)l, 16, voff, soff, 0, 16); }
__device__ __forceinline__ void dma4(rsrc_t rs, unsigned voff, unsigned soff, LAS char* l) { __builtin_amdgcn_raw_ptr_buffer_load_lds(rs, (LAS void*)l, 4, voff, soff, 0, 0); }
__device__ __forceinline__ void st16_wt(rsrc_t rs, unsigned voff, unsigned soff, u32x4 v) { __builtin_amdgcn_raw_buffer_store_b128(v, rs, voff + soff, 0, 16); }
template <int N> __device__ __forceinline__ void wait_vm() { asm volatile("s_waitcnt vmcnt(%0)" ::"n"(N) : "memory"); }
__device__ __forceinline__ void wait_vm0_known() { asm volatile("" ::: "memory"); __builtin_amdgcn_s_waitcnt(0x0F70); asm volatile("" ::: "memory"); }
__device__ __forceinline__ void wait_lgkm0() { asm volatile("" ::: "memory"); __builtin_amdgcn_s_waitcnt(0xC07F); asm volatile("" ::: "memory"); }
__device__ __forceinline__ void bar() {
  asm volatile("" ::: "memory");
  __builtin_amdgcn_s_barrier();
  asm volatile("" ::: "memory");
}
#define SCHED_FENCE() __builtin_amdgcn_sched_barrier(0)
__device__ __forceinline__ bf16x8 lds_rd128(unsigned p) { return *(LAS bf16x8*)(size_t)p; }
__device__ __forceinline__ s16x4 lds_rdtr(unsigned p) { return __builtin_amdgcn_ds_read_tr16_b64_v4i16((LAS s16x4*)(size_t)p); }
struct TrFrag { s16x4 h[2]; };
__device__ __forceinline__ bf16x8 frag(const bf16x8& f) { return f; }
__device__ __forceinline__ bf16x8 frag(const TrFrag& f) { s16x8 ab = __builtin_shufflevector(f.h[0], f.h[1], 0, 1, 2, 3, 4, 5, 6, 7); return __builtin_bit_cast(bf16x8, ab); }
#define MFMA(a, b, c) __builtin_amdgcn_mfma_f32_16x16x32_bf16(frag(a), frag(b), (c), 0, 0, 0)
template <int NM, class FA, class FB, class Side>
__device__ __forceinline__ void phase(f32x4 (*acc)[4], const FA* fa, const FB* fb, Side side) {
#pragma unroll
  for (int i = 0; i < NM; ++i) {
    constexpr int MT = NM / 4;
    const int n_ = i / MT, r_ = i % MT, m_ = (n_ & 1) ? MT - 1 - r_ : r_;
    acc[m_][n_] = MFMA(fa[m_], fb[n_], acc[m_][n_]);
    side(i);
    SCHED_FENCE();
  }
}

template <int NL> __device__ __forceinline__ void wait_flags(gu64* p, bool active, u64 tag) {
#pragma unroll 1
  for (unsigned spins = 0; spins < SPIN_CAP; ++spins) {
    bool ok = true;
    if (active) {
#pragma unroll
      for (int k = 0; k < NL; ++k) ok &= __hip_atomic_load(p + 64 * k, RLX_AGENT) == tag;
    }
    if (__all(ok)) break;
    __builtin_amdgcn_s_sleep(2);
  }
  __builtin_amdgcn_fence(__ATOMIC_ACQUIRE, "wavefront");
}

template <int NL> __device__ __forceinline__ int flag_prefix(gu64* p, int n, int need, u64 tag, int lane) {
  int pre = 0;
#pragma unroll 1
  for (unsigned spins = 0; spins < SPIN_CAP; ++spins) {
    const u64 m0 = ~__ballot(lane >= n || __hip_atomic_load(p, RLX_AGENT) == tag);
    const u64 m1 = NL == 2 ? ~__ballot(__hip_atomic_load(p + 64, RLX_AGENT) == tag) : 0ull;
    pre = m0 ? __builtin_ctzll(m0) : m1 ? 64 + __builtin_ctzll(m1) : 128;
    if (pre >= need) break;
    __builtin_amdgcn_s_sleep(1);
  }
  __builtin_amdgcn_fence(__ATOMIC_ACQUIRE, "wavefront");
  return pre < need ? 128 : pre;
}
__device__ __forceinline__ void lds_wait(unsigned addr, unsigned want) {
#pragma unroll 1
  for (unsigned spins = 0; spins < (SPIN_CAP << 4); ++spins) {
    if (__builtin_amdgcn_readfirstlane(__hip_atomic_load((LAS unsigned*)(size_t)addr, __ATOMIC_RELAXED, __HIP_MEMORY_SCOPE_WORKGROUP)) >= want) break;
    __builtin_amdgcn_s_sleep(1);
  }
  asm volatile("" ::: "memory");
}
__device__ __forceinline__ void publish(unsigned cnt, gu64* flag, u64 tag, int lane) {
  wait_vm<0>();
  wait_vm0_known();
  wait_lgkm0();
  if (lane == 0 && __hip_atomic_fetch_add((LAS unsigned*)(size_t)cnt, 1u, __ATOMIC_RELAXED, __HIP_MEMORY_SCOPE_WORKGROUP) == 3u) __hip_atomic_store(flag, tag, RLX_AGENT);
}

template <class Issue> __device__ __forceinline__ void prod_fill(int n, Issue issue) {
#pragma unroll 1
  for (int t = 0; t < n; ++t) issue();
}
template <int L, int NS, class Issue> __device__ __forceinline__ void prod_run(int n, Issue issue) {
#pragma unroll 1
  for (int t = 0; t < n; ++t) { wait_vm<(NS - 2) * L>(); bar(); issue(); }
}
template <int L, int K, int X = 0> __device__ __forceinline__ void prod_tail() {
  wait_vm<K * L + X>();
  bar();
  if constexpr (K > 0) prod_tail<L, K - 1, X>();
}

__device__ __forceinline__ float wave_sum(float v) {
  v += __builtin_bit_cast(float, __builtin_amdgcn_update_dpp(0, __builtin_bit_cast(int, v), 0xB1, 0xf, 0xf, true));
  v += __builtin_bit_cast(float, __builtin_amdgcn_update_dpp(0, __builtin_bit_cast(int, v), 0x4E, 0xf, 0xf, true));
  v += __builtin_bit_cast(float, __builtin_amdgcn_update_dpp(0, __builtin_bit_cast(int, v), 0x141, 0xf, 0xf, true));
  v += __builtin_bit_cast(float, __builtin_amdgcn_update_dpp(0, __builtin_bit_cast(int, v), 0x140, 0xf, 0xf, true));
  const int iv = __builtin_bit_cast(int, v);
  const float a = __builtin_bit_cast(float, __builtin_amdgcn_readlane(iv, 0)), b = __builtin_bit_cast(float, __builtin_amdgcn_readlane(iv, 16));
  const float c = __builtin_bit_cast(float, __builtin_amdgcn_readlane(iv, 32)), d = __builtin_bit_cast(float, __builtin_amdgcn_readlane(iv, 48));
  return (a + b) + (c + d);
}

struct Args {
  const u16 *x, *Lre, *Lim, *Bre, *Bim, *Cre, *Cim, *logdt, *Dsk, *w1, *b1, *w2, *b2;
  float* out;
  char* ws;
};

constexpr int A1_NS = 3, A1_STAGE = 49152, A1_NT = D / 64;
constexpr int B1_NS = 7, B1_ABYTES = 32 * 448, B1_STAGE = B1_ABYTES + 32 * 256, B1_NT = S / 32;
constexpr int P2_NS = 4, P2_STAGE = 32768;
constexpr int LDS_BYTES = 160 * 1024;
constexpr int LDS_COEF = A1_NS * A1_STAGE;
constexpr int LDS_TOT = LDS_COEF + 14 * 256;
constexpr int LDS_SB = LDS_TOT + 512;
constexpr int LDS_STAT = LDS_SB + 512;
constexpr int LDS_RAW = LDS_STAT + 8 * 272;
constexpr int LDS_RES = P2_NS * P2_STAGE;
constexpr int LDS_U6 = LDS_COEF;
constexpr int LDS_HFL = LDS_COEF + 8192;
constexpr int LDS_RES2 = 158208;
constexpr int LDS_CNT = LDS_BYTES - 256 - 2048 - 64;
constexpr int LDS_FLG = LDS_BYTES - 256 - 2048;
constexpr int LDS_DUMMY = LDS_BYTES - 256;
static_assert(LDS_RAW + 3072 + 768 <= LDS_CNT && B1_NS * B1_STAGE <= LDS_CNT && LDS_RES + 16384 <= LDS_COEF && LDS_RAW + 3840 <= LDS_RES2 && LDS_RES2 + 1536 <= LDS_CNT && P2_NS * P2_STAGE <= LDS_COEF && LDS_RAW % 16 == 0, "lds");

__device__ __forceinline__ f32x2 gelu_tanh2(f32x2 x) {
  const f32x2 z = x * fma2(x * x, bc2(-0.10294323967f), bc2(-2.30220819813f));
  const f32x2 d = (f32x2){__builtin_amdgcn_exp2f(z.x), __builtin_amdgcn_exp2f(z.y)} + bc2(1.0f);
  return x * (f32x2){__builtin_amdgcn_rcpf(d.x), __builtin_amdgcn_rcpf(d.y)};
}
#define CMUL(or_, oi_, ar_, ai_, br_, bi_) do { const float t_ = (ar_) * (br_) - (ai_) * (bi_); oi_ = (ar_) * (bi_) + (ai_) * (br_); or_ = t_; } while (0)

__global__ __launch_bounds__(512, 2) void fused(Args a) {
  extern __shared__ __attribute__((aligned(16))) char smem[];
  LAS char* lds = (LAS char*)smem;
  u64 pc0;
  asm volatile("s_getpc_b64 %0" : "=s"(pc0));
  const int wid = __builtin_amdgcn_readfirstlane(threadIdx.x >> 6), lane = threadIdx.x & 63;
  const int bid = blockIdx.x, xc = bid & 7, idx = bid >> 3, xg = xc >> 1;
  const bool roleA = !(xc & 1);
  const int ri = bid >> 1;
  gu64* sy = (gu64*)(a.ws + WS_SYNC);
  u64 tag = 0;
#define LOAD_TAG() do { const __attribute__((address_space(4))) u64* ep_ = (const __attribute__((address_space(4))) u64*)(a.ws + WS_SYNC); \
    asm volatile("" : "+s"(ep_)::"memory"); tag = *ep_ + 1ull; } while (0)
#define HOLD_TAG() asm volatile("" ::"s"(tag))
#define tag32 ((unsigned)tag)
  const int chunk = 4 * (xg & 1) + (idx & 3), cb = 8 * (xg >> 1) + (idx >> 2);
  const int ft = 4 * xg + (idx & 3), dt1 = idx >> 2;
  const int tt = 4 * xg + (idx & 3), dt = idx >> 2;
  const int t0 = tt * 128, d0 = dt * 128;
  u64 cbs = roleA ? CB_ORDER : CB_ORDER >> (2 * (32 - NAB));
  const int nC = roleA ? 32 - NAB : NAB, ku0 = roleA ? 0 : NBA, nt2 = nC + (roleA ? NBA : 56 - NBA);
  const rsrc_t rsy = mk_rsrc(a.ws + WS_SYNC, SYNC_BYTES);

  auto touch = [&](const u16* base, unsigned bytes, int u0, int n) {
    const rsrc_t rs = mk_rsrc(base, bytes);
#pragma unroll 1
    for (int k = 0; k < n; ++k) dma4(rs, (unsigned)(lane * 128), (unsigned)(u0 + k) * 8192u, lds + LDS_DUMMY);
  };
  auto b1_stage = [&](int s_) { return (s_ & 7) == dt1 && s_ < LLIM ? (s_ + LEAD < LLIM ? s_ + LEAD : dt1) : s_; };
  auto w1_voff = [&](int piece) {
    const int ch = piece * 64 + lane, k = ch / 28, pos = ch - k * 28;
    return (unsigned)(k * F + ft * 224 + (pos ^ (3 * ((k >> 3) & 1))) * 8) * 2u;
  };
  auto a1_vx = [&](int l) { return (unsigned)((chunk * 256 + 32 * (l >> 5) + 16 * ((l >> 3) & 1) + ((l >> 4) & 1)) * D * 2 + ((l & 7) ^ (l >> 4)) * 16); };
  auto a1_vb = [&](int l) { return (unsigned)((cb * 64 + (l >> 3)) * D * 2 + ((l & 7) ^ (l >> 4)) * 16); };
  if (roleA) {
    const rsrc_t rs = mk_rsrc(a.x, S * D * 2), rre = mk_rsrc(a.Bre, P * D * 2), rim = mk_rsrc(a.Bim, P * D * 2);
    const unsigned vxe = a1_vx(lane), vxo = (vxe ^ 64u) + 64 * D * 2, vbe = a1_vb(lane);
    const unsigned sx = (unsigned)((wid >> 2) * 128 + (wid & 3) * 4) * (D * 2), sbb = (unsigned)wid * (8 * D * 2), vb = (wid & 1) ? vbe ^ 64u : vbe;
#pragma unroll
    for (int t = 0; t < A1_NS - 1; ++t) {
#pragma unroll
      for (int j = 0; j < 4; ++j) dma16(rs, (j & 1) ? vxo : vxe, sx + (j >> 1) * (2 * D * 2) + t * 128, lds + t * A1_STAGE + wid * 4096 + j * 1024);
      dma16(rre, vb, sbb + t * 128, lds + t * A1_STAGE + 32768 + (wid >> 1) * 4096 + (wid & 1) * 1024);
      dma16(rim, vb, sbb + t * 128, lds + t * A1_STAGE + 32768 + (wid >> 1) * 4096 + (wid & 1) * 1024 + 2048);
    }
    if (wid == 7) {
      dma4(mk_rsrc((const void*)(pc0 & ~255ull), CODE_PF_BYTES), (unsigned)(lane * 128), (unsigned)((idx % 5) * 8192), lds + LDS_DUMMY);
      __builtin_amdgcn_s_sleep(24);
      touch(a.w1, S * F * 2, PF_N0 * ri, PF_N0);
    }
    LOAD_TAG();
  }
  int isB = xc & 1;
  asm volatile("" : "+s"(isB));
  if (isB) {
    const int row = ri * 16 + wid * 2;
    u32x4 r[2][2], brow[2][2];
    {
      const u16* xp = a.x + (size_t)row * D + lane * 8;
      const u16* bp = (ri < 64 ? a.Bre : a.Bim) + (size_t)(row & 1023) * D + lane * 8;
#pragma unroll
      for (int i = 0; i < 4; ++i) r[i >> 1][i & 1] = *(const u32x4*)(xp + i * 512);
#pragma unroll
      for (int i = 0; i < 4; ++i) brow[i >> 1][i & 1] = *(const u32x4*)(bp + i * 512);
    }
    LOAD_TAG();
    const rsrc_t rx = mk_rsrc(a.ws + WS_HL, S * D * 2);
#pragma unroll
    for (int pass = 0; pass < 2; ++pass) {
      float s1[2], s2[2];
#pragma unroll
      for (int i = 0; i < 2; ++i) {
        float a1 = 0.f, a2 = 0.f;
#pragma unroll
        for (int k = 0; k < 8; ++k) { const unsigned w = r[i][k >> 2][k & 3]; a1 = dot2(w, 0x3f803f80u, a1); a2 = dot2(w, w, a2); }
        s1[i] = a1; s2[i] = a2;
      }
#pragma unroll
      for (int i = 0; i < 2; ++i) { s1[i] = wave_sum(s1[i]); s2[i] = wave_sum(s2[i]); }
#pragma unroll
      for (int i = 0; i < 2; ++i) {
        const float mean = s1[i] * (1.0f / D), var = fmaxf(fmaf(-mean, mean, s2[i] * (1.0f / D)), 0.f);
        const unsigned ms = pk_bf16(mean, __builtin_amdgcn_sqrtf(rbf(rbf(var) + 1e-5f)));
        const float rs_ = __builtin_amdgcn_rcpf(bfhi(ms)), mb = bflo(ms);
#pragma unroll
        for (int k = 0; k < 8; ++k) {
          const unsigned w = r[i][k >> 2][k & 3];
          r[i][k >> 2][k & 3] = pk_bf16((bflo(w) - mb) * rs_, (bfhi(w) - mb) * rs_);
        }
        if (pass == 0) {
          if (lane == 0) __hip_atomic_store(sy + SY_ST + row + i, ((u64)tag32 << 32) | ms, RLX_AGENT);
        } else {
          st16_wt(rx, (unsigned)lane * 16u, (unsigned)((row + i) * D) * 2u, r[i][0]);
          st16_wt(rx, (unsigned)lane * 16u + 1024u, (unsigned)((row + i) * D) * 2u, r[i][1]);
        }
      }
    }
    asm volatile("" ::"v"(brow[0][0]), "v"(brow[0][1]), "v"(brow[1][0]), "v"(brow[1][1]));
    wait_vm<0>();
    bar();
    if (wid == 0 && lane == 0) __hip_atomic_store(sy + SY_LN + ri, tag, RLX_AGENT);
    if (wid != 6) {
      const rsrc_t rs = mk_rsrc(a.w1, S * F * 2);
      const int p0_ = 2 * (wid == 7 ? 6 : wid);
      const unsigned v0 = w1_voff(p0_), v1 = w1_voff(p0_ + 1);
#pragma unroll
      for (int t = 0; t < B1_NS - 1; ++t) {
        dma16(rs, v0, b1_stage(t) * (32 * F * 2), lds + t * B1_STAGE + p0_ * 1024);
        dma16(rs, v1, b1_stage(t) * (32 * F * 2), lds + t * B1_STAGE + p0_ * 1024 + 1024);
      }
    }
    {
      float sums[2];
#pragma unroll
      for (int i = 0; i < 2; ++i) {
        float s = 0.f;
#pragma unroll
        for (int k = 0; k < 8; ++k) { const unsigned w = brow[i][k >> 2][k & 3]; s = dot2(w, 0x3f803f80u, s); }
        sums[i] = wave_sum(s);
      }
      if (lane < 2) __hip_atomic_store(sy + SY_SB + row + lane, ((u64)tag32 << 32) | __float_as_uint(lane ? sums[1] : sums[0]), RLX_AGENT);
    }
  }

  if (wid >= 4) {
    const int pw = wid - 4;
    int lane = __builtin_amdgcn_mbcnt_hi(~0u, __builtin_amdgcn_mbcnt_lo(~0u, 0u));
    asm volatile("" : "+v"(lane));
    if (pw == 3) {
      auto flags_fetch = [&]() {
        dma16c(rsy, (unsigned)lane * 16u, SY_LN * 8, lds + LDS_FLG);
        dma16c(rsy, (unsigned)lane * 16u, SY_LN * 8 + 1024, lds + LDS_FLG + 1024);
      };
      auto flags_check = [&]() {
        bool ok = true;
#pragma unroll
        for (int k = 0; k < 4; ++k) ok &= *(LAS u64*)(lds + LDS_FLG + k * 512 + lane * 8) == tag;
        if (!__all(ok)) wait_flags<4>(sy + SY_LN + lane, true, tag);
      };
      const rsrc_t rxb = mk_rsrc(a.ws + WS_XB, 256 * 16384);
      u32x4 zero4 = {0u, 0u, 0u, 0u};
      unsigned l16 = (unsigned)lane * 16u;
      asm volatile("" : "+v"(zero4), "+v"(l16));
      auto xb_clear = [&](int k) { __builtin_amdgcn_raw_buffer_store_b128(zero4, rxb, l16, (unsigned)(bid * 16384 + k * 1024), 16); };
      if (lane < 4) *(LAS unsigned*)(size_t)(LDS_CNT + lane * 4) = 0u;
      if (roleA) {
        st16_wt(rsy, (unsigned)lane * 16u, (unsigned)(SY_GR * 8 + (chunk * P + cb * 64) * 16), (u32x4){0u, 0u, 0u, 0u});
        dma4(mk_rsrc(a.Lre, P * 2), (unsigned)(lane & 31) * 4u, cb * 128, lds + LDS_RAW + 3072);
        dma4(mk_rsrc(a.Lim, P * 2), (unsigned)(lane & 31) * 4u, cb * 128, lds + LDS_RAW + 3072 + 256);
        dma4(mk_rsrc(a.logdt, P * 2), (unsigned)(lane & 31) * 4u, cb * 128, lds + LDS_RAW + 3072 + 512);
        HOLD_TAG();
        wait_vm<5 + PF_N0>();
        bar(); bar(); bar();
        wait_vm<0>();
        if (lane == 0) __hip_atomic_store(sy + SY_LN + 128 + ri, tag, RLX_AGENT);
        LAS float* cf = (LAS float*)(lds + LDS_COEF) + lane;
        const float lr = bf2f(*(LAS u16*)(lds + LDS_RAW + 3072 + lane * 2)), li = bf2f(*(LAS u16*)(lds + LDS_RAW + 3072 + 256 + lane * 2));
        const float dtt = rbf(expf(bf2f(*(LAS u16*)(lds + LDS_RAW + 3072 + 512 + lane * 2)))), er = expf(dtt * lr);
        float arg = dtt * li;
        asm volatile("" : "+v"(arg));
        bar();
        float sn, cs;
        sincosf(arg, &sn, &cs);
        float pr = er * cs, pi = er * sn;
        asm volatile("" : "+v"(pr), "+v"(pi));
        bar();
        const float nr = pr - 1.0f, den = lr * lr + li * li;
        cf[0] = pr; cf[64] = pi;
        cf[128] = 2.0f * (nr * lr + pi * li) / den; cf[192] = 2.0f * (pi * lr - nr * li) / den;
        bar();
#pragma unroll
        for (int k = 0; k < 8; ++k) {
          const float t = pr * pr - pi * pi; pi = 2.0f * pr * pi; pr = t;
          if (k >= 3) { cf[(4 + 2 * (k - 3)) * 64] = pr; cf[(5 + 2 * (k - 3)) * 64] = pi; }
        }
#pragma unroll 1
        for (int t = 6; t < 10; ++t) { bar(); xb_clear(4 * t - 24); xb_clear(4 * t - 23); xb_clear(4 * t - 22); xb_clear(4 * t - 21); }
        bar();
        dma4(mk_rsrc((const void*)(pc0 & ~255ull), CODE_PF_BYTES), (unsigned)(lane * 128), (unsigned)((idx % 5) * 8192), lds + LDS_DUMMY);
        bar();
        asm volatile("" ::"v"(zero4));
        const unsigned sbo = (unsigned)(SY_SB * 8 + cb * 512 + (lane >> 5) * (P * 8) + (lane & 31) * 16);
#pragma unroll 1
        for (unsigned spins = 0;; ++spins) {
          dma16c(rsy, (unsigned)lane * 16u, (unsigned)(SY_ST * 8 + chunk * 2048), lds + LDS_RAW);
          dma16c(rsy, (unsigned)lane * 16u, (unsigned)(SY_ST * 8 + chunk * 2048 + 1024), lds + LDS_RAW + 1024);
          dma16c(rsy, sbo, 0, lds + LDS_RAW + 2048);
          if (spins == 0) { flags_fetch(); bar(); bar(); }
          wait_vm<0>();
          u32x2 gs[4], gb[2];
          bool ok = true;
#pragma unroll
          for (int k = 0; k < 4; ++k) { gs[k] = *(LAS u32x2*)(lds + LDS_RAW + k * 512 + lane * 8); ok &= gs[k][1] == tag32; }
#pragma unroll
          for (int k = 0; k < 2; ++k) { gb[k] = *(LAS u32x2*)(lds + LDS_RAW + 2048 + k * 512 + lane * 8); ok &= gb[k][1] == tag32; }
          if (__all(ok) || spins > SPIN_CAP) {
#pragma unroll
            for (int k = 0; k < 4; ++k) {
              LAS float* e = (LAS float*)(lds + LDS_STAT + (2 * k + (lane >> 5)) * 272 + (lane & 15) * 16 + ((lane >> 4) & 1) * 4);
              e[0] = -bflo(gs[k][0]); e[2] = __builtin_amdgcn_rcpf(bfhi(gs[k][0]));
            }
#pragma unroll
            for (int k = 0; k < 2; ++k) *(LAS unsigned*)(lds + LDS_SB + k * 256 + lane * 4) = gb[k][0];
            break;
          }
          __builtin_amdgcn_s_sleep(2);
        }
        touch(a.w1, S * F * 2, 128 * (14 - PF_N2) + PF_N2 * ri, PF_N2);
        touch(a.Cre, D * P * 2, 2 * ri, 2); touch(a.Cim, D * P * 2, 2 * ri, 2);
        bar();
        flags_check();
        wait_lgkm0();
        bar();
      } else {
        wait_vm<0>();
        dma4(mk_rsrc((const void*)(pc0 & ~255ull), CODE_PF_BYTES), (unsigned)(lane * 128), (unsigned)((idx % 5) * 8192), lds + LDS_DUMMY);
#pragma unroll 1
        for (int t = 0; t < 24; ++t) bar();
        if (bid == 255) flags_fetch();
#pragma unroll 1
        for (int t = 24; t < 40; ++t) { bar(); xb_clear(t - 24); }
#pragma unroll 1
        for (int t = 40; t < 52; ++t) bar();
        dma4(mk_rsrc((const void*)(pc0 & ~255ull), CODE_PF_BYTES), (unsigned)(lane * 128), (unsigned)((idx % 5) * 8192), lds + LDS_DUMMY);
#pragma unroll 1
        for (int t = 52; t < B1_NT - 6; ++t) bar();
        asm volatile("" ::"v"(zero4));
        wait_vm<0>();
        if (bid == 255) {
          flags_check();
          if (lane == 0) __hip_atomic_store(sy, tag, RLX_AGENT);
        }
        bar();
        touch(a.w2, F * S * 2, NBA * 32 + PF_WB * ri, PF_WB); touch(a.w2, F * S * 2, PF_WB * ri, PF_WB);
#pragma unroll 1
        for (int t = 0; t < 5; ++t) bar();
      }
    } else if (roleA) {
      if (pw < 2) {
        const rsrc_t rs = mk_rsrc(a.x, S * D * 2);
        const unsigned vxe = a1_vx(lane), vxo = (vxe ^ 64u) + 64 * D * 2;
        unsigned so = (A1_NS - 1) * 128 + pw * (128 * D * 2), lo = (A1_NS - 1) * A1_STAGE + pw * 16384;
        auto issue = [&]() {
#pragma unroll
          for (int j = 0; j < 16; ++j) dma16(rs, (j & 1) ? vxo : vxe, so + (j >> 1) * (2 * D * 2), lds + lo + j * 1024);
          so += 128; lo = lo >= (A1_NS - 1) * A1_STAGE ? lo - (A1_NS - 1) * A1_STAGE : lo + A1_STAGE;
        };
        HOLD_TAG();
        wait_vm<0>();
        prod_run<16, A1_NS>(A1_NT - (A1_NS - 1), issue);
        prod_tail<16, A1_NS - 2>();
      } else {
        const unsigned ve = a1_vb(lane), vo = ve ^ 64u;
        const rsrc_t rre = mk_rsrc(a.Bre, P * D * 2), rim = mk_rsrc(a.Bim, P * D * 2);
        unsigned so = (A1_NS - 1) * 128, lo = (A1_NS - 1) * A1_STAGE + 32768;
        auto issue = [&]() {
#pragma unroll
          for (int j = 0; j < 8; ++j) {
            dma16(rre, (j & 1) ? vo : ve, so + j * (8 * D * 2), lds + lo + (j >> 1) * 4096 + (j & 1) * 1024);
            dma16(rim, (j & 1) ? vo : ve, so + j * (8 * D * 2), lds + lo + (j >> 1) * 4096 + (j & 1) * 1024 + 2048);
          }
          so += 128; lo = lo >= (A1_NS - 1) * A1_STAGE ? lo - (A1_NS - 1) * A1_STAGE : lo + A1_STAGE;
        };
        HOLD_TAG();
        wait_vm<0>();
        prod_run<16, A1_NS>(A1_NT - (A1_NS - 1), issue);
        prod_tail<16, A1_NS - 2>();
      }
    } else {
      const int dd0 = dt1 * 128;
      if (pw < 2) {
        unsigned voff[7];
#pragma unroll
        for (int j = 0; j < 7; ++j) voff[j] = w1_voff(pw * 7 + j);
        const rsrc_t rs = mk_rsrc(a.w1, S * F * 2);
        int st = B1_NS - 1;
        unsigned so = b1_stage(st) * (32 * F * 2), lo = (B1_NS - 1) * B1_STAGE + pw * 7168;
        auto issue = [&]() {
#pragma unroll
          for (int j = 0; j < 7; ++j) dma16(rs, voff[j], so, lds + lo + j * 1024);
          so = b1_stage(++st) * (32 * F * 2); lo = lo >= (B1_NS - 1) * B1_STAGE ? lo - (B1_NS - 1) * B1_STAGE : lo + B1_STAGE;
        };
        wait_vm<0>();
        prod_run<7, B1_NS>(B1_NT - (B1_NS - 1), issue);
        prod_tail<7, B1_NS - 2>();
      } else {
        unsigned voff[2];
#pragma unroll
        for (int j = 0; j < 2; ++j) {
          const int k = j * 8 + (lane >> 4), pos = lane & 15;
          const int c = pos ^ (((k & 3) << 1) | (((k >> 3) & 1) << 3));
          voff[j] = (unsigned)((lane >> 4) * D + dd0 + c * 8) * 2u;
        }
        const rsrc_t rs = mk_rsrc(a.ws + WS_HL, S * D * 2);
        int st = 0;
        unsigned so = b1_stage(0) * (32 * D * 2), lo = B1_ABYTES;
        auto issue = [&]() {
#pragma unroll
          for (int j = 0; j < 8; ++j) dma16c(rs, voff[(j >> 1) & 1], so + j * 4 * (D * 2), lds + lo + j * 1024);
          so = b1_stage(++st) * (32 * D * 2); lo = lo >= (B1_NS - 1) * B1_STAGE ? lo - (B1_NS - 1) * B1_STAGE : lo + B1_STAGE;
        };
        int done = B1_NS + 3;
        wait_flags<1>(sy + SY_LN + lane, lane < 2 * (done + LEAD), tag);
        prod_fill(B1_NS - 1, issue);
        prod_run<8, B1_NS>(1, issue);
        dma16c(rsy, (unsigned)lane * 16u, SY_LN * 8, lds + LDS_RES2);
        prod_run<8, B1_NS>(3, issue);
        wait_vm<24>();
        const u32x4 fl = *(LAS u32x4*)(lds + LDS_RES2 + lane * 16);
        int pre = __all(fl[0] == tag32 && fl[2] == tag32 && fl[1] == (unsigned)(tag >> 32) && fl[3] == (unsigned)(tag >> 32)) ? 128 : 2 * (done + LEAD);
#pragma unroll 1
        while (pre < 128 && done < B1_NT) {
          const int n = pre / 2 - LEAD - done;
          prod_run<8, B1_NS>(n, issue);
          done += n;
          wait_vm<(B1_NS - 2) * 8>();
          bar();
          pre = flag_prefix<2>(sy + SY_LN + lane, 64, 2 * (done + LEAD) + 2, tag, lane);
          issue();
          ++done;
        }
        prod_run<8, B1_NS>(B1_NT - done, issue);
        prod_tail<8, B1_NS - 2>();
      }
    }
    bar();
    const int nU = nt2 - nC;
    int us = 0;
    auto u_tile = [&](int s_) { return ku0 + ((s_ & 7) == dt ? (s_ + 8 < nU ? s_ + 8 : dt) : s_); };
    if (pw == 1 || pw == 2) {
      const rsrc_t rxx = mk_rsrc(a.x, S * D * 2);
      const int dk = d0 + (roleA ? 0 : 64);
#pragma unroll
      for (int k = 0; k < 8; ++k) dma16(rxx, (unsigned)((lane >> 3) * (D * 2) + ((lane & 7) ^ (lane >> 3)) * 16), (unsigned)((t0 + 64 * (pw - 1) + 8 * k) * D + dk) * 2u, lds + LDS_RES + (8 * (pw - 1) + k) * 1024);
      if (pw == 1) dma16c(rsy, (unsigned)lane * 16u, (unsigned)(SY_ST + t0) * 8u, lds + LDS_RES2);
      else { dma4(mk_rsrc(a.b2, S * 2), (unsigned)lane * 4u, (unsigned)t0 * 2u, lds + LDS_RES2 + 1024); dma4(mk_rsrc(a.Dsk, D * 2), (unsigned)lane * 4u, (unsigned)dk * 2u, lds + LDS_RES2 + 1280); }
    }
    if (pw == 0 || pw == 3) {
      const int dh = d0 + (((pw ? 1 : 0) ^ (roleA ? 0 : 1)) << 6), r4 = lane >> 4;
      const unsigned vce = (unsigned)(dh + (lane >> 3)) * (P * 2) + (unsigned)(((lane & 7) ^ (lane >> 4)) * 16), vco = vce ^ 64u;
      const unsigned vu = (unsigned)((dh + (lane >> 2)) * 64 + (((lane & 3) ^ (((r4 & 1) << 1) ^ ((r4 >> 1) * 3))) * 16));
      const rsrc_t ru = mk_rsrc(a.ws + WS_U, D * F * 2);
      rsrc_t rc = mk_rsrc(a.Cre, D * P * 2), rc2 = mk_rsrc(a.Cim, D * P * 2);
      unsigned so = 0, lo = pw ? 8192 : 0, sh = 0;
      auto issueC = [&]() {
        so = ((unsigned)cbs & 15u) * 128;
#pragma unroll
        for (int j = 0; j < 8; ++j) dma16(rc, (j & 1) ? vco : vce, so + j * 8 * (P * 2), lds + lo + j * 1024);
        const rsrc_t t_ = rc; rc = rc2; rc2 = t_;
        cbs >>= sh; sh ^= 4; lo = (lo + P2_STAGE) & (P2_NS * P2_STAGE - 1);
      };
      auto issueU = [&]() {
#pragma unroll
        for (int j = 0; j < 8; ++j) dma16c(ru, vu, so + (j & 1) * (D * 64) + (j >> 1) * 1024, lds + lo + j * 1024);
        so = u_tile(++us) * (2 * D * 64); lo = (lo + P2_STAGE) & (P2_NS * P2_STAGE - 1);
      };
      prod_fill(P2_NS - 1, issueC);
      if (roleA) {
        prod_run<8, P2_NS>(nC - 6, issueC);
        dma16c(rsy, (unsigned)(lane & 7) * 16u, (unsigned)(SY_U + dt * 16) * 8u, lds + LDS_FLG + (pw ? 1024 : 0));
        prod_run<8, P2_NS>(3, issueC);
      } else {
        prod_run<8, P2_NS>(nC - 3, issueC);
      }
      so = u_tile(0) * (2 * D * 64);
      constexpr int F0 = 2 * NBA / 7;
      const int f0 = roleA ? 0 : F0;
      gu64* uf = sy + SY_U + dt * 16 + ((f0 + lane) & 15);
      LAS char* ufl = lds + LDS_FLG + (pw ? 1024 : 0);
      int done = 0, pre = 0;
      bool look = roleA;
#pragma unroll 1
      while (pre < 128 && done < nU) {
        wait_vm<16>();
        bar();
        const int kn = ku0 + done + 8 < 55 ? ku0 + done + 8 : 55, need = roleA ? 16 : (2 * kn + 1) / 7 + 1 - f0;
        int p2 = 0;
        if (look) {
          const u64 m = ~__ballot(lane >= 16 - f0 || *(LAS u64*)(ufl + ((f0 + lane) & 15) * 8) == tag);
          p2 = m ? __builtin_ctzll(m) : 128;
        }
        pre = p2 >= need ? p2 : flag_prefix<1>(uf, 16 - f0, need, tag, lane);
        issueU();
        ++done;
        if (pre < 128) {
          const int n = ((7 * (f0 + pre)) >> 1) - ku0 - 8 - done;
          look = n >= 3;
          prod_run<8, P2_NS>(look ? n - 3 : n, issueU);
          if (look) {
            dma16c(rsy, (unsigned)(lane & 7) * 16u, (unsigned)(SY_U + dt * 16) * 8u, ufl);
            prod_run<8, P2_NS>(3, issueU);
          }
          done += n;
        }
      }
      prod_run<8, P2_NS>(nU - done, issueU);
    } else {
      unsigned voff[2];
#pragma unroll
      for (int j = 0; j < 2; ++j) {
        const int k = j * 8 + (lane >> 4), pos = lane & 15;
        const int c = pos ^ (((k & 3) << 1) | (((k >> 3) & 1) << 3));
        voff[j] = (unsigned)(((pw - 1) * 32 + (lane >> 4)) * S + t0 + c * 8) * 2u;
      }
      const unsigned vh = (unsigned)(lane * 16 + (pw - 1) * 8192);
      const rsrc_t rh = mk_rsrc(a.ws + WS_HT, 2 * P * S * 2), rw = mk_rsrc(a.w2, F * S * 2);
      unsigned so = 0, lo = 16384 + (pw - 1) * 8192, sh = 0;
      const int nf = nC / 2 + (roleA ? 0 : 1);
      const int hfi = (int)((unsigned)(cbs >> (4 * (lane & 15))) & 15u);
      gu64* hf = sy + SY_H + (lane == nC / 2 ? chunk * 16 + cb : (tt >> 1) * 16 + hfi);
      auto issueH = [&]() {
        so = (tt * 32 + sh * 4 + ((unsigned)cbs & 15u)) * 16384;
#pragma unroll
        for (int j = 0; j < 8; ++j) dma16c(rh, vh, so + j * 1024, lds + lo + j * 1024);
        cbs >>= sh; sh ^= 4; lo = (lo + P2_STAGE) & (P2_NS * P2_STAGE - 1);
      };
      auto issueW = [&]() {
#pragma unroll
        for (int j = 0; j < 8; ++j) dma16(rw, voff[(j >> 1) & 1], so + j * 4 * (S * 2), lds + lo + j * 1024);
        so = u_tile(++us) * (64 * S * 2); lo = (lo + P2_STAGE) & (P2_NS * P2_STAGE - 1);
      };
      if (roleA) lds_wait(LDS_CNT, 4u);
      int done = P2_NS - 1, pre = flag_prefix<1>(hf, nf, 2, tag, lane);
      prod_fill(P2_NS - 1, issueH);
#pragma unroll 1
      while (pre < 128 && done < nC) {
        const int n = (2 * pre < nC ? 2 * pre : nC) - done;
        const bool look = n >= 3;
        prod_run<8, P2_NS>(look ? n - 3 : n, issueH);
        if (look) {
          dma16c(rsy, (unsigned)(lane & 7) * 16u, (unsigned)(SY_H + (tt >> 1) * 16) * 8u, lds + LDS_HFL + (pw - 1) * 1024);
          prod_run<8, P2_NS>(3, issueH);
        }
        done += n;
        if (done < nC) {
          wait_vm<16>();
          bar();
          int p2 = 0;
          if (look) {
            const u64 m = ~__ballot(lane >= nf || (lane < nC / 2 && *(LAS u64*)(lds + LDS_HFL + (pw - 1) * 1024 + hfi * 8) == tag));
            p2 = m ? __builtin_ctzll(m) : 128;
          }
          pre = p2 > done / 2 ? p2 : flag_prefix<1>(hf, nf, done / 2 + 1, tag, lane);
          issueH();
          ++done;
        }
      }
      prod_run<8, P2_NS>(nC - done, issueH);
      so = u_tile(0) * (64 * S * 2);
      if (pre < 128) (void)flag_prefix<1>(hf, nf, nf, tag, lane);
      prod_run<8, P2_NS>(nU, issueW);
    }
    prod_tail<8, P2_NS - 2>();
    return;
  }

  const int wm = wid >> 1, wn = wid & 1;
  int cl = lane & 15, g = lane >> 4, q4 = cl >> 2, p4 = lane & 3, ln = lane;
#define RELANE() do { ln = __builtin_amdgcn_mbcnt_hi(~0u, __builtin_amdgcn_mbcnt_lo(~0u, 0u)); asm volatile("" : "+v"(ln)); cl = ln & 15; g = ln >> 4; q4 = cl >> 2; p4 = ln & 3; } while (0)
  const int kh = roleA ? 0 : 1, pa = bid ^ 1;
  const rsrc_t rxb = mk_rsrc(a.ws + WS_XB, 256 * 16384);

  if (roleA) {
    HOLD_TAG();
    RELANE();
    const int p0 = cb * 64;
    const unsigned rb = (unsigned)(cl * 128 + ((g ^ (cl >> 1)) * 16));
    unsigned pa0 = rb + wm * 16384, pa1 = pa0 ^ 64u, pb0 = rb + 32768 + wn * 8192, pb1 = pb0 ^ 64u;
    f32x4 acc[8][4];
#pragma unroll
    for (int m = 0; m < 8; ++m)
#pragma unroll
      for (int n = 0; n < 4; ++n) acc[m][n] = (f32x4){0.f, 0.f, 0.f, 0.f};
    bf16x8 xa[8], xb[4], ya[8], yb[4];
    auto rd = [&](bf16x8* fa, bf16x8* fb, unsigned pa_, unsigned pb_, int r) {
      if (r == 0) fa[0] = lds_rd128(pa_); else if (r < 5) fb[r - 1] = lds_rd128(pb_ + (r - 1) * 2048); else fa[r - 4] = lds_rd128(pa_ + (r - 4) * 2048);
    };
    int slot = 0, dl = 0;
    wait_vm0_known();
    bar();
#pragma unroll
    for (int r = 0; r < 12; ++r) rd(xa, xb, pa0, pb0, r);
#pragma unroll 1
    for (int t = 0; t < A1_NT; ++t) {
      SCHED_FENCE(); wait_lgkm0(); SCHED_FENCE();
      phase<32>(acc, xa, xb, [&](int i) {
        if (i % A1_RS == 0 && i / A1_RS < 12) rd(ya, yb, pa1, pb1, i / A1_RS);
        if (i == 25) { dl = (slot == A1_NS - 1) ? -(A1_NS - 1) * A1_STAGE : A1_STAGE; slot = (slot == A1_NS - 1) ? 0 : slot + 1; }
        if (i == 27) { pa0 += dl; pb0 += dl; asm volatile("" : "+v"(pa0), "+v"(pb0)); }
        if (i == 29) { pa1 += dl; pb1 += dl; asm volatile("" : "+v"(pa1), "+v"(pb1)); }
      });
      wait_lgkm0();
      bar();
      SCHED_FENCE();
      phase<32>(acc, ya, yb, [&](int i) { if (i % A1_RS == 0 && i / A1_RS < 12) rd(xa, xb, pa0, pb0, i / A1_RS); });
    }
    RELANE();
    float cf[2][14], sb[4];
#pragma unroll
    for (int q = 0; q < 2; ++q) {
#pragma unroll
      for (int k = 0; k < 14; ++k) cf[q][k] = ((LAS float*)(lds + LDS_COEF))[k * 64 + 32 * wn + 16 * q + cl];
      sb[2 * q] = ((LAS float*)(lds + LDS_SB))[32 * wn + 16 * q + cl]; sb[2 * q + 1] = ((LAS float*)(lds + LDS_SB))[64 + 32 * wn + 16 * q + cl];
    }
    f32x2 er[2], ei[2];
#pragma unroll
    for (int q = 0; q < 2; ++q) { er[q] = bc2(0.f); ei[q] = bc2(0.f); }
#pragma unroll
    for (int m = 0; m < 8; ++m)
#pragma unroll
      for (int jj = 0; jj < 2; ++jj) {
        const f32x4 st = *(LAS f32x4*)(lds + LDS_STAT + (4 * wm + g) * 272 + (2 * m + jj) * 16);
        const f32x2 c2 = {st[0], st[1]}, r2 = {st[2], st[3]};
#pragma unroll
        for (int q = 0; q < 2; ++q) {
          const f32x2 ur = fma2(c2, bc2(sb[2 * q]), (f32x2){acc[m][2 * q][2 * jj], acc[m][2 * q][2 * jj + 1]}) * r2;
          const f32x2 ui = fma2(c2, bc2(sb[2 * q + 1]), (f32x2){acc[m][2 * q + 1][2 * jj], acc[m][2 * q + 1][2 * jj + 1]}) * r2;
          acc[m][2 * q][2 * jj] = ur.x; acc[m][2 * q][2 * jj + 1] = ur.y; acc[m][2 * q + 1][2 * jj] = ui.x; acc[m][2 * q + 1][2 * jj + 1] = ui.y;
          const f32x2 nr = fma2(bc2(cf[q][0]), er[q], fma2(bc2(-cf[q][1]), ei[q], ur));
          const f32x2 ni = fma2(bc2(cf[q][0]), ei[q], fma2(bc2(cf[q][1]), er[q], ui));
          er[q] = nr; ei[q] = ni;
        }
        if (jj == 1 && (m & 1)) asm volatile("" : "+v"(er[0]), "+v"(ei[0]), "+v"(er[1]), "+v"(ei[1]));
      }
    float sr[2], si[2], pr_[2], pi_[2];
    LAS float* tot = (LAS float*)(lds + LDS_TOT);
#pragma unroll
    for (int q = 0; q < 2; ++q) {
      float tr, ti;
      CMUL(tr, ti, cf[q][4], cf[q][5], er[q].x, ei[q].x);
      sr[q] = tr + er[q].y; si[q] = ti + ei[q].y;
      float vr = __shfl_up(sr[q], 16), vi = __shfl_up(si[q], 16);
      CMUL(tr, ti, cf[q][6], cf[q][7], vr, vi);
      if (g >= 1) { sr[q] += tr; si[q] += ti; }
      vr = __shfl_up(sr[q], 32); vi = __shfl_up(si[q], 32);
      CMUL(tr, ti, cf[q][8], cf[q][9], vr, vi);
      if (g >= 2) { sr[q] += tr; si[q] += ti; }
      vr = __shfl_up(sr[q], 16); vi = __shfl_up(si[q], 16);
      pr_[q] = g >= 1 ? vr : 0.f; pi_[q] = g >= 1 ? vi : 0.f;
      if (wm == 0 && g == 3) *(LAS f32x2*)(tot + (32 * wn + 16 * q + cl) * 2) = (f32x2){sr[q], si[q]};
    }
    if (wm == 0) {
      wait_lgkm0();
      if (ln == 0) __hip_atomic_store((LAS unsigned*)(size_t)(LDS_CNT + 4 + 4 * wn), 1u, __ATOMIC_RELAXED, __HIP_MEMORY_SCOPE_WORKGROUP);
    } else {
      lds_wait(LDS_CNT + 4 + 4 * wn, 1u);
    }
    f32x2 tq[2];
#pragma unroll
    for (int q = 0; q < 2; ++q) {
      tq[q] = *(LAS f32x2*)(tot + (32 * wn + 16 * q + cl) * 2);
      if (wm == 1 && g == 3 && chunk < 7) {
        float tr, ti;
        CMUL(tr, ti, cf[q][10], cf[q][11], tq[q].x, tq[q].y);
        st16_wt(rsy, (unsigned)(SY_GR * 8 + (chunk * P + p0 + 32 * wn + 16 * q + cl) * 16), 0, (u32x4){__float_as_uint(tr + sr[q]), 1u, __float_as_uint(ti + si[q]), 1u});
      }
    }
#pragma unroll
    for (int m = 0; m < 8; ++m)
#pragma unroll
      for (int q = 0; q < 2; ++q) {
        const f32x4 ur = acc[m][2 * q], ui = acc[m][2 * q + 1];
        acc[m][2 * q] = ur * cf[q][2] - ui * cf[q][3];
        acc[m][2 * q + 1] = ui * (-cf[q][2]) - ur * cf[q][3];
        asm volatile("" : "+v"(acc[m][2 * q]), "+v"(acc[m][2 * q + 1]));
      }
    float hr[2] = {0.f, 0.f}, hi[2] = {0.f, 0.f};
    if (chunk > 0) {
      float gr[2][2], gi[2][2];
#pragma unroll 1
      for (unsigned spins = 0;; ++spins) {
        bool ok = true;
#pragma unroll
        for (int q = 0; q < 2; ++q)
#pragma unroll
          for (int k = 0; k < 2; ++k) {
            const int cc = (g + 4 * k < chunk) ? g + 4 * k : 0;
            const u32x4 v = __builtin_amdgcn_raw_buffer_load_b128(rsy, (unsigned)(SY_GR * 8 + (cc * P + p0 + 32 * wn + 16 * q + cl) * 16), 0, 16);
            ok &= (v[1] == 1u) & (v[3] == 1u);
            gr[q][k] = __uint_as_float(v[0]); gi[q][k] = __uint_as_float(v[2]);
          }
        if (__all(ok) || spins > SPIN_CAP) break;
        __builtin_amdgcn_s_sleep(1);
        asm volatile("" ::: "memory");
      }
#pragma unroll
      for (int q = 0; q < 2; ++q) {
        float ar_ = 0.f, ai_ = 0.f;
#pragma unroll
        for (int k = 0; k < 2; ++k) {
          float vr = gr[q][k], vi = gi[q][k];
          const int e = chunk - 1 - (g + 4 * k);
#pragma unroll
          for (int i = 0; i < 6 - 4 * k; ++i) {
            float tr, ti;
            CMUL(tr, ti, cf[q][12], cf[q][13], vr, vi);
            vr = i < e ? tr : vr; vi = i < e ? ti : vi;
          }
          ar_ += e >= 0 ? vr : 0.f; ai_ += e >= 0 ? vi : 0.f;
        }
        ar_ += __shfl_xor(ar_, 16); ai_ += __shfl_xor(ai_, 16);
        ar_ += __shfl_xor(ar_, 32); ai_ += __shfl_xor(ai_, 32);
        hr[q] = ar_; hi[q] = ai_;
      }
    }
    f32x2 wr[2], wi[2];
#pragma unroll
    for (int q = 0; q < 2; ++q) {
      float tr, ti;
      if (wm == 1) { CMUL(tr, ti, cf[q][10], cf[q][11], hr[q], hi[q]); hr[q] = tr + tq[q].x; hi[q] = ti + tq[q].y; }
      CMUL(tr, ti, cf[q][6], cf[q][7], hr[q], hi[q]);
      if (g & 1) { hr[q] = tr; hi[q] = ti; }
      CMUL(tr, ti, cf[q][8], cf[q][9], hr[q], hi[q]);
      if (g & 2) { hr[q] = tr; hi[q] = ti; }
      hr[q] += pr_[q]; hi[q] += pi_[q];
      CMUL(tr, ti, cf[q][4], cf[q][5], hr[q], hi[q]);
      const f32x2 h0r = {hr[q], tr + er[q].x}, h0i = {hi[q], ti + ei[q].x};
      wr[q] = h0r * cf[q][2] - h0i * cf[q][3];
      wi[q] = h0i * (-cf[q][2]) - h0r * cf[q][3];
    }
    LAS char* scr = lds + wid * P2_STAGE + 16384;
    const unsigned fsw = (unsigned)(((cl & 3) << 1) | (((cl >> 3) & 1) << 3));
    const rsrc_t rh = mk_rsrc(a.ws + WS_HT, 2 * P * S * 2);
#pragma unroll
    for (int q = 0; q < 2; ++q) {
      const f32x2 ar2 = bc2(cf[q][0]), ai2 = bc2(cf[q][1]);
#pragma unroll
      for (int mh = 0; mh < 2; ++mh) {
        u32x4 o[2][2];
#pragma unroll
        for (int mq = 0; mq < 4; ++mq) {
          const int m = 4 * mh + mq;
          const f32x2 u0r = {acc[m][2 * q][0], acc[m][2 * q][1]}, u0i = {acc[m][2 * q + 1][0], acc[m][2 * q + 1][1]};
          const f32x2 u1r = {acc[m][2 * q][2], acc[m][2 * q][3]}, u1i = {acc[m][2 * q + 1][2], acc[m][2 * q + 1][3]};
          const f32x2 n0r = fma2(ar2, wr[q], fma2(ai2, wi[q], u0r)), n0i = fma2(ar2, wi[q], fma2(-ai2, wr[q], u0i));
          const f32x2 n1r = fma2(ar2, n0r, fma2(ai2, n0i, u1r)), n1i = fma2(ar2, n0i, fma2(-ai2, n0r, u1i));
          wr[q] = n1r; wi[q] = n1i;
          o[0][0][mq] = pk_bf16(n0r.x, n1r.x); o[0][1][mq] = pk_bf16(n0r.y, n1r.y);
          o[1][0][mq] = pk_bf16(n0i.x, n1i.x); o[1][1][mq] = pk_bf16(n0i.y, n1i.y);
        }
#pragma unroll
        for (int part = 0; part < 2; ++part)
#pragma unroll
          for (int run = 0; run < 2; ++run)
            *(LAS u32x4*)(scr + (part * 32 + 16 * q + cl) * 256 + (((unsigned)(4 * g + 2 * run + mh) ^ fsw) * 16)) = o[part][run];
      }
#pragma unroll
      for (int i = 0; i < 8; ++i) {
        const int part = i >> 2, pc = 4 * q + (i & 3);
        const u32x4 v = *(LAS u32x4*)(scr + part * 8192 + pc * 1024 + ln * 16);
        st16_wt(rh, (unsigned)(ln * 16 + pc * 1024), (unsigned)(((2 * chunk + wm) * 32 + part * 16 + cb) * 16384 + wn * 8192), v);
      }
      SCHED_FENCE();
    }
  } else {
    RELANE();
    const int f0 = ft * 224, dd0 = dt1 * 128;
    const int k0 = 8 * g + q4;
    unsigned pa0 = (unsigned)(k0 * 448 + ((12 * wm + p4) ^ (3 * (g & 1))) * 16 + (p4 & 1) * 8);
    unsigned pa1 = (unsigned)(k0 * 448 + ((12 * wm + p4) ^ (3 * (g & 1))) * 16 + ((p4 & 1) ^ 1) * 8);
    unsigned pa2 = (unsigned)(k0 * 448 + ((24 + 2 * wm + (p4 >> 1)) ^ (3 * (g & 1))) * 16 + (p4 & 1) * 8);
    unsigned pb[4];
#pragma unroll
    for (int n = 0; n < 4; ++n) pb[n] = (unsigned)(B1_ABYTES + k0 * 256 + ((8 * wn + 2 * n + (p4 >> 1)) ^ ((q4 << 1) | ((g & 1) << 3))) * 16 + (p4 & 1) * 8);
    f32x4 acc[7][4];
#pragma unroll
    for (int m = 0; m < 7; ++m) {
      const u32x2 bv = *(const u32x2*)(a.b1 + f0 + (m < 6 ? 96 * wm + 32 * (m >> 1) + 8 * g + 4 * ((m & 1) ^ (g & 1)) : 192 + 16 * wm + 4 * g));
#pragma unroll
      for (int n = 0; n < 4; ++n) acc[m][n] = (f32x4){bflo(bv[0]), bfhi(bv[0]), bflo(bv[1]), bfhi(bv[1])};
    }
    TrFrag xa[7], xb[4], ya[7], yb[4];
    int slot = 0, dl = 0;
    auto side = [&](TrFrag* fa, TrFrag* fb, int i) {
      const int f = i >> 1, h = i & 1;
      if (f == 0) fa[0].h[h] = lds_rdtr(pa0 + h * 1792);
      else if (f < 5) fb[f - 1].h[h] = lds_rdtr(pb[f - 1] + h * 1024);
      else if (f < 10) fa[f - 4].h[h] = lds_rdtr((((f - 4) & 1) ? pa1 : pa0) + ((f - 4) >> 1) * 64 + h * 1792);
      else if (f == 10) fa[6].h[h] = lds_rdtr(pa2 + h * 1792);
      else if (i == 22) { dl = (slot == B1_NS - 1) ? -(B1_NS - 1) * B1_STAGE : B1_STAGE; slot = (slot == B1_NS - 1) ? 0 : slot + 1; }
      else if (i == 23) { pa0 += dl; pb[0] += dl; asm volatile("" : "+v"(pa0), "+v"(pb[0])); }
      else if (i == 24) { pb[1] += dl; pb[2] += dl; asm volatile("" : "+v"(pb[1]), "+v"(pb[2])); }
      else if (i == 25) { pb[3] += dl; pa1 += dl; asm volatile("" : "+v"(pb[3]), "+v"(pa1)); }
      else if (i == 26) { pa2 += dl; asm volatile("" : "+v"(pa2)); }
    };
    wait_vm0_known();
    bar();
#pragma unroll
    for (int i = 0; i < 28; ++i) side(xa, xb, i);
#pragma unroll 1
    for (int t = 0; t < B1_NT; t += 2) {
      SCHED_FENCE(); wait_lgkm0(); bar(); SCHED_FENCE();
      phase<28>(acc, xa, xb, [&](int i) { side(ya, yb, i); });
      wait_lgkm0(); bar(); SCHED_FENCE();
      phase<28>(acc, ya, yb, [&](int i) { side(xa, xb, i); });
    }
    RELANE();
    const rsrc_t ru = mk_rsrc(a.ws + WS_U, D * F * 2);
    auto act4 = [&](const f32x4& v) {
      const f32x2 v0 = gelu_tanh2((f32x2){v[0], v[1]}), v1 = gelu_tanh2((f32x2){v[2], v[3]});
      return (u32x2){pk_bf16(v0.x, v0.y), pk_bf16(v1.x, v1.y)};
    };
#pragma unroll
    for (int n = 0; n < 4; ++n) *(LAS u32x2*)(lds + LDS_U6 + (64 * wn + 16 * n + cl) * 64 + 32 * wm + 8 * g) = act4(acc[6][n]);
    wait_lgkm0();
    if (ln == 0) __hip_atomic_fetch_add((LAS unsigned*)(size_t)(LDS_CNT + 4 + 4 * wn), 1u, __ATOMIC_RELAXED, __HIP_MEMORY_SCOPE_WORKGROUP);
#pragma unroll
    for (int n = 0; n < 4; ++n) {
      const unsigned vo = (unsigned)((dd0 + 64 * wn + 16 * n + cl) * 64);
#pragma unroll
      for (int mm = 0; mm < 3; ++mm) {
        const u32x2 w0 = act4(acc[2 * mm][n]), w1 = act4(acc[2 * mm + 1][n]);
        const bool sw = g & 1;
        const u32x2 lo = sw ? w1 : w0, hi = sw ? w0 : w1;
        st16_wt(ru, vo + 16 * g, (unsigned)((ft * 7 + 3 * wm + mm) * D * 64), (u32x4){lo[0], lo[1], hi[0], hi[1]});
      }
      if (n == 1) {
        lds_wait(LDS_CNT + 4 + 4 * wn, 2u);
#pragma unroll
        for (int k = 0; k < 2; ++k) {
          const unsigned o = (unsigned)(wn * 4096 + wm * 2048 + k * 1024 + ln * 16);
          st16_wt(ru, o, (unsigned)(((ft * 7 + 6) * D + dd0) * 64), *(LAS u32x4*)(lds + LDS_U6 + o));
        }
      }
    }
  }

  publish(LDS_CNT, sy + (roleA ? SY_H + chunk * 16 + cb : SY_U + dt1 * 16 + ft), tag, ln);

  f32x4 acc[4][4];
#pragma unroll
  for (int i = 0; i < 16; ++i) acc[i >> 2][i & 3] = (f32x4){0.f, 0.f, 0.f, 0.f};
  const unsigned au0 = (unsigned)(wm * 4096 + cl * 64 + ((g ^ (((q4 & 1) << 1) ^ ((q4 >> 1) * 3))) * 16));
  unsigned pa0 = (unsigned)((wm * 32 + cl) * 128 + ((g ^ (cl >> 1)) * 16)), pa1 = pa0 ^ 64u;
  const unsigned sw0 = pa0 ^ au0 ^ 65536u, sw1 = pa1 ^ (au0 + 1024u) ^ 65536u;
  unsigned pb[4];
#pragma unroll
  for (int n = 0; n < 4; ++n) pb[n] = (unsigned)(16384 + (8 * g + q4) * 256 + ((8 * wn + 2 * n + (p4 >> 1)) ^ ((q4 << 1) | ((g & 1) << 3))) * 16 + (p4 & 1) * 8);
  bf16x8 xa[4], ya[4];
  TrFrag xb[4], yb[4];
  static_assert(P2_NS == 4 && NAB % 2 == 0 && NBA % 2 == 0 && NAB >= P2_NS && NAB <= 16, "phase-2 loop: two stages per trip, 64-KB windows");
  auto rd = [&](bf16x8* fa, TrFrag* fb, int so, int ks, int r) {
    const unsigned pa_ = (ks ? pa1 : pa0) + so;
    if (r == 0) fa[0] = lds_rd128(pa_);
    else if (r < 9) fb[(r - 1) >> 1].h[(r - 1) & 1] = lds_rdtr(pb[(r - 1) >> 1] + so + ks * 8192 + ((r - 1) & 1) * 1024);
    else if (r < 12) fa[r - 8] = lds_rd128(pa_ + ((r - 8) >> 1) * 8192 + ((r - 8) & 1) * 2048);
  };
  bar();
#pragma unroll
  for (int r = 0; r < 12; ++r) rd(xa, xb, 0, 0, r);
#pragma unroll 1
  for (int t = 0; t < nt2; t += 2) {
    unsigned x0, x1;
    SCHED_FENCE(); wait_lgkm0(); SCHED_FENCE();
    phase<16>(acc, xa, xb, [&](int i) { rd(ya, yb, 0, 1, i); });
    wait_lgkm0(); bar(); SCHED_FENCE();
    phase<16>(acc, ya, yb, [&](int i) {
      rd(xa, xb, P2_STAGE, 0, i);
      if (i == 13) { const bool sw = (t + 2 == nC); x0 = sw ? sw0 : 65536u; x1 = sw ? sw1 : 65536u; asm volatile("" : "+v"(x0), "+v"(x1)); }
      if (i == 14) { pa0 ^= x0; asm volatile("" : "+v"(pa0)); }
    });
    wait_lgkm0(); SCHED_FENCE();
    phase<16>(acc, xa, xb, [&](int i) {
      rd(ya, yb, P2_STAGE, 1, i);
      if (i == 12) { pb[0] ^= 65536u; pb[1] ^= 65536u; asm volatile("" : "+v"(pb[0]), "+v"(pb[1])); }
      if (i == 13) { pb[2] ^= 65536u; pb[3] ^= 65536u; asm volatile("" : "+v"(pb[2]), "+v"(pb[3])); }
      if (i == 14) { pa1 ^= x1; asm volatile("" : "+v"(pa1)); }
    });
    wait_lgkm0(); bar(); SCHED_FENCE();
    phase<16>(acc, ya, yb, [&](int i) { rd(xa, xb, 0, 0, i); });
  }
  const unsigned xo = (unsigned)(wid * 4096 + ln * 16);
#pragma unroll
  for (int n = 0; n < 4; ++n) {
    u32x4 w = {pk_bf16(acc[2][n][0], acc[2][n][1]), pk_bf16(acc[2][n][2], acc[2][n][3]), pk_bf16(acc[3][n][0], acc[3][n][1]), pk_bf16(acc[3][n][2], acc[3][n][3])};
#pragma unroll
    for (int k = 0; k < 4; ++k) w[k] = w[k] ? w[k] : 0x80000000u;
    st16_wt(rxb, xo + n * 1024, bid * 16384, w);
  }
  {
#pragma unroll
    for (int n = 0; n < 4; ++n) {
      const int tl = 64 * wn + 16 * n + cl;
      const unsigned ms = *(LAS unsigned*)(lds + LDS_RES2 + tl * 8);
      const float rs_ = __builtin_amdgcn_rcpf(bfhi(ms)), mb = bflo(ms), b2 = bf2f(*(LAS u16*)(lds + LDS_RES2 + 1024 + tl * 2));
#pragma unroll
      for (int mi = 0; mi < 2; ++mi) {
        const int c8 = 4 * wm + 2 * mi + (g >> 1);
        const u32x2 xv = *(LAS u32x2*)(lds + LDS_RES + tl * 128 + ((c8 ^ (tl & 7)) * 16) + (g & 1) * 8);
        const u32x2 dk = *(LAS u32x2*)(lds + LDS_RES2 + 1280 + c8 * 16 + (g & 1) * 8);
#pragma unroll
        for (int h = 0; h < 2; ++h) {
          const float x0 = bflo(xv[h]), x1 = bfhi(xv[h]);
          const unsigned xn = pk_bf16((x0 - mb) * rs_, (x1 - mb) * rs_);
          acc[mi][n][2 * h] += fmaf(bflo(xn), bflo(dk[h]), x0 + b2); acc[mi][n][2 * h + 1] += fmaf(bfhi(xn), bfhi(dk[h]), x1 + b2);
        }
        asm volatile("" : "+v"(acc[mi][n]));
      }
    }
  }
  u32x4 rv[4];
#pragma unroll 1
  for (unsigned spins = 0; spins < SPIN_CAP; ++spins) {
    bool ok = true;
#pragma unroll
    for (int n = 0; n < 4; ++n) rv[n] = __builtin_amdgcn_raw_buffer_load_b128(rxb, xo + n * 1024 + pa * 16384, 0, 16);
#pragma unroll
    for (int n = 0; n < 4; ++n) ok &= (rv[n][0] != 0u) & (rv[n][1] != 0u) & (rv[n][2] != 0u) & (rv[n][3] != 0u);
    if (__all(ok)) break;
#pragma unroll 1
    for (; spins < SPIN_CAP; ++spins) {
      __builtin_amdgcn_s_sleep(2);
      asm volatile("" ::: "memory");
      if (__builtin_amdgcn_readfirstlane(__builtin_amdgcn_raw_buffer_load_b32(rxb, wid * 4096 + 4092 + pa * 16384, 0, 16)) != 0u) break;
    }
    asm volatile("" ::: "memory");
  }
#pragma unroll
  for (int n = 0; n < 4; ++n) {
    acc[0][n] += (f32x4){bflo(rv[n][0]), bfhi(rv[n][0]), bflo(rv[n][1]), bfhi(rv[n][1])};
    acc[1][n] += (f32x4){bflo(rv[n][2]), bfhi(rv[n][2]), bflo(rv[n][3]), bfhi(rv[n][3])};
  }
  {
    LAS char* tb = lds + wid * 8192;
#pragma unroll
    for (int n = 0; n < 4; ++n)
#pragma unroll
      for (int mi = 0; mi < 2; ++mi) *(LAS f32x4*)(tb + (16 * n + cl) * 128 + (((4 * mi + g) ^ (cl & 7)) * 16)) = acc[mi][n];
    const rsrc_t rout = mk_rsrc(a.out, S * D * 4);
#pragma unroll
    for (int k = 0; k < 8; ++k) {
      const u32x4 v = *(LAS u32x4*)(tb + k * 1024 + ln * 16);
      __builtin_amdgcn_raw_buffer_store_b128(v, rout, (unsigned)((t0 + 64 * wn + 8 * k + (ln >> 3)) * D + d0 + 64 * kh + 32 * wm) * 4u + (unsigned)(((ln & 7) ^ (ln >> 3)) * 16), 0, 16);
    }
  }
}

extern "C" void kernel_launch(void* const* d_in, const int* in_sizes, int n_in, void* d_out, int out_size, void* d_ws, size_t ws_size,
                              hipStream_t stream) {
  (void)hipFuncSetAttribute((const void*)fused, hipFuncAttributeMaxDynamicSharedMemorySize, LDS_BYTES);
  Args a;
  a.x = (const u16*)d_in[0]; a.Lre = (const u16*)d_in[1]; a.Lim = (const u16*)d_in[2]; a.Bre = (const u16*)d_in[3]; a.Bim = (const u16*)d_in[4];
  a.Cre = (const u16*)d_in[5]; a.Cim = (const u16*)d_in[6]; a.logdt = (const u16*)d_in[7]; a.Dsk = (const u16*)d_in[8];
  a.w1 = (const u16*)d_in[10]; a.b1 = (const u16*)d_in[11]; a.w2 = (const u16*)d_in[12]; a.b2 = (const u16*)d_in[13];
  a.out = (float*)d_out; a.ws = (char*)d_ws;
  fused<<<256, 512, LDS_BYTES, stream>>>(a);
}
```
